```python
import torch

def _softmax(x, axis=-1):
    return torch.softmax(x, dim=axis)
def _gelu(x):
    return torch.nn.functional.gelu(x, approximate="tanh")
def _silu(x):
    return torch.nn.functional.silu(x)
def _sigmoid(x):
    return torch.sigmoid(x)
def _layernorm(x, axis=-1, eps=1e-5):
    m = x.mean(dim=axis, keepdim=True)
    v = x.var(dim=axis, keepdim=True, unbiased=False)
    return (x - m) / torch.sqrt(v + eps)
def _scatter_add(tgt, idx, src, axis=0):
    return tgt.clone().index_add_(axis, idx, src)
def _segment_sum(src, seg, n):
    out = torch.zeros((n,) + src.shape[1:], dtype=src.dtype)
    return out.index_add_(0, seg, src)
def _pad(x, pw):
    flat = tuple(v for pair in reversed(pw) for v in pair)
    return torch.nn.functional.pad(x, flat)

kernel_name = 'concat_heads__gqa_block_cached__gqa_block_cached__near_ridge'

def setup_inputs():

    _w = (lambda a: (lambda t: t.to(torch.bfloat16) if t.is_floating_point() else t)(torch.as_tensor(a)))
    def _A_setup():
        B, Sq, D = 16, 64, 448
        Hq, Hkv, Sk, Dh = 16, 8, 1024, 256
        assert Hq % Hkv == 0, (Hq, Hkv)
        return {
            "x": _w((lambda *s: torch.randn(*s, dtype=torch.bfloat16) * 0.8)(B, Sq, D)),
            "k_cache": _w((lambda *s: torch.randn(*s, dtype=torch.bfloat16) * 0.8)(B, Hkv, Sk, Dh)),
            "v_cache": _w((lambda *s: torch.randn(*s, dtype=torch.bfloat16) * 0.05)(B, Hkv, Sk, Dh)),
            "wq": _w((lambda *s: torch.randn(*s, dtype=torch.bfloat16) * 0.8)(D, Hq * Dh) * (D ** -0.5)),
            "wo": _w((lambda *s: torch.randn(*s, dtype=torch.bfloat16) * 0.05)(Hq * Dh, D) * ((Hq * Dh) ** -0.5)),
        }
    _A_in = _A_setup()

    def _B_setup():
        B, Sq, D = 16, 64, 448
        Hq, Hkv, Sk, Dh = 16, 8, 1024, 256
        assert Hq % Hkv == 0, (Hq, Hkv)
        return {
            "x": _w((lambda *s: torch.randn(*s, dtype=torch.bfloat16) * 0.8)(B, Sq, D)),
            "k_cache": _w((lambda *s: torch.randn(*s, dtype=torch.bfloat16) * 0.8)(B, Hkv, Sk, Dh)),
            "v_cache": _w((lambda *s: torch.randn(*s, dtype=torch.bfloat16) * 0.05)(B, Hkv, Sk, Dh)),
            "wq": _w((lambda *s: torch.randn(*s, dtype=torch.bfloat16) * 0.8)(D, Hq * Dh) * (D ** -0.5)),
            "wo": _w((lambda *s: torch.randn(*s, dtype=torch.bfloat16) * 0.05)(Hq * Dh, D) * ((Hq * Dh) ** -0.5)),
        }
    _B_in = _B_setup()

    return {
        "A_x": _A_in["x"],
        "A_k_cache": _A_in["k_cache"],
        "A_v_cache": _A_in["v_cache"],
        "A_wq": _A_in["wq"],
        "A_wo": _A_in["wo"],
        "B_x": _B_in["x"],
        "B_k_cache": _B_in["k_cache"],
        "B_v_cache": _B_in["v_cache"],
        "B_wq": _B_in["wq"],
        "B_wo": _B_in["wo"],
    }

def reference(A_x, A_k_cache, A_v_cache, A_wq, A_wo, B_x, B_k_cache, B_v_cache, B_wq, B_wo):
    def _A_ref(x, k_cache, v_cache, wq, wo):
        B, Sq, D = x.shape
        Hq, Dh = 16, 256
        Hkv = k_cache.shape[1]
        rep = Hq // Hkv
        q = (lambda x, *d: x.permute(*d))((x @ wq).reshape(B, Sq, Hq, Dh), 0, 2, 1, 3)
        k = (lambda x, n, axis: torch.repeat_interleave(x, n, dim=axis))(k_cache, rep, axis=1)
        v = (lambda x, n, axis: torch.repeat_interleave(x, n, dim=axis))(v_cache, rep, axis=1)
        scores = torch.einsum("bhqd,bhkd->bhqk", q, k) * (Dh ** -0.5)
        p = _softmax(scores, axis=-1)
        o = torch.einsum("bhqk,bhkd->bhqd", p, v)
        return (lambda x, *d: x.permute(*d))(o, 0, 2, 1, 3).reshape(B, Sq, Hq * Dh) @ wo

    def _B_ref(x, k_cache, v_cache, wq, wo):
        B, Sq, D = x.shape
        Hq, Dh = 16, 256
        Hkv = k_cache.shape[1]
        rep = Hq // Hkv
        q = (lambda x, *d: x.permute(*d))((x @ wq).reshape(B, Sq, Hq, Dh), 0, 2, 1, 3)
        k = (lambda x, n, axis: torch.repeat_interleave(x, n, dim=axis))(k_cache, rep, axis=1)
        v = (lambda x, n, axis: torch.repeat_interleave(x, n, dim=axis))(v_cache, rep, axis=1)
        scores = torch.einsum("bhqd,bhkd->bhqk", q, k) * (Dh ** -0.5)
        p = _softmax(scores, axis=-1)
        o = torch.einsum("bhqk,bhkd->bhqd", p, v)
        return (lambda x, *d: x.permute(*d))(o, 0, 2, 1, 3).reshape(B, Sq, Hq * Dh) @ wo

    a = _A_ref(A_x, A_k_cache, A_v_cache, A_wq, A_wo)
    b = _B_ref(B_x, B_k_cache, B_v_cache, B_wq, B_wo)
    return torch.concatenate([a, b], axis=-1)
```

```cpp
#include <hip/hip_runtime.h>
#include <hip/hip_bf16.h>

#define LAS __attribute__((address_space(3)))
#define DI __device__ __forceinline__
typedef __attribute__((ext_vector_type(8))) __bf16 bf16x8;
typedef __attribute__((ext_vector_type(4))) __bf16 bf16x4;
typedef __attribute__((ext_vector_type(2))) __bf16 bf16x2;
typedef __attribute__((ext_vector_type(4))) float f32x4;
typedef __attribute__((ext_vector_type(2))) float f32x2;
typedef __attribute__((ext_vector_type(4))) unsigned u32x4;
typedef __attribute__((ext_vector_type(2))) unsigned u32x2;

namespace {
constexpr int NB = 16, SQ = 64, DM = 448, HKV = 8, SK = 1024, DH = 256;
constexpr int NQ = 4096;
constexpr int KV_BYTES = SK * DH * 2;
constexpr int W_BYTES = DM * NQ * 2;
constexpr int LDS_K = 0, LDS_V = 65536, LDS_QP = 131072, LDS_TOTAL = 163840;
constexpr int PART_BYTES = 7 * 16 * 1024;
constexpr float SCL = 0.0625f * 1.4426950408889634f;
constexpr int AUX_NT = 2;

#define SBAR() __builtin_amdgcn_sched_barrier(0)
#define WG_BARRIER() do { SBAR(); asm volatile("s_waitcnt lgkmcnt(0)\n\ts_barrier" ::: "memory"); SBAR(); } while (0)
#define MFMA(a, b, c) __builtin_amdgcn_mfma_f32_16x16x32_bf16((a), (b), (c), 0, 0, 0)

DI unsigned pk2(float a, float b) { f32x2 f = {a, b}; bf16x2 r = __builtin_convertvector(f, bf16x2); return __builtin_bit_cast(unsigned, r); }
DI bf16x8 asbf(u32x4 v) { return __builtin_bit_cast(bf16x8, v); }
DI u32x4 lds_r128(LAS char* p) { return *(LAS u32x4*)p; }
DI void lds_w128(LAS char* p, u32x4 v) { *(LAS u32x4*)p = v; }
DI bf16x4 lds_tr(LAS char* p) { return __builtin_amdgcn_ds_read_tr16_b64_v4bf16((LAS bf16x4*)p); }
DI unsigned perm_lo(unsigned hi, unsigned lo) { return __builtin_amdgcn_perm(hi, lo, 0x05040100u); }
DI unsigned perm_hi(unsigned hi, unsigned lo) { return __builtin_amdgcn_perm(hi, lo, 0x07060302u); }
DI float xmax(float v) {
  auto a = __builtin_amdgcn_permlane16_swap(__float_as_uint(v), __float_as_uint(v), false, false);
  v = fmaxf(__uint_as_float(a[0]), __uint_as_float(a[1]));
  auto b = __builtin_amdgcn_permlane32_swap(__float_as_uint(v), __float_as_uint(v), false, false);
  return fmaxf(__uint_as_float(b[0]), __uint_as_float(b[1]));
}
DI float xsum(float v) {
  auto a = __builtin_amdgcn_permlane16_swap(__float_as_uint(v), __float_as_uint(v), false, false);
  v = __uint_as_float(a[0]) + __uint_as_float(a[1]);
  auto b = __builtin_amdgcn_permlane32_swap(__float_as_uint(v), __float_as_uint(v), false, false);
  return __uint_as_float(b[0]) + __uint_as_float(b[1]);
}
typedef __amdgpu_buffer_rsrc_t rsrc_t;
DI rsrc_t mk_rsrc(const void* p, int bytes) { return __builtin_amdgcn_make_buffer_rsrc((void*)p, 0, bytes, 0x00020000); }

DI void oproj_step(f32x4 (&acc)[4][4], const u32x2 (&r)[8], LAS char* ob) {
  bf16x8 b[4];
#pragma unroll
  for (int rt = 0; rt < 4; ++rt) b[rt] = asbf(lds_r128(ob + rt * 1024));
#pragma unroll
  for (int i = 0; i < 4; ++i) {
    u32x4 a;
#pragma unroll
    for (int m = 0; m < 4; ++m) a[m] = (i & 1) ? perm_hi(r[2 * m + 1][i >> 1], r[2 * m][i >> 1]) : perm_lo(r[2 * m + 1][i >> 1], r[2 * m][i >> 1]);
#pragma unroll
    for (int rt = 0; rt < 4; ++rt) acc[i][rt] = MFMA(asbf(a), b[rt], acc[i][rt]);
  }
}
constexpr int OOB = 0x40000000;
DI void oproj_load(u32x2 (&r)[8], rsrc_t rw, int voff, int kidx) {
  const int vo = kidx < 16 ? voff : OOB;
#pragma unroll
  for (int j = 0; j < 8; ++j)
    r[j] = __builtin_amdgcn_raw_buffer_load_b64(rw, vo, ((kidx & 15) * 32 + 16 * (j >> 2) + (j & 3)) * (DM * 2), 0);
}
DI void oproj_store(const f32x4 (&acc)[4][4], char* part, int u, int lane) {
#pragma unroll
  for (int rt = 0; rt < 4; ++rt)
#pragma unroll
    for (int i = 0; i < 4; ++i) *(f32x4*)(part + ((u * 4 + rt) * 4 + i) * 1024 + lane * 16) = acc[i][rt];
}

__global__ __launch_bounds__(512, 2) void gqa_fused(const char* __restrict__ xA, const char* __restrict__ kA, const char* __restrict__ vA,
                                                    const char* __restrict__ wqA, const char* __restrict__ woA,
                                                    const char* __restrict__ xB, const char* __restrict__ kB, const char* __restrict__ vB,
                                                    const char* __restrict__ wqB, const char* __restrict__ woB, char* __restrict__ ws) {
  __shared__ __attribute__((aligned(1024))) char smem_[LDS_TOTAL];
  LAS char* smem = (LAS char*)smem_;
  const int tid = threadIdx.x, lane = tid & 63;
  const int wid = __builtin_amdgcn_readfirstlane(tid >> 6);
  const int g = lane >> 4, c = lane & 15;
  const int bid = blockIdx.x, kvh = bid & 7, bb = (bid >> 3) & 15, blk = bid >> 7;
  const char* xg = (blk ? xB : xA) + (size_t)bb * SQ * DM * 2;
  const char* kg = (blk ? kB : kA) + (size_t)(bb * HKV + kvh) * KV_BYTES;
  const char* vg = (blk ? vB : vA) + (size_t)(bb * HKV + kvh) * KV_BYTES;
  const char* wq = blk ? wqB : wqA;
  const char* wo = (blk ? woB : woA) + (size_t)kvh * 512 * DM * 2;
  char* part = ws + (size_t)bid * PART_BYTES;
  const rsrc_t rwo = mk_rsrc(wo, 512 * DM * 2);

  if (wid < 4) {
    const int w = wid, hh = w >> 1, rh = w & 1;
    u32x4 qown[2][4];
    {
      const rsrc_t rx = mk_rsrc(xg, SQ * DM * 2);
      const rsrc_t rwq = mk_rsrc(wq, W_BYTES);
      u32x4 xr[14];
#pragma unroll
      for (int f = 0; f < 14; ++f) {
        const int fr = w * 14 + f;
        xr[f] = __builtin_amdgcn_raw_buffer_load_b128(rx, c * (DM * 2) + g * 16, (fr & 3) * 16 * (DM * 2) + (fr >> 2) * 64, 0);
      }
      const int wvoff = g * 8 * (NQ * 2) + (kvh * 512 + w * 128 + c * 8) * 2;
      const int ks0 = bb < 14 ? bb : bb - 14;
      u32x4 R[3][8];
#define QLOAD(ST, S) do { int ks_ = ks0 + (S); ks_ = ks_ >= 14 ? ks_ - 14 : ks_; const int so_ = ks_ * 32 * (NQ * 2); const int vo_ = (S) < 14 ? wvoff : OOB; \
        _Pragma("unroll") for (int j = 0; j < 8; ++j) R[ST][j] = __builtin_amdgcn_raw_buffer_load_b128(rwq, vo_, so_ + j * (NQ * 2), 0); } while (0)
      QLOAD(0, 0); QLOAD(1, 1); QLOAD(2, 2);
      SBAR();
#pragma unroll
      for (int f = 0; f < 14; ++f) lds_w128(smem + LDS_V + (w * 14 + f) * 1024 + lane * 16, xr[f]);
      WG_BARRIER();
      f32x4 acc[8][4];
#pragma unroll
      for (int i = 0; i < 8; ++i)
#pragma unroll
        for (int rt = 0; rt < 4; ++rt) acc[i][rt] = f32x4{0.f, 0.f, 0.f, 0.f};
#define QSTEP(ST, S) do { int ks_ = ks0 + (S); ks_ = ks_ >= 14 ? ks_ - 14 : ks_; \
        LAS char* xb_ = smem + LDS_V + ks_ * 4096 + lane * 16; bf16x8 b_[4]; \
        _Pragma("unroll") for (int rt = 0; rt < 4; ++rt) b_[rt] = asbf(lds_r128(xb_ + rt * 1024)); \
        _Pragma("unroll") for (int i = 0; i < 8; ++i) { u32x4 a_; \
          _Pragma("unroll") for (int m = 0; m < 4; ++m) a_[m] = (i & 1) ? perm_hi(R[ST][2 * m + 1][i >> 1], R[ST][2 * m][i >> 1]) : perm_lo(R[ST][2 * m + 1][i >> 1], R[ST][2 * m][i >> 1]); \
          _Pragma("unroll") for (int rt = 0; rt < 4; ++rt) acc[i][rt] = MFMA(asbf(a_), b_[rt], acc[i][rt]); } } while (0)
#pragma unroll 1
      for (int s = 0; s < 12; s += 3) {
        QSTEP(0, s); SBAR(); QLOAD(0, s + 3); SBAR();
        QSTEP(1, s + 1); SBAR(); QLOAD(1, s + 4); SBAR();
        QSTEP(2, s + 2); SBAR(); QLOAD(2, s + 5); SBAR();
      }
      QSTEP(0, 12); SBAR();
      QSTEP(1, 13); SBAR();
#undef QSTEP
#undef QLOAD
#pragma unroll
      for (int rtl = 0; rtl < 2; ++rtl)
#pragma unroll
        for (int f = 0; f < 4; ++f) {
          u32x4 o, p;
#pragma unroll
          for (int m = 0; m < 4; ++m) {
            const float a0 = rh ? acc[2 * m][2 + rtl][f] : acc[2 * m][rtl][f], a1 = rh ? acc[2 * m + 1][2 + rtl][f] : acc[2 * m + 1][rtl][f];
            const float b0 = rh ? acc[2 * m][rtl][f] : acc[2 * m][2 + rtl][f], b1 = rh ? acc[2 * m + 1][rtl][f] : acc[2 * m + 1][2 + rtl][f];
            o[m] = pk2(a0, a1); p[m] = pk2(b0, b1);
          }
          qown[rtl][f] = o;
          lds_w128(smem + LDS_QP + (w ^ 1) * 8192 + (rtl * 4 + f) * 1024 + lane * 16, p);
        }
    }
    f32x4 o[16][2];
#pragma unroll
    for (int dt = 0; dt < 16; ++dt) { o[dt][0] = f32x4{0.f, 0.f, 0.f, 0.f}; o[dt][1] = f32x4{0.f, 0.f, 0.f, 0.f}; }
    float m_[2] = {-__builtin_inff(), -__builtin_inff()}, l_[2] = {0.f, 0.f};
    u32x4 pf[2][2];
    pf[0][0] = pf[0][1] = pf[1][0] = pf[1][1] = u32x4{0u, 0u, 0u, 0u};
    const int sig = (c + 12) & 15;
    int kb[4];
#pragma unroll
    for (int f = 0; f < 4; ++f) kb[f] = LDS_K + c * 512 + (((4 * g + f) ^ sig) << 4);
    const int keyl = 4 * g + (c >> 2);
    int vb = LDS_V + keyl * 512 + ((keyl & 7) << 5) + (c & 3) * 8;
    LAS char* qp = smem + LDS_QP + w * 8192 + lane * 16;
    const int own = rh * 256, oth = 256 - own;
#pragma unroll 1
    for (int t = -1; t < 16; ++t) {
      WG_BARRIER();
      if (t >= 0) {
#pragma unroll
        for (int bq = 0; bq < 8; ++bq) {
          const int vbb = vb ^ (bq << 5);
#pragma unroll
          for (int a = 0; a < 2; ++a) {
            const int dt = a * 8 + bq;
#pragma unroll
            for (int ks = 0; ks < 2; ++ks) {
              const bf16x4 lo = lds_tr(smem + vbb + a * 256 + ks * 16384);
              const bf16x4 hi = lds_tr(smem + vbb + a * 256 + ks * 16384 + 8192);
              const bf16x8 vf = __builtin_shufflevector(lo, hi, 0, 1, 2, 3, 4, 5, 6, 7);
              o[dt][0] = MFMA(vf, asbf(pf[ks][0]), o[dt][0]);
              o[dt][1] = MFMA(vf, asbf(pf[ks][1]), o[dt][1]);
            }
          }
        }
        vb ^= 32768;
      }
      if (t < 15) {
        f32x4 s[4][2];
#pragma unroll
        for (int kt = 0; kt < 4; ++kt) { s[kt][0] = f32x4{0.f, 0.f, 0.f, 0.f}; s[kt][1] = f32x4{0.f, 0.f, 0.f, 0.f}; }
#pragma unroll
        for (int f = 0; f < 4; ++f) {
#pragma unroll
          for (int kt = 0; kt < 4; ++kt) {
            const bf16x8 kf = asbf(lds_r128(smem + kb[f] + own + kt * 8192));
            s[kt][0] = MFMA(kf, asbf(qown[0][f]), s[kt][0]);
            s[kt][1] = MFMA(kf, asbf(qown[1][f]), s[kt][1]);
          }
        }
#pragma unroll
        for (int f = 0; f < 4; ++f) {
          const bf16x8 q0 = asbf(lds_r128(qp + f * 1024)), q1 = asbf(lds_r128(qp + (4 + f) * 1024));
#pragma unroll
          for (int kt = 0; kt < 4; ++kt) {
            const bf16x8 kf = asbf(lds_r128(smem + kb[f] + oth + kt * 8192));
            s[kt][0] = MFMA(kf, q0, s[kt][0]);
            s[kt][1] = MFMA(kf, q1, s[kt][1]);
          }
        }
#pragma unroll
        for (int f = 0; f < 4; ++f) kb[f] ^= 32768;
        float al[2];
#pragma unroll
        for (int r = 0; r < 2; ++r) {
          float mx = fmaxf(fmaxf(s[0][r][0], s[0][r][1]), fmaxf(s[0][r][2], s[0][r][3]));
#pragma unroll
          for (int kt = 1; kt < 4; ++kt) mx = fmaxf(mx, fmaxf(fmaxf(s[kt][r][0], s[kt][r][1]), fmaxf(s[kt][r][2], s[kt][r][3])));
          mx = xmax(mx);
          const float mn = fmaxf(m_[r], mx);
          al[r] = __builtin_amdgcn_exp2f((m_[r] - mn) * SCL);
          m_[r] = mn;
          const float nm = -mn * SCL;
          float ps = 0.f;
#pragma unroll
          for (int kt = 0; kt < 4; ++kt)
#pragma unroll
            for (int e = 0; e < 4; ++e) { const float p = __builtin_amdgcn_exp2f(fmaf(s[kt][r][e], SCL, nm)); s[kt][r][e] = p; ps += p; }
          l_[r] = l_[r] * al[r] + ps;
#pragma unroll
          for (int ks = 0; ks < 2; ++ks)
            pf[ks][r] = u32x4{pk2(s[2 * ks][r][0], s[2 * ks][r][1]), pk2(s[2 * ks][r][2], s[2 * ks][r][3]),
                              pk2(s[2 * ks + 1][r][0], s[2 * ks + 1][r][1]), pk2(s[2 * ks + 1][r][2], s[2 * ks + 1][r][3])};
        }
        if (__any(al[0] != 1.f || al[1] != 1.f)) {
#pragma unroll
          for (int dt = 0; dt < 16; ++dt) { o[dt][0] *= al[0]; o[dt][1] *= al[1]; }
        }
      }
    }
    {
      const float i0 = 1.f / xsum(l_[0]), i1 = 1.f / xsum(l_[1]);
#pragma unroll
      for (int kk = 0; kk < 8; ++kk)
#pragma unroll
        for (int r = 0; r < 2; ++r) {
          const float iv = r ? i1 : i0;
          const f32x4 a = o[2 * kk][r] * iv, b = o[2 * kk + 1][r] * iv;
          lds_w128(smem + LDS_K + ((hh * 8 + kk) * 4 + rh * 2 + r) * 1024 + lane * 16,
                   u32x4{pk2(a[0], a[1]), pk2(a[2], a[3]), pk2(b[0], b[1]), pk2(b[2], b[3])});
        }
    }
    const int u = 4 + w;
    const int ovoff = u < 7 ? g * 4 * (DM * 2) + (u * 64 + c * 4) * 2 : OOB;
    u32x2 Rw[4][8];
    oproj_load(Rw[0], rwo, ovoff, 0); oproj_load(Rw[1], rwo, ovoff, 1); oproj_load(Rw[2], rwo, ovoff, 2); oproj_load(Rw[3], rwo, ovoff, 3);
    WG_BARRIER();
    if (u < 7) {
      f32x4 acc[4][4];
#pragma unroll
      for (int i = 0; i < 4; ++i)
#pragma unroll
        for (int rt = 0; rt < 4; ++rt) acc[i][rt] = f32x4{0.f, 0.f, 0.f, 0.f};
#pragma unroll 1
      for (int k0 = 0; k0 < 16; k0 += 4) {
#pragma unroll
        for (int s = 0; s < 4; ++s) {
          oproj_step(acc, Rw[s], smem + LDS_K + (k0 + s) * 4096 + lane * 16); SBAR();
          oproj_load(Rw[s], rwo, ovoff, k0 + s + 4); SBAR();
        }
      }
      oproj_store(acc, part, u, lane);
    }
  } else {
    const int h = wid - 4, hi = lane >> 5, ch = lane & 31;
    const rsrc_t rk = mk_rsrc(kg, KV_BYTES), rv = mk_rsrc(vg, KV_BYTES);
    const int voff = h * 1024 + lane * 16;
    const int row0 = 2 * h + hi;
    const int sig0 = (row0 + 12) & 15;
    const int kw0 = LDS_K + row0 * 512 + ((ch ^ sig0) << 4), kw1 = LDS_K + (row0 + 8) * 512 + ((ch ^ sig0 ^ 8) << 4);
    const int vw0 = LDS_V + row0 * 512 + ((ch ^ (row0 << 1)) << 4);
    u32x4 S[6][8];
#define LOADU(SET, RS, TILE) do { const int vo_ = (TILE) < 16 ? voff : OOB; _Pragma("unroll") for (int i = 0; i < 8; ++i) \
      S[SET][i] = __builtin_amdgcn_raw_buffer_load_b128(RS, vo_, ((TILE) & 15) * 32768 + i * 4096, AUX_NT); SBAR(); } while (0)
#define WRITEK(SET, BUF) do { _Pragma("unroll") for (int i = 0; i < 8; ++i) \
      lds_w128(smem + ((i & 1) ? kw1 : kw0) + (BUF) + (i >> 1) * 8192, S[SET][i]); SBAR(); } while (0)
#define WRITEV(SET, BUF) do { _Pragma("unroll") for (int i = 0; i < 8; ++i) \
      lds_w128(smem + vw0 + (BUF) + i * 4096, S[SET][i]); SBAR(); } while (0)
#define WAITV(N) do { asm volatile("s_waitcnt vmcnt(" #N ")" ::: "memory"); SBAR(); } while (0)
    LOADU(0, rk, 0); LOADU(1, rk, 1); LOADU(2, rv, 0);
    WG_BARRIER();
    WRITEK(0, 0); LOADU(3, rk, 2);
    WRITEK(1, 32768); LOADU(4, rv, 1);
    WAITV(16); LOADU(5, rk, 3);
    WAITV(16); LOADU(0, rv, 2);
    WAITV(16); LOADU(1, rk, 4);
    WG_BARRIER();
    WRITEV(2, 0); LOADU(2, rv, 3);
    WG_BARRIER();
#pragma unroll 1
    for (int t = 0; t < 15; t += 3) {
      const int b0 = (t & 1) << 15, b1 = b0 ^ 32768;
      WRITEK(3, b0); LOADU(3, rk, t + 5); WRITEV(4, b1); LOADU(4, rv, t + 4);
      WG_BARRIER();
      WRITEK(5, b1); LOADU(5, rk, t + 6); WRITEV(0, b0); LOADU(0, rv, t + 5);
      WG_BARRIER();
      WRITEK(1, b0); LOADU(1, rk, t + 7); WRITEV(2, b1); LOADU(2, rv, t + 6);
      WG_BARRIER();
    }
#undef LOADU
#undef WRITEK
#undef WRITEV
#undef WAITV
    const int u = h;
    const int ovoff = g * 4 * (DM * 2) + (u * 64 + c * 4) * 2;
    u32x2 Rw[4][8];
    oproj_load(Rw[0], rwo, ovoff, 0); oproj_load(Rw[1], rwo, ovoff, 1); oproj_load(Rw[2], rwo, ovoff, 2); oproj_load(Rw[3], rwo, ovoff, 3);
    WG_BARRIER();
    f32x4 acc[4][4];
#pragma unroll
    for (int i = 0; i < 4; ++i)
#pragma unroll
      for (int rt = 0; rt < 4; ++rt) acc[i][rt] = f32x4{0.f, 0.f, 0.f, 0.f};
#pragma unroll 1
    for (int k0 = 0; k0 < 16; k0 += 4) {
#pragma unroll
      for (int s = 0; s < 4; ++s) {
        oproj_step(acc, Rw[s], smem + LDS_K + (k0 + s) * 4096 + lane * 16); SBAR();
        oproj_load(Rw[s], rwo, ovoff, k0 + s + 4); SBAR();
      }
    }
    oproj_store(acc, part, u, lane);
  }
}

__global__ __launch_bounds__(256) void gqa_reduce(const char* __restrict__ ws, char* __restrict__ out) {
  const int lane = threadIdx.x & 63, wv = blockIdx.x * 4 + (threadIdx.x >> 6);
  const int grp = wv / 28, ur = wv % 28, u = ur >> 2, rt = ur & 3;
  const int g = lane >> 4, c = lane & 15;
  const char* p = ws + (size_t)grp * 8 * PART_BYTES + ur * 4096 + lane * 16;
  f32x4 a[4];
#pragma unroll
  for (int i = 0; i < 4; ++i) a[i] = *(const f32x4*)(p + i * 1024);
#pragma unroll
  for (int k = 1; k < 8; ++k)
#pragma unroll
    for (int i = 0; i < 4; ++i) a[i] += *(const f32x4*)(p + (size_t)k * PART_BYTES + i * 1024);
  const int blk = grp >> 4, bb = grp & 15;
  char* o = out + ((size_t)(bb * SQ + rt * 16 + c) * (2 * DM) + blk * DM + u * 64 + g * 16) * 2;
  *(u32x4*)o = u32x4{pk2(a[0][0], a[1][0]), pk2(a[2][0], a[3][0]), pk2(a[0][1], a[1][1]), pk2(a[2][1], a[3][1])};
  *(u32x4*)(o + 16) = u32x4{pk2(a[0][2], a[1][2]), pk2(a[2][2], a[3][2]), pk2(a[0][3], a[1][3]), pk2(a[2][3], a[3][3])};
}
}

extern "C" void kernel_launch(void* const* d_in, const int* in_sizes, int n_in, void* d_out, int out_size, void* d_ws, size_t ws_size,
                              hipStream_t stream) {
  (void)in_sizes; (void)n_in; (void)out_size; (void)ws_size;
  gqa_fused<<<256, 512, 0, stream>>>((const char*)d_in[0], (const char*)d_in[1], (const char*)d_in[2], (const char*)d_in[3], (const char*)d_in[4],
                                     (const char*)d_in[5], (const char*)d_in[6], (const char*)d_in[7], (const char*)d_in[8], (const char*)d_in[9],
                                     (char*)d_ws);
  gqa_reduce<<<224, 256, 0, stream>>>((const char*)d_ws, (char*)d_out);
}
```

```cpp
#include <hip/hip_runtime.h>
#include <hip/hip_bf16.h>

#define LAS __attribute__((address_space(3)))
#define DI __device__ __forceinline__
typedef __attribute__((ext_vector_type(8))) __bf16 bf16x8;
typedef __attribute__((ext_vector_type(4))) __bf16 bf16x4;
typedef __attribute__((ext_vector_type(2))) __bf16 bf16x2;
typedef __attribute__((ext_vector_type(4))) float f32x4;
typedef __attribute__((ext_vector_type(2))) float f32x2;
typedef __attribute__((ext_vector_type(4))) unsigned u32x4;
typedef __attribute__((ext_vector_type(2))) unsigned u32x2;

namespace {
constexpr int SQ = 64, DM = 448, HKV = 8, SK = 1024, DH = 256;
constexpr int NQ = 4096;
constexpr int KV_BYTES = SK * DH * 2;
constexpr int W_BYTES = DM * NQ * 2;
constexpr int LDS_K = 0, LDS_V = 65536, LDS_QP = 131072, LDS_TOTAL = 163840;
constexpr int PART_BYTES = 7 * 16 * 1024;
constexpr float SCL = 0.0625f * 1.4426950408889634f;
constexpr int AUX_NT = 2;
#define AUX_W 2
constexpr int OOB = 0x40000000;

#define SBAR() __builtin_amdgcn_sched_barrier(0)
#define WG_BARRIER() do { SBAR(); asm volatile("s_waitcnt lgkmcnt(0)\n\ts_barrier" ::: "memory"); SBAR(); } while (0)
#define STAMP(idx) do {} while (0)
#define STAMP_IF(c, idx) do {} while (0)
#define STAMPC(idx) do {} while (0)
#define STAMP_DECL do {} while (0)
#define STAMP_OUT() do {} while (0)
#define MFMA(a, b, c) __builtin_amdgcn_mfma_f32_16x16x32_bf16((a), (b), (c), 0, 0, 0)

DI unsigned pk2(float a, float b) { f32x2 f = {a, b}; bf16x2 r = __builtin_convertvector(f, bf16x2); return __builtin_bit_cast(unsigned, r); }
DI bf16x8 asbf(u32x4 v) { return __builtin_bit_cast(bf16x8, v); }
DI u32x4 lds_r128(LAS char* p) { return *(LAS u32x4*)p; }
DI void lds_w128(LAS char* p, u32x4 v) { *(LAS u32x4*)p = v; }
DI bf16x4 lds_tr(LAS char* p) { return __builtin_amdgcn_ds_read_tr16_b64_v4bf16((LAS bf16x4*)p); }
DI unsigned perm2(int e, unsigned hi, unsigned lo) { return __builtin_amdgcn_perm(hi, lo, e ? 0x07060302u : 0x05040100u); }
DI unsigned swap1(unsigned v) { return (unsigned)__builtin_amdgcn_mov_dpp((int)v, 0xB1, 0xf, 0xf, true); }
DI float xmax(float v) {
  auto a = __builtin_amdgcn_permlane16_swap(__float_as_uint(v), __float_as_uint(v), false, false);
  v = fmaxf(__uint_as_float(a[0]), __uint_as_float(a[1]));
  auto b = __builtin_amdgcn_permlane32_swap(__float_as_uint(v), __float_as_uint(v), false, false);
  return fmaxf(__uint_as_float(b[0]), __uint_as_float(b[1]));
}
DI float xsum(float v) {
  auto a = __builtin_amdgcn_permlane16_swap(__float_as_uint(v), __float_as_uint(v), false, false);
  v = __uint_as_float(a[0]) + __uint_as_float(a[1]);
  auto b = __builtin_amdgcn_permlane32_swap(__float_as_uint(v), __float_as_uint(v), false, false);
  return __uint_as_float(b[0]) + __uint_as_float(b[1]);
}
DI int lane_id() {
  int l; asm volatile("v_mbcnt_lo_u32_b32 %0, -1, 0\n\tv_mbcnt_hi_u32_b32 %0, -1, %0" : "=v"(l)); return l; }
typedef __amdgpu_buffer_rsrc_t rsrc_t;
DI rsrc_t mk_rsrc(const void* p, int bytes) { return __builtin_amdgcn_make_buffer_rsrc((void*)p, 0, bytes, 0x00020000); }

DI void oproj_load(u32x4 (&r)[8], rsrc_t rw, int voff, int kp) {
  const int vo = kp < 8 ? voff : OOB;
#pragma unroll
  for (int j = 0; j < 8; ++j)
    r[j] = __builtin_amdgcn_raw_buffer_load_b128(rw, vo, ((kp & 7) * 64 + 16 * (j >> 2) + (j & 3)) * (DM * 2), AUX_W);
}
DI void oproj_pair(f32x4 (&acc)[4][4], const u32x4 (&r)[8], LAS char* ob, bool odd) {
#pragma unroll
  for (int st = 0; st < 2; ++st) {
    bf16x8 b[4];
#pragma unroll
    for (int rt = 0; rt < 4; ++rt) b[rt] = asbf(lds_r128(ob + st * 4096 + rt * 1024));
#pragma unroll
    for (int q = 0; q < 2; ++q) {
      unsigned d[8];
#pragma unroll
      for (int j = 0; j < 8; ++j) {
        const unsigned sw = swap1(st == 0 ? r[j][2 + q] : r[j][q]);
        d[j] = st == 0 ? (odd ? sw : r[j][q]) : (odd ? r[j][2 + q] : sw);
      }
#pragma unroll
      for (int e = 0; e < 2; ++e) {
        u32x4 a;
#pragma unroll
        for (int m = 0; m < 4; ++m) a[m] = perm2(e, d[2 * m + 1], d[2 * m]);
#pragma unroll
        for (int rt = 0; rt < 4; ++rt) acc[2 * q + e][rt] = MFMA(asbf(a), b[rt], acc[2 * q + e][rt]);
      }
    }
  }
}
DI void oproj_store(const f32x4 (&acc)[4][4], char* part, int u, int lane) {
#pragma unroll
  for (int rt = 0; rt < 4; ++rt)
#pragma unroll
    for (int i = 0; i < 4; ++i) *(f32x4*)(part + ((u * 4 + rt) * 4 + i) * 1024 + lane * 16) = acc[i][rt];
}

__global__ __launch_bounds__(512, 2) void gqa_fused(const char* __restrict__ xA, const char* __restrict__ kA, const char* __restrict__ vA,
                                                    const char* __restrict__ wqA, const char* __restrict__ woA,
                                                    const char* __restrict__ xB, const char* __restrict__ kB, const char* __restrict__ vB,
                                                    const char* __restrict__ wqB, const char* __restrict__ woB, char* __restrict__ ws) {
  __shared__ __attribute__((aligned(1024))) char smem_[LDS_TOTAL];
  LAS char* smem = (LAS char*)smem_;
  const int wid = __builtin_amdgcn_readfirstlane(threadIdx.x >> 6);
  const int bid = blockIdx.x, kvh = bid & 7, bb = (bid >> 3) & 15, blk = bid >> 7;
  const char* xg = (blk ? xB : xA) + (size_t)bb * SQ * DM * 2;
  const char* kg = (blk ? kB : kA) + (size_t)(bb * HKV + kvh) * KV_BYTES;
  const char* vg = (blk ? vB : vA) + (size_t)(bb * HKV + kvh) * KV_BYTES;
  const char* wq = blk ? wqB : wqA;
  const char* wo = (blk ? woB : woA) + (size_t)kvh * 512 * DM * 2;
  char* part = ws + (size_t)bid * PART_BYTES;
  const rsrc_t rwo = mk_rsrc(wo, 512 * DM * 2);
  const int kp0 = bb & 7;
  STAMP_DECL;
  STAMP(0); STAMPC(13);
#define XLOAD() u32x4 xr[7]; do { const rsrc_t rx = mk_rsrc(xg, SQ * DM * 2); _Pragma("unroll") for (int f = 0; f < 7; ++f) { const int fr = wid * 7 + f; \
      xr[f] = __builtin_amdgcn_raw_buffer_load_b128(rx, c * (DM * 2) + g * 16, (fr & 3) * 16 * (DM * 2) + (fr >> 2) * 64, AUX_W); } SBAR(); } while (0)
#define XWRITE() do { _Pragma("unroll") for (int f = 0; f < 7; ++f) lds_w128(smem + LDS_V + (wid * 7 + f) * 1024 + lane * 16, xr[f]); } while (0)

  if (wid < 4) {
    const int w = wid, hh = w >> 1, rh = w & 1;
    u32x4 qown[2][4];
    {
      const int lane = lane_id(), g = lane >> 4, c = lane & 15;
      XLOAD();
      const rsrc_t rwq = mk_rsrc(wq, W_BYTES);
      const int wvoff = g * 8 * (NQ * 2) + (kvh * 512 + w * 128 + c * 8) * 2;
      const int ks0 = bb < 14 ? bb : bb - 14;
      u32x4 R[3][8];
#define KSOF(S) ((ks0 + (S)) >= 14 ? ks0 + (S) - 14 : ks0 + (S))
#define QLOAD1(ST, S, J) R[ST][J] = __builtin_amdgcn_raw_buffer_load_b128(rwq, wvoff, KSOF(S) * 32 * (NQ * 2) + (J) * (NQ * 2), AUX_W)
#pragma unroll
      for (int j = 0; j < 8; ++j) QLOAD1(0, 0, j);
#pragma unroll
      for (int j = 0; j < 8; ++j) QLOAD1(1, 1, j);
      SBAR();
      XWRITE();
      WG_BARRIER();
      STAMP(1);
      f32x4 acc[8][4];
#pragma unroll
      for (int i = 0; i < 8; ++i)
#pragma unroll
        for (int rt = 0; rt < 4; ++rt) acc[i][rt] = f32x4{0.f, 0.f, 0.f, 0.f};
      bf16x8 bq[4]; u32x4 aq[2];
      LAS char* xb = smem + LDS_V + lane * 16;
#pragma unroll
      for (int rt = 0; rt < 4; ++rt) bq[rt] = asbf(lds_r128(xb + ks0 * 4096 + rt * 1024));
#pragma unroll
      for (int m = 0; m < 4; ++m) aq[0][m] = perm2(0, R[0][2 * m + 1][0], R[0][2 * m][0]);
      SBAR();
#define QSTEP(CUR, NXT, LD, S, DOLD, DONX) do { \
        _Pragma("unroll") for (int i = 0; i < 8; ++i) { \
          _Pragma("unroll") for (int rt = 0; rt < 4; ++rt) { \
            acc[i][rt] = MFMA(asbf(aq[i & 1]), bq[rt], acc[i][rt]); \
            if (i < 7) aq[(i + 1) & 1][rt] = perm2((i + 1) & 1, R[CUR][2 * rt + 1][(i + 1) >> 1], R[CUR][2 * rt][(i + 1) >> 1]); \
            else if (DONX) { aq[0][rt] = perm2(0, R[NXT][2 * rt + 1][0], R[NXT][2 * rt][0]); bq[rt] = asbf(lds_r128(xb + KSOF((S) + 1) * 4096 + rt * 1024)); } \
            if (DOLD && rt == 1) QLOAD1(LD, (S) + 2, i); \
            SBAR(); } } } while (0)
#pragma unroll 1
      for (int s = 0; s < 12; s += 3) {
        QSTEP(0, 1, 2, s, true, true);
        QSTEP(1, 2, 0, s + 1, true, true);
        QSTEP(2, 0, 1, s + 2, true, true);
      }
      QSTEP(0, 1, 2, 12, false, true);
      QSTEP(1, 2, 0, 13, false, false);
#undef QSTEP
#undef QLOAD1
#undef KSOF
#pragma unroll
      for (int rtl = 0; rtl < 2; ++rtl)
#pragma unroll
        for (int f = 0; f < 4; ++f) {
          u32x4 lo, hi;
#pragma unroll
          for (int m = 0; m < 4; ++m) {
            lo[m] = pk2(acc[2 * m][rtl][f], acc[2 * m + 1][rtl][f]);
            hi[m] = pk2(acc[2 * m][2 + rtl][f], acc[2 * m + 1][2 + rtl][f]);
          }
          qown[rtl][f] = rh ? hi : lo;
          lds_w128(smem + LDS_QP + (w ^ 1) * 8192 + (rtl * 4 + f) * 1024 + lane * 16, rh ? lo : hi);
        }
    }
    STAMP(2);
    const int lane = lane_id(), g = lane >> 4, c = lane & 15;
    f32x4 o[16][2];
#pragma unroll
    for (int dt = 0; dt < 16; ++dt) { o[dt][0] = f32x4{0.f, 0.f, 0.f, 0.f}; o[dt][1] = f32x4{0.f, 0.f, 0.f, 0.f}; }
    float m_[2] = {-__builtin_inff(), -__builtin_inff()}, l_[2] = {0.f, 0.f};
    u32x4 pf[2][2];
    pf[0][0] = pf[0][1] = pf[1][0] = pf[1][1] = u32x4{0u, 0u, 0u, 0u};
    const int sig = (c + 12) & 15;
    int kb[4];
#pragma unroll
    for (int f = 0; f < 4; ++f) kb[f] = LDS_K + c * 512 + rh * 256 + (((4 * g + f) ^ sig) << 4);
    const int keyl = 4 * g + (c >> 2);
    int vb = LDS_V + keyl * 512 + ((keyl & 7) << 5) + (c & 3) * 8;
    LAS char* qp = smem + LDS_QP + w * 8192 + lane * 16;
#pragma unroll 1
    for (int t = -1; t < 16; ++t) {
      WG_BARRIER();
      STAMP_IF(t == -1, 3); STAMP_IF(t == 0, 4); STAMP_IF(t == 4, 5); STAMP_IF(t == 8, 6); STAMP_IF(t == 12, 7); STAMP_IF(t == 15, 8);
      constexpr int PD = 6;
      if (t >= 0) {
        bf16x4 vl[8], vh[8];
#define VADDR(N) (smem + (vb ^ (((N) >> 2) << 5)) + (((N) >> 1) & 1) * 256 + ((N) & 1) * 16384)
#pragma unroll
        for (int n = 0; n < PD; ++n) { vl[n] = lds_tr(VADDR(n)); vh[n] = lds_tr(VADDR(n) + 8192); }
        SBAR();
#pragma unroll
        for (int n = 0; n < 32; ++n) {
          const int dt = ((n >> 1) & 1) * 8 + (n >> 2);
          const bf16x8 vf = __builtin_shufflevector(vl[n & 7], vh[n & 7], 0, 1, 2, 3, 4, 5, 6, 7);
          o[dt][0] = MFMA(vf, asbf(pf[n & 1][0]), o[dt][0]);
          if (n + PD < 32) vl[(n + PD) & 7] = lds_tr(VADDR(n + PD));
          SBAR();
          o[dt][1] = MFMA(vf, asbf(pf[n & 1][1]), o[dt][1]);
          if (n + PD < 32) vh[(n + PD) & 7] = lds_tr(VADDR(n + PD) + 8192);
          SBAR();
        }
#undef VADDR
        vb ^= 32768;
      }
      if (t < 15) {
        f32x4 s[4][2];
#pragma unroll
        for (int kt = 0; kt < 4; ++kt) { s[kt][0] = f32x4{0.f, 0.f, 0.f, 0.f}; s[kt][1] = f32x4{0.f, 0.f, 0.f, 0.f}; }
        bf16x8 kfr[8], qfr[2];
#define KADDR(N) (smem + (kb[(N) >> 3] ^ ((((N) >> 2) & 1) << 8)) + ((N) & 3) * 8192)
#pragma unroll
        for (int n = 0; n < PD; ++n) kfr[n] = asbf(lds_r128(KADDR(n)));
        SBAR();
#pragma unroll
        for (int n = 0; n < 32; ++n) {
          const int G = n >> 2, kt = n & 3;
          s[kt][0] = MFMA(kfr[n & 7], (G & 1) ? qfr[0] : asbf(qown[0][G >> 1]), s[kt][0]);
          if (n + PD < 32) kfr[(n + PD) & 7] = asbf(lds_r128(KADDR(n + PD)));
          SBAR();
          s[kt][1] = MFMA(kfr[n & 7], (G & 1) ? qfr[1] : asbf(qown[1][G >> 1]), s[kt][1]);
          if (!(G & 1) && kt < 2) qfr[kt] = asbf(lds_r128(qp + (kt * 4 + (G >> 1)) * 1024));
          SBAR();
        }
#undef KADDR
#pragma unroll
        for (int f = 0; f < 4; ++f) kb[f] ^= 32768;
        float al[2];
#pragma unroll
        for (int r = 0; r < 2; ++r) {
          float mx = fmaxf(fmaxf(s[0][r][0], s[0][r][1]), fmaxf(s[0][r][2], s[0][r][3]));
#pragma unroll
          for (int kt = 1; kt < 4; ++kt) mx = fmaxf(mx, fmaxf(fmaxf(s[kt][r][0], s[kt][r][1]), fmaxf(s[kt][r][2], s[kt][r][3])));
          mx = xmax(mx);
          const float mn = fmaxf(m_[r], mx);
          al[r] = __builtin_amdgcn_exp2f((m_[r] - mn) * SCL);
          m_[r] = mn;
          const float nm = -mn * SCL;
          float ps = 0.f;
#pragma unroll
          for (int kt = 0; kt < 4; ++kt)
#pragma unroll
            for (int e = 0; e < 4; ++e) { const float p = __builtin_amdgcn_exp2f(fmaf(s[kt][r][e], SCL, nm)); s[kt][r][e] = p; ps += p; }
          l_[r] = l_[r] * al[r] + ps;
#pragma unroll
          for (int ks = 0; ks < 2; ++ks)
            pf[ks][r] = u32x4{pk2(s[2 * ks][r][0], s[2 * ks][r][1]), pk2(s[2 * ks][r][2], s[2 * ks][r][3]),
                              pk2(s[2 * ks + 1][r][0], s[2 * ks + 1][r][1]), pk2(s[2 * ks + 1][r][2], s[2 * ks + 1][r][3])};
        }
        if (__any(al[0] != 1.f || al[1] != 1.f)) {
#pragma unroll
          for (int dt = 0; dt < 16; ++dt) { o[dt][0] *= al[0]; o[dt][1] *= al[1]; }
        }
      }
    }
    STAMP(9);
    {
      const float i0 = 1.f / xsum(l_[0]), i1 = 1.f / xsum(l_[1]);
#pragma unroll
      for (int kk = 0; kk < 8; ++kk)
#pragma unroll
        for (int r = 0; r < 2; ++r) {
          const float iv = r ? i1 : i0;
          const f32x4 a = o[2 * kk][r] * iv, b = o[2 * kk + 1][r] * iv;
          lds_w128(smem + LDS_K + ((hh * 8 + kk) * 4 + rh * 2 + r) * 1024 + lane * 16,
                   u32x4{pk2(a[0], a[1]), pk2(a[2], a[3]), pk2(b[0], b[1]), pk2(b[2], b[3])});
        }
    }
    const int u = 4 + w;
    WG_BARRIER();
    STAMP(10);
    if (u < 7) {
      const int ovoff = (g * 4 + (c & 1) * 32) * (DM * 2) + u * 128 + (c >> 1) * 16;
      u32x4 Rw[4][8];
#pragma unroll
      for (int s = 0; s < 4; ++s) oproj_load(Rw[s], rwo, ovoff, (kp0 + s) & 7);
      SBAR();
      f32x4 acc[4][4];
#pragma unroll
      for (int i = 0; i < 4; ++i)
#pragma unroll
        for (int rt = 0; rt < 4; ++rt) acc[i][rt] = f32x4{0.f, 0.f, 0.f, 0.f};
#pragma unroll
      for (int s = 0; s < 4; ++s) {
        oproj_pair(acc, Rw[s], smem + LDS_K + ((kp0 + s) & 7) * 8192 + lane * 16, c & 1); SBAR();
        oproj_load(Rw[s], rwo, ovoff, (kp0 + s + 4) & 7); SBAR();
      }
#pragma unroll
      for (int s = 0; s < 4; ++s) { oproj_pair(acc, Rw[s], smem + LDS_K + ((kp0 + s + 4) & 7) * 8192 + lane * 16, c & 1); SBAR(); }
      STAMP(11);
      oproj_store(acc, part, u, lane);
      STAMP(12); STAMPC(14); STAMP_OUT();
    }
  } else {
    const int lane = lane_id(), g = lane >> 4, c = lane & 15;
    XLOAD();
    const int h = wid - 4, hi = lane >> 5, ch = lane & 31;
    const rsrc_t rk = mk_rsrc(kg, KV_BYTES), rv = mk_rsrc(vg, KV_BYTES);
    const int voff = h * 1024 + lane * 16;
    const int row0 = 2 * h + hi;
    const int sig0 = (row0 + 12) & 15;
    const int kw0 = LDS_K + row0 * 512 + ((ch ^ sig0) << 4), kw1 = LDS_K + (row0 + 8) * 512 + ((ch ^ sig0 ^ 8) << 4);
    const int vw0 = LDS_V + row0 * 512 + ((ch ^ (row0 << 1)) << 4);
    const int u = h;
    const int ovoff = (g * 4 + (c & 1) * 32) * (DM * 2) + u * 128 + (c >> 1) * 16;
    u32x4 S[6][8];
#define LOADU(SET, RS, TILE) do { const int vo_ = (TILE) < 16 ? voff : OOB; _Pragma("unroll") for (int i = 0; i < 8; ++i) \
      S[SET][i] = __builtin_amdgcn_raw_buffer_load_b128(RS, vo_, ((TILE) & 15) * 32768 + i * 4096, AUX_NT); SBAR(); } while (0)
#define WRITEK(SET, BUF) do { _Pragma("unroll") for (int i = 0; i < 8; ++i) \
      lds_w128(smem + ((i & 1) ? kw1 : kw0) + (BUF) + (i >> 1) * 8192, S[SET][i]); SBAR(); } while (0)
#define WRITEV(SET, BUF) do { _Pragma("unroll") for (int i = 0; i < 8; ++i) \
      lds_w128(smem + vw0 + (BUF) + i * 4096, S[SET][i]); SBAR(); } while (0)
#define WAITV(N) do { asm volatile("s_waitcnt vmcnt(" #N ")" ::: "memory"); SBAR(); } while (0)
#define WLOAD(SET, P) do { oproj_load(S[SET], rwo, ovoff, (kp0 + (P)) & 7); SBAR(); } while (0)
    LOADU(0, rk, 0); LOADU(1, rk, 1);
    STAMP(1);
    XWRITE();
    STAMP(3);
    WG_BARRIER();
    WRITEK(0, 0); LOADU(2, rv, 0);
    WRITEK(1, 32768); LOADU(3, rk, 2);
    WAITV(8); LOADU(4, rv, 1);
    WAITV(8); LOADU(5, rk, 3);
    WAITV(8); LOADU(0, rv, 2);
    WAITV(8); LOADU(1, rk, 4);
    STAMP(2);
    WG_BARRIER();
    WRITEV(2, 0); LOADU(2, rv, 3);
    WG_BARRIER();
#pragma unroll 1
    for (int t = 0; t < 12; t += 3) {
      const int b0 = (t & 1) << 15, b1 = b0 ^ 32768;
      WRITEK(3, b0); LOADU(3, rk, t + 5); WRITEV(4, b1); LOADU(4, rv, t + 4);
      STAMP_IF(t == 0, 4); STAMP_IF(t == 3, 5); STAMP_IF(t == 6, 6); STAMP_IF(t == 9, 7);
      WG_BARRIER();
      WRITEK(5, b1); LOADU(5, rk, t + 6); WRITEV(0, b0); LOADU(0, rv, t + 5);
      WG_BARRIER();
      WRITEK(1, b0); LOADU(1, rk, t + 7); WRITEV(2, b1); LOADU(2, rv, t + 6);
      WG_BARRIER();
    }
    WLOAD(1, 0);
    WRITEK(3, 0); WLOAD(3, 1); WRITEV(4, 32768); WLOAD(4, 2);
    STAMP(8);
    WG_BARRIER();
    WRITEK(5, 32768); WLOAD(5, 3); WRITEV(0, 0);
    WG_BARRIER();
    WRITEV(2, 32768);
    WG_BARRIER();
    WG_BARRIER();
    STAMP(10);
    f32x4 acc[4][4];
#pragma unroll
    for (int i = 0; i < 4; ++i)
#pragma unroll
      for (int rt = 0; rt < 4; ++rt) acc[i][rt] = f32x4{0.f, 0.f, 0.f, 0.f};
#define OPAIR(SET, P) do { oproj_pair(acc, S[SET], smem + LDS_K + ((kp0 + (P)) & 7) * 8192 + lane * 16, c & 1); SBAR(); } while (0)
    OPAIR(1, 0); WLOAD(1, 4);
    OPAIR(3, 1); WLOAD(3, 5);
    OPAIR(4, 2); WLOAD(4, 6);
    OPAIR(5, 3); WLOAD(5, 7);
    OPAIR(1, 4); OPAIR(3, 5); OPAIR(4, 6); OPAIR(5, 7);
#undef OPAIR
#undef LOADU
#undef WRITEK
#undef WRITEV
#undef WAITV
#undef WLOAD
    STAMP(11);
    oproj_store(acc, part, u, lane);
    STAMP(12); STAMPC(14); STAMP_OUT();
  }
}

__global__ __launch_bounds__(256) void gqa_reduce(const char* __restrict__ ws, char* __restrict__ out) {
  const int lane = threadIdx.x & 63, wv = blockIdx.x * 4 + (threadIdx.x >> 6);
  const int grp = wv / 28, ur = wv % 28, u = ur >> 2, rt = ur & 3;
  const int g = lane >> 4, c = lane & 15;
  const char* p = ws + (size_t)grp * 8 * PART_BYTES + ur * 4096 + lane * 16;
  f32x4 a[4];
#pragma unroll
  for (int i = 0; i < 4; ++i) a[i] = *(const f32x4*)(p + i * 1024);
#pragma unroll
  for (int k = 1; k < 8; ++k)
#pragma unroll
    for (int i = 0; i < 4; ++i) a[i] += *(const f32x4*)(p + (size_t)k * PART_BYTES + i * 1024);
  const int blk = grp >> 4, bb = grp & 15;
  char* o = out + ((size_t)(bb * SQ + rt * 16 + c) * (2 * DM) + blk * DM + u * 64 + g * 16) * 2;
  *(u32x4*)o = u32x4{pk2(a[0][0], a[1][0]), pk2(a[2][0], a[3][0]), pk2(a[0][1], a[1][1]), pk2(a[2][1], a[3][1])};
  *(u32x4*)(o + 16) = u32x4{pk2(a[0][2], a[1][2]), pk2(a[2][2], a[3][2]), pk2(a[0][3], a[1][3]), pk2(a[2][3], a[3][3])};
}
}

extern "C" void kernel_launch(void* const* d_in, const int* in_sizes, int n_in, void* d_out, int out_size, void* d_ws, size_t ws_size,
                              hipStream_t stream) {
  (void)in_sizes; (void)n_in; (void)out_size; (void)ws_size;
  gqa_fused<<<256, 512, 0, stream>>>((const char*)d_in[0], (const char*)d_in[1], (const char*)d_in[2], (const char*)d_in[3], (const char*)d_in[4],
                                     (const char*)d_in[5], (const char*)d_in[6], (const char*)d_in[7], (const char*)d_in[8], (const char*)d_in[9],
                                     (char*)d_ws);
  gqa_reduce<<<224, 256, 0, stream>>>((const char*)d_ws, (char*)d_out);
}
```

```cpp
#include <hip/hip_runtime.h>
#include <hip/hip_bf16.h>

#define LAS __attribute__((address_space(3)))
#define DI __device__ __forceinline__
typedef __attribute__((ext_vector_type(8))) __bf16 bf16x8;
typedef __attribute__((ext_vector_type(4))) __bf16 bf16x4;
typedef __attribute__((ext_vector_type(2))) __bf16 bf16x2;
typedef __attribute__((ext_vector_type(16))) float f32x16;
typedef __attribute__((ext_vector_type(4))) float f32x4;
typedef __attribute__((ext_vector_type(2))) float f32x2;
typedef __attribute__((ext_vector_type(4))) unsigned u32x4;

namespace {
constexpr int SQ = 64, DM = 448, HKV = 8, SK = 1024, DH = 256;
constexpr int NQ = 4096;
constexpr int KV_BYTES = SK * DH * 2;
constexpr int W_BYTES = DM * NQ * 2;
constexpr int LDS_K = 0, LDS_V = 65536, LDS_QP = 131072, LDS_TOTAL = 163840;
constexpr int XRS = 912;
constexpr int SLAB = 7168;
constexpr size_t CTL_OFF = (size_t)16 << 20;
constexpr float SCL = 0.0625f * 1.4426950408889634f;
constexpr float THR = 8.f;
constexpr int AUX_NT = 2, AUX_SC1 = 16;
#define AUX_W 2
constexpr int OOB = 0x40000000;
constexpr unsigned SPIN_CAP = 1u << 21;

#define SBAR() __builtin_amdgcn_sched_barrier(0)
#define WG_BARRIER() do { SBAR(); asm volatile("s_waitcnt lgkmcnt(0)\n\ts_barrier" ::: "memory"); SBAR(); } while (0)
#define STAMP(idx) do {} while (0)
#define STAMP_IF(c, idx) do {} while (0)
#define STAMPC(idx) do {} while (0)
#define STAMP_DECL do {} while (0)
#define STAMP_OUT() do {} while (0)
#define MFMA(a, b, c) __builtin_amdgcn_mfma_f32_16x16x32_bf16((a), (b), (c), 0, 0, 0)
#define MFMA32(a, b, c) __builtin_amdgcn_mfma_f32_32x32x16_bf16((a), (b), (c), 0, 0, 0)
#define RLX_AGENT __ATOMIC_RELAXED, __HIP_MEMORY_SCOPE_AGENT

DI unsigned pk2(float a, float b) { f32x2 f = {a, b}; bf16x2 r = __builtin_convertvector(f, bf16x2); return __builtin_bit_cast(unsigned, r); }
DI bf16x8 asbf(u32x4 v) { return __builtin_bit_cast(bf16x8, v); }
DI u32x4 lds_r128(LAS char* p) { return *(LAS u32x4*)p; }
DI void lds_w128(LAS char* p, u32x4 v) { *(LAS u32x4*)p = v; }
DI bf16x4 lds_tr(LAS char* p) { return __builtin_amdgcn_ds_read_tr16_b64_v4bf16((LAS bf16x4*)p); }
DI unsigned perm2(int e, unsigned hi, unsigned lo) { return __builtin_amdgcn_perm(hi, lo, e ? 0x07060302u : 0x05040100u); }
DI unsigned swap1(unsigned v) { return (unsigned)__builtin_amdgcn_mov_dpp((int)v, 0xB1, 0xf, 0xf, true); }
DI float max3(float a, float b, float c) { return fmaxf(fmaxf(a, b), c); }
DI float hmax(float v) {
  auto b = __builtin_amdgcn_permlane32_swap(__float_as_uint(v), __float_as_uint(v), false, false);
  return fmaxf(__uint_as_float(b[0]), __uint_as_float(b[1]));
}
DI float hsum(float v) {
  auto b = __builtin_amdgcn_permlane32_swap(__float_as_uint(v), __float_as_uint(v), false, false);
  return __uint_as_float(b[0]) + __uint_as_float(b[1]);
}
DI int lane_id() {
  int l; asm volatile("v_mbcnt_lo_u32_b32 %0, -1, 0\n\tv_mbcnt_hi_u32_b32 %0, -1, %0" : "=v"(l)); return l; }
typedef __amdgpu_buffer_rsrc_t rsrc_t;
DI rsrc_t mk_rsrc(const void* p, int bytes) { return __builtin_amdgcn_make_buffer_rsrc((void*)p, 0, bytes, 0x00020000); }

DI int oproj_voff(int lane, int u) { const int g = lane >> 4, c = lane & 15;
  return (16 * (g & 1) + 4 * (g >> 1) + (c & 1) * 32) * (DM * 2) + u * 128 + (c >> 1) * 16; }
DI void oproj_load(u32x4 (&r)[8], rsrc_t rw, int voff, int kp) {
#pragma unroll
  for (int j = 0; j < 8; ++j)
    r[j] = __builtin_amdgcn_raw_buffer_load_b128(rw, voff, (kp * 64 + 8 * (j >> 2) + (j & 3)) * (DM * 2), AUX_W);
}
DI void oproj_pair(f32x4 (&acc)[4][4], const u32x4 (&r)[8], LAS char* ob, bool odd) {
#pragma unroll
  for (int st = 0; st < 2; ++st) {
    bf16x8 b[4];
#pragma unroll
    for (int rt = 0; rt < 4; ++rt) b[rt] = asbf(lds_r128(ob + st * 4096 + rt * 1024));
#pragma unroll
    for (int q = 0; q < 2; ++q) {
      unsigned d[8];
#pragma unroll
      for (int j = 0; j < 8; ++j) {
        const unsigned sw = swap1(st == 0 ? r[j][2 + q] : r[j][q]);
        d[j] = st == 0 ? (odd ? sw : r[j][q]) : (odd ? r[j][2 + q] : sw);
      }
#pragma unroll
      for (int e = 0; e < 2; ++e) {
        u32x4 a;
#pragma unroll
        for (int m = 0; m < 4; ++m) a[m] = perm2(e, d[2 * m + 1], d[2 * m]);
#pragma unroll
        for (int rt = 0; rt < 4; ++rt) acc[2 * q + e][rt] = MFMA(asbf(a), b[rt], acc[2 * q + e][rt]);
      }
    }
  }
}
DI void publish(const f32x4 (&acc)[4][4], char* ws, int bid, int u, volatile LAS unsigned* ready) {
  const int lane = lane_id(), g = lane >> 4, c = lane & 15, me = bid & 7;
  const rsrc_t rp = mk_rsrc(ws + (size_t)(bid >> 3) * 64 * SLAB, 64 * SLAB);
  const int voff = ((c >> 3) * 8 + me) * SLAB + u * 1024 + g * 128 + (c & 7) * 16;
#pragma unroll
  for (int rt = 0; rt < 4; ++rt)
#pragma unroll
    for (int p = 0; p < 2; ++p) {
      const u32x4 v = {pk2(acc[0][rt][2 * p], acc[1][rt][2 * p]), pk2(acc[2][rt][2 * p], acc[3][rt][2 * p]),
                       pk2(acc[0][rt][2 * p + 1], acc[1][rt][2 * p + 1]), pk2(acc[2][rt][2 * p + 1], acc[3][rt][2 * p + 1])};
      __builtin_amdgcn_raw_buffer_store_b128(v, rp, voff, rt * 16 * SLAB + p * 512, AUX_SC1);
    }
  asm volatile("s_waitcnt vmcnt(0)" ::: "memory");
  for (unsigned spins = 0; *ready == 0u && spins < SPIN_CAP; ++spins) __builtin_amdgcn_s_sleep(2);
  if (lane < 8) __hip_atomic_fetch_add((unsigned*)(ws + CTL_OFF + (size_t)((bid & ~7) + lane) * 512), 1u, RLX_AGENT);
}
DI unsigned* ctl_chal(char* ws, int wg, int slot) { return (unsigned*)(ws + CTL_OFF + (size_t)wg * 512 + 128 + slot * 4); }
DI unsigned* ctl_resp(char* ws, int wg, int slot) { return (unsigned*)(ws + CTL_OFF + (size_t)wg * 512 + 256 + slot * 4); }
DI void hs_echo(char* ws, int bid, int lane) {
  const unsigned cv = __hip_atomic_load(ctl_chal(ws, bid, lane), RLX_AGENT);
  const unsigned rv = __hip_atomic_load(ctl_resp(ws, (bid & ~7) + lane, bid & 7), RLX_AGENT);
  if (cv != rv) __hip_atomic_store(ctl_resp(ws, (bid & ~7) + lane, bid & 7), cv, RLX_AGENT);
}

__global__ __launch_bounds__(512, 2) void gqa_fused(const char* __restrict__ xA, const char* __restrict__ kA, const char* __restrict__ vA,
                                                    const char* __restrict__ wqA, const char* __restrict__ woA,
                                                    const char* __restrict__ xB, const char* __restrict__ kB, const char* __restrict__ vB,
                                                    const char* __restrict__ wqB, const char* __restrict__ woB,
                                                    char* __restrict__ out, char* ws) {
  __shared__ __attribute__((aligned(1024))) char smem_[LDS_TOTAL];
  LAS char* smem = (LAS char*)smem_;
  const int wid = __builtin_amdgcn_readfirstlane(threadIdx.x >> 6);
  const int bid = blockIdx.x, kvh = bid & 7, bb = (bid >> 3) & 15, blk = bid >> 7;
  const char* xg = (blk ? xB : xA) + (size_t)bb * SQ * DM * 2;
  const char* kg = (blk ? kB : kA) + (size_t)(bb * HKV + kvh) * KV_BYTES;
  const char* vg = (blk ? vB : vA) + (size_t)(bb * HKV + kvh) * KV_BYTES;
  const char* wq = blk ? wqB : wqA;
  const char* wo = (blk ? woB : woA) + (size_t)kvh * 512 * DM * 2;
  const rsrc_t rwo = mk_rsrc(wo, 512 * DM * 2);
  const int kp0 = bb & 7;
  volatile LAS unsigned* ready = (volatile LAS unsigned*)(smem + LDS_QP + 3 * 8192);
  STAMP_DECL;
  STAMP(0); STAMPC(13);
#define XPIECE(f) ((((wid + kvh) & 7) * 7) + (f))
#define XLOAD() u32x4 xr[7]; do { const rsrc_t rx = mk_rsrc(xg, SQ * DM * 2); _Pragma("unroll") for (int f = 0; f < 7; ++f) \
      xr[f] = __builtin_amdgcn_raw_buffer_load_b128(rx, lane * 16, XPIECE(f) * 1024, AUX_W); SBAR(); } while (0)
#define XWRITE() do { _Pragma("unroll") for (int f = 0; f < 7; ++f) { const unsigned idx_ = XPIECE(f) * 64 + lane; \
      lds_w128(smem + LDS_V + (idx_ + idx_ / 56u) * 16, xr[f]); } SBAR(); } while (0)

  if (wid < 4) {
    const int w = wid, hh = w >> 1, rh = w & 1;
    u32x4 qown[8];
    {
      const int lane = lane_id(), g = lane >> 4, c = lane & 15;
      XLOAD();
      const rsrc_t rwq = mk_rsrc(wq, W_BYTES);
      const int wvoff = g * 8 * (NQ * 2) + (kvh * 512 + w * 128 + c * 8) * 2;
      const int ks0 = bb < 14 ? bb : bb - 14;
      u32x4 R[3][8];
#define KSOF(S) ((ks0 + (S)) >= 14 ? ks0 + (S) - 14 : ks0 + (S))
#define QLOAD1(ST, S, J) R[ST][J] = __builtin_amdgcn_raw_buffer_load_b128(rwq, wvoff, KSOF(S) * 32 * (NQ * 2) + (J) * (NQ * 2), AUX_W)
#pragma unroll
      for (int j = 0; j < 8; ++j) QLOAD1(0, 0, j);
#pragma unroll
      for (int j = 0; j < 8; ++j) QLOAD1(1, 1, j);
      SBAR();
      XWRITE();
      WG_BARRIER();
      STAMP(1);
      f32x4 acc[8][4];
#pragma unroll
      for (int i = 0; i < 8; ++i)
#pragma unroll
        for (int rt = 0; rt < 4; ++rt) acc[i][rt] = f32x4{0.f, 0.f, 0.f, 0.f};
      bf16x8 bq[4]; u32x4 aq[2];
      LAS char* xb = smem + LDS_V + c * XRS + g * 16;
#pragma unroll
      for (int rt = 0; rt < 4; ++rt) bq[rt] = asbf(lds_r128(xb + ks0 * 64 + rt * 16 * XRS));
#pragma unroll
      for (int m = 0; m < 4; ++m) aq[0][m] = perm2(0, R[0][2 * m + 1][0], R[0][2 * m][0]);
      SBAR();
#define QSTEP(CUR, NXT, LD, S, DOLD, DONX) do { \
        _Pragma("unroll") for (int i = 0; i < 8; ++i) { \
          _Pragma("unroll") for (int rt = 0; rt < 4; ++rt) { \
            acc[i][rt] = MFMA(asbf(aq[i & 1]), bq[rt], acc[i][rt]); \
            if (i < 7) aq[(i + 1) & 1][rt] = perm2((i + 1) & 1, R[CUR][2 * rt + 1][(i + 1) >> 1], R[CUR][2 * rt][(i + 1) >> 1]); \
            else if (DONX) { aq[0][rt] = perm2(0, R[NXT][2 * rt + 1][0], R[NXT][2 * rt][0]); bq[rt] = asbf(lds_r128(xb + KSOF((S) + 1) * 64 + rt * 16 * XRS)); } \
            if (DOLD && rt == 1) QLOAD1(LD, (S) + 2, i); \
            SBAR(); } } } while (0)
#pragma unroll 1
      for (int s = 0; s < 12; s += 3) {
        QSTEP(0, 1, 2, s, true, true);
        QSTEP(1, 2, 0, s + 1, true, true);
        QSTEP(2, 0, 1, s + 2, true, true);
      }
      SBAR(); __builtin_amdgcn_s_barrier(); SBAR();
      QSTEP(0, 1, 2, 12, false, true);
      QSTEP(1, 2, 0, 13, false, false);
#undef QSTEP
#undef QLOAD1
#undef KSOF
#pragma unroll
      for (int f = 0; f < 4; ++f) {
        u32x4 lo[2], hi[2];
#pragma unroll
        for (int m = 0; m < 4; ++m) {
          auto a = __builtin_amdgcn_permlane16_swap(pk2(acc[2 * m][0][f], acc[2 * m + 1][0][f]), pk2(acc[2 * m][1][f], acc[2 * m + 1][1][f]), false, false);
          auto b = __builtin_amdgcn_permlane16_swap(pk2(acc[2 * m][2][f], acc[2 * m + 1][2][f]), pk2(acc[2 * m][3][f], acc[2 * m + 1][3][f]), false, false);
          lo[0][m] = a[0]; lo[1][m] = a[1]; hi[0][m] = b[0]; hi[1][m] = b[1];
        }
#pragma unroll
        for (int e = 0; e < 2; ++e) {
          qown[e * 4 + f] = rh ? hi[e] : lo[e];
          lds_w128(smem + LDS_QP + (w ^ 1) * 8192 + (e * 4 + f) * 1024 + lane * 16, rh ? lo[e] : hi[e]);
        }
      }
    }
    STAMP(2);
    {
      const int lane = lane_id(), h = lane >> 5, r = lane & 31;
      f32x16 o[8];
#pragma unroll
      for (int dt = 0; dt < 8; ++dt)
#pragma unroll
        for (int i = 0; i < 16; ++i) o[dt][i] = 0.f;
      float mc = 0.f, lsum = 0.f;
      f32x16 s0, s1;
      u32x4 p0[2], p1[2];
      int kb = LDS_K + r * 512 + rh * 256 + (((8 * h) ^ (r & 15)) << 4);
      int vb = LDS_V + (4 * h + ((lane & 15) >> 2)) * 512 + (((lane & 15) >> 2) << 6) + ((lane >> 4) & 1) * 32 + (lane & 3) * 8;
      LAS char* qp = smem + LDS_QP + w * 8192 + lane * 16;
      constexpr int PD = 4, RN = 6;
      bf16x4 vl[RN], vh[RN]; bf16x8 kf[RN], qf[2];
      float t0, t1, t2, t3, t4, mx;
#define FETCH(N) do { constexpr int n_ = (N), blk_ = n_ >> 4, j_ = n_ & 15; \
        if (blk_ & 1) kf[n_ % RN] = asbf(lds_r128(smem + (kb ^ (((j_ >> 1) << 4) | ((j_ & 1) << 8))) + (blk_ >> 1) * 16384)); \
        else { LAS char* a_ = smem + (vb ^ ((j_ & 3) << 6)) + ((j_ >> 2) & 1) * 256 + (blk_ + (j_ >> 3)) * 8192; vl[n_ % RN] = lds_tr(a_); vh[n_ % RN] = lds_tr(a_ + 4096); } } while (0)
#define FETCHQ(J) qf[((J) >> 1) & 1] = asbf(lds_r128(qp + ((J) >> 1) * 1024))
#define JOB_AB(X, j) do { \
        if ((j) < 4) { _Pragma("unroll") for (int i_ = 0; i_ < 4; ++i_) X[4 * (j) + i_] = fmaf(X[4 * (j) + i_], SCL, -mc); } \
        else if ((j) == 4) { t0 = max3(X[0], X[1], X[2]); t1 = max3(X[3], X[4], X[5]); t2 = max3(X[6], X[7], X[8]); } \
        else if ((j) == 5) { t3 = max3(X[9], X[10], X[11]); t4 = max3(X[12], X[13], X[14]); t0 = max3(t0, t1, X[15]); } \
        else if ((j) == 6) { t0 = fmaxf(t0, max3(t2, t3, t4)); } \
        else if ((j) == 7) { mx = hmax(t0); } } while (0)
#define JOB_EXP(X, P, j) do { X[j] = __builtin_amdgcn_exp2f(X[j]); \
        if ((j) > 0) lsum += X[(j) - 1]; \
        if ((j) >= 2 && !((j) & 1)) P[((j) - 2) >> 3][(((j) - 2) >> 1) & 3] = pk2(X[(j) - 2], X[(j) - 1]); \
        if ((j) == 15) { lsum += X[15]; P[1][3] = pk2(X[14], X[15]); } } while (0)
#define RESCALE(X) do { if (__any(mx > THR)) { const float d_ = fmaxf(mx, 0.f), al_ = __builtin_amdgcn_exp2f(-d_); mc += d_; lsum *= al_; \
          _Pragma("unroll") for (int i_ = 0; i_ < 16; ++i_) X[i_] -= d_; \
          _Pragma("unroll") for (int dt = 0; dt < 8; ++dt) o[dt] *= al_; } SBAR(); } while (0)
#define PVBLOCK(B, P, NEXT, JOB, X) do { _Pragma("unroll") for (int j = 0; j < 16; ++j) { \
          const bf16x8 a_ = __builtin_shufflevector(vl[((B) * 16 + j) % RN], vh[((B) * 16 + j) % RN], 0, 1, 2, 3, 4, 5, 6, 7); \
          o[j & 7] = MFMA32(a_, asbf(P[j >> 3]), o[j & 7]); \
          if (j + PD < 16 || (NEXT)) { switch (j) { \
            case 0: FETCH((B) * 16 + 0 + PD); break; case 1: FETCH((B) * 16 + 1 + PD); break; case 2: FETCH((B) * 16 + 2 + PD); break; case 3: FETCH((B) * 16 + 3 + PD); break; \
            case 4: FETCH((B) * 16 + 4 + PD); break; case 5: FETCH((B) * 16 + 5 + PD); break; case 6: FETCH((B) * 16 + 6 + PD); break; case 7: FETCH((B) * 16 + 7 + PD); break; \
            case 8: FETCH((B) * 16 + 8 + PD); break; case 9: FETCH((B) * 16 + 9 + PD); break; case 10: FETCH((B) * 16 + 10 + PD); break; case 11: FETCH((B) * 16 + 11 + PD); break; \
            case 12: FETCH((B) * 16 + 12 + PD); break; case 13: FETCH((B) * 16 + 13 + PD); break; case 14: FETCH((B) * 16 + 14 + PD); break; default: FETCH((B) * 16 + 15 + PD); break; } } \
          if ((NEXT) && j == 14) FETCHQ(1); \
          if ((JOB) == 1) JOB_AB(X, j); \
          SBAR(); } } while (0)
#define QKBLOCK(B, S, NEXT, JOB, X, P) do { _Pragma("unroll") for (int j = 0; j < 16; ++j) { \
          const bf16x8 q_ = (j & 1) ? qf[(j >> 1) & 1] : asbf(qown[j >> 1]); \
          if (j == 0) { f32x16 z_; _Pragma("unroll") for (int i_ = 0; i_ < 16; ++i_) z_[i_] = 0.f; S = MFMA32(kf[((B) * 16 + j) % RN], q_, z_); } \
          else S = MFMA32(kf[((B) * 16 + j) % RN], q_, S); \
          if (j + PD < 16 || (NEXT)) { switch (j) { \
            case 0: FETCH((B) * 16 + 0 + PD); break; case 1: FETCH((B) * 16 + 1 + PD); break; case 2: FETCH((B) * 16 + 2 + PD); break; case 3: FETCH((B) * 16 + 3 + PD); break; \
            case 4: FETCH((B) * 16 + 4 + PD); break; case 5: FETCH((B) * 16 + 5 + PD); break; case 6: FETCH((B) * 16 + 6 + PD); break; case 7: FETCH((B) * 16 + 7 + PD); break; \
            case 8: FETCH((B) * 16 + 8 + PD); break; case 9: FETCH((B) * 16 + 9 + PD); break; case 10: FETCH((B) * 16 + 10 + PD); break; case 11: FETCH((B) * 16 + 11 + PD); break; \
            case 12: FETCH(((B) * 16 + 12 + PD) & 63); break; case 13: FETCH(((B) * 16 + 13 + PD) & 63); break; case 14: FETCH(((B) * 16 + 14 + PD) & 63); break; default: FETCH(((B) * 16 + 15 + PD) & 63); break; } } \
          if (!(j & 1) && j + 3 < 16) FETCHQ(j + 3); \
          if ((JOB) == 2) JOB_EXP(X, P, j); \
          SBAR(); } } while (0)
#define FETCH4(N) do { FETCH(N); FETCH((N) + 1); FETCH((N) + 2); FETCH((N) + 3); } while (0)
      static_assert(PD == 4, "FETCH4");
      WG_BARRIER();
      STAMP(3);
      FETCH4(16); FETCHQ(1); SBAR();
      QKBLOCK(1, s0, 0, 0, s0, p0);
      FETCH4(48); FETCHQ(1); SBAR();
#pragma unroll
      for (int j = 0; j < 8; ++j) JOB_AB(s0, j);
      mc = mx;
#pragma unroll
      for (int i = 0; i < 16; ++i) s0[i] -= mx;
      SBAR();
      QKBLOCK(3, s1, 0, 2, s0, p0);
      kb ^= 32768;
#pragma unroll 1
      for (int t = 0; t < 15; ++t) {
        WG_BARRIER();
        STAMP_IF(t == 0, 4); STAMP_IF(t == 4, 5); STAMP_IF(t == 8, 6); STAMP_IF(t == 12, 7);
        FETCH4(0); SBAR();
        PVBLOCK(0, p0, 1, 1, s1);
        RESCALE(s1);
        QKBLOCK(1, s0, 1, 2, s1, p1);
        PVBLOCK(2, p1, 1, 1, s0);
        RESCALE(s0);
        QKBLOCK(3, s1, 0, 2, s0, p0);
        kb ^= 32768; vb ^= 32768;
      }
      WG_BARRIER();
      STAMP(8);
      FETCH4(0); SBAR();
      PVBLOCK(0, p0, 0, 1, s1);
      RESCALE(s1);
      FETCH4(32); SBAR();
#pragma unroll
      for (int j = 0; j < 16; ++j) JOB_EXP(s1, p1, j);
      SBAR();
      PVBLOCK(2, p1, 0, 0, s0);
#undef FETCH
#undef FETCHQ
#undef FETCH4
#undef JOB_AB
#undef JOB_EXP
#undef RESCALE
#undef PVBLOCK
#undef QKBLOCK
      STAMP(9);
      const float iv = 1.f / hsum(lsum);
      LAS char* ow = smem + LDS_K + (hh * 8 * 4 + rh * 2 + (r >> 4)) * 1024 + (2 * h * 16 + (r & 15)) * 16;
#pragma unroll
      for (int dt = 0; dt < 8; ++dt)
#pragma unroll
        for (int pp = 0; pp < 2; ++pp)
          lds_w128(ow + dt * 4096 + pp * 256,
                   u32x4{pk2(o[dt][8 * pp] * iv, o[dt][8 * pp + 1] * iv), pk2(o[dt][8 * pp + 2] * iv, o[dt][8 * pp + 3] * iv),
                         pk2(o[dt][8 * pp + 4] * iv, o[dt][8 * pp + 5] * iv), pk2(o[dt][8 * pp + 6] * iv, o[dt][8 * pp + 7] * iv)});
      if (w == 3 && lane == 0) *ready = 0u;
    }
    WG_BARRIER();
    STAMP(10);
    const int lane = lane_id();
    if (w < 3) {
      const int u = 4 + w;
      const int ovoff = oproj_voff(lane, u);
      u32x4 Rw[4][8];
#pragma unroll
      for (int s = 0; s < 4; ++s) oproj_load(Rw[s], rwo, ovoff, (kp0 + s) & 7);
      SBAR();
      f32x4 acc[4][4];
#pragma unroll
      for (int i = 0; i < 4; ++i)
#pragma unroll
        for (int rt = 0; rt < 4; ++rt) acc[i][rt] = f32x4{0.f, 0.f, 0.f, 0.f};
#pragma unroll
      for (int s = 0; s < 4; ++s) {
        oproj_pair(acc, Rw[s], smem + LDS_K + ((kp0 + s) & 7) * 8192 + lane * 16, lane & 1); SBAR();
        oproj_load(Rw[s], rwo, ovoff, (kp0 + s + 4) & 7); SBAR();
      }
#pragma unroll
      for (int s = 0; s < 4; ++s) { oproj_pair(acc, Rw[s], smem + LDS_K + ((kp0 + s + 4) & 7) * 8192 + lane * 16, lane & 1); SBAR(); }
      STAMP(11);
      publish(acc, ws, bid, u, ready);
    } else {
      const int l8 = lane & 7;
      for (unsigned spins = 0; spins < SPIN_CAP; ++spins) {
        const unsigned cv = __hip_atomic_load(ctl_chal(ws, (bid & ~7) + l8, bid & 7), RLX_AGENT);
        const unsigned rv = __hip_atomic_load(ctl_resp(ws, bid, l8), RLX_AGENT);
        if (__all(cv == rv)) break;
        if (lane < 8) hs_echo(ws, bid, lane);
        __builtin_amdgcn_s_sleep(8);
      }
      if (lane == 0) *ready = 1u;
      const unsigned* cnt = (const unsigned*)(ws + CTL_OFF + (size_t)bid * 512);
      for (unsigned spins = 0; spins < SPIN_CAP; ++spins) {
        if (__hip_atomic_load(cnt, RLX_AGENT) >= 56u) break;
        if ((spins & 63u) == 63u && lane < 8) hs_echo(ws, bid, lane);
        __builtin_amdgcn_s_sleep(2);
      }
    }
  } else {
    const int lane = lane_id();
    const int h = wid - 4, hi = lane >> 5, ch = lane & 31;
    unsigned hs_r = 0;
    if (h == 0 && lane < 8) {
      if (lane == 0) __hip_atomic_store((unsigned*)(ws + CTL_OFF + (size_t)bid * 512), 0u, RLX_AGENT);
      hs_r = __hip_atomic_load(ctl_resp(ws, bid, lane), RLX_AGENT);
    }
    XLOAD();
    const rsrc_t rk = mk_rsrc(kg, KV_BYTES), rv = mk_rsrc(vg, KV_BYTES);
    const int voff = h * 1024 + lane * 16;
    const int row0 = 2 * h + hi;
    const int kw0 = LDS_K + row0 * 512 + ((ch ^ row0) << 4), kw1 = LDS_K + (row0 + 8) * 512 + ((ch ^ row0 ^ 8) << 4);
    const int vw0 = LDS_V + row0 * 512 + ((ch ^ ((row0 & 3) << 2)) << 4);
    const int ovoff = oproj_voff(lane, h);
    u32x4 S[6][8];
#define LOADU(SET, RS, TILE) do { _Pragma("unroll") for (int i = 0; i < 8; ++i) \
      S[SET][i] = __builtin_amdgcn_raw_buffer_load_b128(RS, voff, (TILE) * 32768 + i * 4096, AUX_NT); SBAR(); } while (0)
#define WRITEK(SET, BUF) do { _Pragma("unroll") for (int i = 0; i < 8; ++i) \
      lds_w128(smem + ((i & 1) ? kw1 : kw0) + (BUF) + (i >> 1) * 8192, S[SET][i]); SBAR(); } while (0)
#define WRITEV(SET, BUF) do { _Pragma("unroll") for (int i = 0; i < 8; ++i) \
      lds_w128(smem + vw0 + (BUF) + i * 4096, S[SET][i]); SBAR(); } while (0)
#define WLOAD(SET, P) do { oproj_load(S[SET], rwo, ovoff, (kp0 + (P)) & 7); SBAR(); } while (0)
#define HS_POST() do { if (h == 0 && lane < 8) __hip_atomic_store(ctl_chal(ws, (bid & ~7) + lane, bid & 7), hs_r + 1u, RLX_AGENT); SBAR(); } while (0)
    int t0;
    if (blk) {
      LOADU(2, rk, 0); LOADU(3, rk, 1);
      XWRITE(); HS_POST();
      WRITEK(2, 0); LOADU(4, rv, 0);
      WRITEK(3, 32768); LOADU(5, rk, 2);
      LOADU(2, rv, 1); LOADU(3, rk, 3); LOADU(0, rv, 2); LOADU(1, rk, 4);
      STAMP(1);
      WG_BARRIER();
      WG_BARRIER();
      STAMP(2);
      WG_BARRIER();
      WRITEV(4, 0); LOADU(4, rv, 3);
      WG_BARRIER();
      WRITEK(5, 0); WRITEV(2, 32768); LOADU(5, rk, 5);
      WG_BARRIER();
      WRITEK(3, 32768); WRITEV(0, 0); LOADU(2, rv, 4);
      WG_BARRIER();
      WRITEK(1, 0); WRITEV(4, 32768); LOADU(0, rk, 6);
      WG_BARRIER();
      WRITEK(5, 32768); WRITEV(2, 0); LOADU(1, rv, 5);
      WG_BARRIER();
      t0 = 4;
    } else {
      XWRITE(); HS_POST();
      STAMP(1);
      WG_BARRIER();
      WG_BARRIER();
      STAMP(2);
      LOADU(2, rk, 0); LOADU(3, rk, 1);
      WRITEK(2, 0); LOADU(4, rv, 0);
      WG_BARRIER();
      WRITEK(3, 32768); LOADU(0, rk, 2); WRITEV(4, 0); LOADU(1, rv, 1);
      WG_BARRIER();
      t0 = 0;
    }
#define STEADY(T) do { const int b0_ = ((T) & 1) << 15; WRITEK(0, b0_); LOADU(0, rk, (T) + 3); WRITEV(1, b0_ ^ 32768); LOADU(1, rv, (T) + 2); } while (0)
#pragma unroll 1
    for (int t = t0; t < 9; ++t) {
      STEADY(t);
      STAMP_IF(t == 4, 5); STAMP_IF(t == 8, 6);
      WG_BARRIER();
    }
    unsigned hs_c = 0, hs_v = 0;
    if (h == 0 && lane < 8) { hs_c = __hip_atomic_load(ctl_chal(ws, bid, lane), RLX_AGENT); hs_v = __hip_atomic_load(ctl_resp(ws, (bid & ~7) + lane, bid & 7), RLX_AGENT); }
    SBAR();
    STEADY(9); WLOAD(2, 0);
    WG_BARRIER();
    STEADY(10);
    if (h == 0 && lane < 8 && hs_c != hs_v) __hip_atomic_store(ctl_resp(ws, (bid & ~7) + lane, bid & 7), hs_c, RLX_AGENT);
    SBAR();
    WLOAD(3, 1);
    WG_BARRIER();
    STEADY(11); WLOAD(4, 2);
    WG_BARRIER();
    STEADY(12); WLOAD(5, 3);
    STAMP(7);
    WG_BARRIER();
    WRITEK(0, 32768); WRITEV(1, 0); LOADU(1, rv, 15);
    WG_BARRIER();
    WRITEV(1, 32768);
    STAMP(8);
    WG_BARRIER();
    WG_BARRIER();
    STAMP(10);
    f32x4 acc[4][4];
#pragma unroll
    for (int i = 0; i < 4; ++i)
#pragma unroll
      for (int rt = 0; rt < 4; ++rt) acc[i][rt] = f32x4{0.f, 0.f, 0.f, 0.f};
#define OPAIR(SET, P) do { oproj_pair(acc, S[SET], smem + LDS_K + ((kp0 + (P)) & 7) * 8192 + lane * 16, lane & 1); SBAR(); } while (0)
    OPAIR(2, 0); WLOAD(2, 4);
    OPAIR(3, 1); WLOAD(3, 5);
    OPAIR(4, 2); WLOAD(4, 6);
    OPAIR(5, 3); WLOAD(5, 7);
    OPAIR(2, 4); OPAIR(3, 5); OPAIR(4, 6); OPAIR(5, 7);
#undef OPAIR
#undef STEADY
#undef LOADU
#undef WRITEK
#undef WRITEV
#undef WLOAD
#undef HS_POST
    STAMP(11);
    publish(acc, ws, bid, h, ready);
  }
  WG_BARRIER();
  STAMP(15);
  if (wid < 7) {
    const int lane = lane_id();
    const rsrc_t rp = mk_rsrc(ws + ((size_t)(bid >> 3) * 64 + kvh * 8) * SLAB, 8 * SLAB);
    u32x4 v[8];
#pragma unroll
    for (int s = 0; s < 8; ++s) v[s] = __builtin_amdgcn_raw_buffer_load_b128(rp, wid * 1024 + lane * 16, s * SLAB, AUX_SC1);
    float a[8];
#pragma unroll
    for (int k = 0; k < 4; ++k) { a[2 * k] = __uint_as_float(v[0][k] << 16); a[2 * k + 1] = __uint_as_float(v[0][k] & 0xffff0000u); }
#pragma unroll
    for (int s = 1; s < 8; ++s)
#pragma unroll
      for (int k = 0; k < 4; ++k) { a[2 * k] += __uint_as_float(v[s][k] << 16); a[2 * k + 1] += __uint_as_float(v[s][k] & 0xffff0000u); }
    char* o = out + ((size_t)(bb * SQ + kvh * 8 + (lane & 7)) * (2 * DM) + blk * DM + wid * 64 + ((lane >> 3) & 3) * 16 + (lane >> 5) * 8) * 2;
    *(u32x4*)o = u32x4{pk2(a[0], a[1]), pk2(a[2], a[3]), pk2(a[4], a[5]), pk2(a[6], a[7])};
  }
  STAMP(12); STAMPC(14); STAMP_OUT();
}
}

extern "C" void kernel_launch(void* const* d_in, const int* in_sizes, int n_in, void* d_out, int out_size, void* d_ws, size_t ws_size,
                              hipStream_t stream) {
  (void)in_sizes; (void)n_in; (void)out_size; (void)ws_size;
  gqa_fused<<<256, 512, 0, stream>>>((const char*)d_in[0], (const char*)d_in[1], (const char*)d_in[2], (const char*)d_in[3], (const char*)d_in[4],
                                     (const char*)d_in[5], (const char*)d_in[6], (const char*)d_in[7], (const char*)d_in[8], (const char*)d_in[9],
                                     (char*)d_out, (char*)d_ws);
}
```

```cpp
#include <hip/hip_runtime.h>
#include <hip/hip_bf16.h>
#include <utility>
#include <type_traits>

#define LAS __attribute__((address_space(3)))
#define DI __device__ __forceinline__
typedef __attribute__((ext_vector_type(8))) __bf16 bf16x8;
typedef __attribute__((ext_vector_type(4))) __bf16 bf16x4;
typedef __attribute__((ext_vector_type(2))) __bf16 bf16x2;
typedef __attribute__((ext_vector_type(16))) float f32x16;
typedef __attribute__((ext_vector_type(4))) float f32x4;
typedef __attribute__((ext_vector_type(2))) float f32x2;
typedef __attribute__((ext_vector_type(4))) unsigned u32x4;

namespace {
constexpr int SQ = 64, DM = 448, HKV = 8, SK = 1024, DH = 256;
constexpr int NQ = 4096;
constexpr int KV_BYTES = SK * DH * 2;
constexpr int W_BYTES = DM * NQ * 2;
constexpr int LDS_K = 0, LDS_V = 65536, LDS_QP = 131072, LDS_TOTAL = 163840;
constexpr int LDS_X = 34816, LDS_QT = LDS_V + 32768;
constexpr int XRS = 912;
constexpr int SLAB = 7168;
constexpr size_t CTL_OFF = (size_t)16 << 20;
constexpr float SCL = 0.0625f * 1.4426950408889634f;
constexpr float THR = 8.f;
constexpr int AUX_NT = 2, AUX_SC1 = 16;
#define AUX_W 2
#define OPOL ""
#define QPOL "nt"
#define WARM_AUX 2
#define OROT 1
constexpr int OOB = 0x40000000;
constexpr unsigned SPIN_CAP = 1u << 21;

#define SBAR() __builtin_amdgcn_sched_barrier(0)
#define WG_BARRIER() do { SBAR(); asm volatile("s_waitcnt lgkmcnt(0)\n\ts_barrier" ::: "memory"); SBAR(); } while (0)
#define STAMP(idx) do {} while (0)
#define STAMP_IF(c, idx) do {} while (0)
#define STAMPC(idx) do {} while (0)
#define STAMP_DECL do {} while (0)
#define STAMP_OUT() do {} while (0)
#define PACEN 7
#define PACE() asm volatile("s_nop %0" :: "i"(PACEN))
#define NAP() do {} while (0)
#define MFMA(a, b, c) __builtin_amdgcn_mfma_f32_16x16x32_bf16((a), (b), (c), 0, 0, 0)
#define MFMA32(a, b, c) __builtin_amdgcn_mfma_f32_32x32x16_bf16((a), (b), (c), 0, 0, 0)
#define RLX_AGENT __ATOMIC_RELAXED, __HIP_MEMORY_SCOPE_AGENT

DI unsigned pk2(float a, float b) { f32x2 f = {a, b}; bf16x2 r = __builtin_convertvector(f, bf16x2); return __builtin_bit_cast(unsigned, r); }
DI bf16x8 asbf(u32x4 v) { return __builtin_bit_cast(bf16x8, v); }
DI u32x4 lds_r128(LAS char* p) { return *(LAS u32x4*)p; }
DI void lds_w128(LAS char* p, u32x4 v) { *(LAS u32x4*)p = v; }
DI bf16x4 lds_tr(LAS char* p) { return __builtin_amdgcn_ds_read_tr16_b64_v4bf16((LAS bf16x4*)p); }
DI unsigned perm2(int e, unsigned hi, unsigned lo) { return __builtin_amdgcn_perm(hi, lo, e ? 0x07060302u : 0x05040100u); }
DI float max3(float a, float b, float c) { return fmaxf(fmaxf(a, b), c); }
DI float hmax(float v) {
  auto b = __builtin_amdgcn_permlane32_swap(__float_as_uint(v), __float_as_uint(v), false, false);
  return fmaxf(__uint_as_float(b[0]), __uint_as_float(b[1]));
}
DI float hsum(float v) {
  auto b = __builtin_amdgcn_permlane32_swap(__float_as_uint(v), __float_as_uint(v), false, false);
  return __uint_as_float(b[0]) + __uint_as_float(b[1]);
}
DI int lane_id() {
  int l; asm volatile("v_mbcnt_lo_u32_b32 %0, -1, 0\n\tv_mbcnt_hi_u32_b32 %0, -1, %0" : "=v"(l)); return l; }
typedef __amdgpu_buffer_rsrc_t rsrc_t;
DI rsrc_t mk_rsrc(const void* p, int bytes) { return __builtin_amdgcn_make_buffer_rsrc((void*)p, 0, bytes, 0x00020000); }
template <class F, int... I> DI void static_for_(F&& f, std::integer_sequence<int, I...>) { (f(std::integral_constant<int, I>{}), ...); }
template <int N, class F> DI void static_for(F&& f) { static_for_(f, std::make_integer_sequence<int, N>{}); }

DI unsigned* ctl_chal(char* ws, int wg, int slot) { return (unsigned*)(ws + CTL_OFF + (size_t)wg * 512 + 128 + slot * 4); }
DI unsigned* ctl_resp(char* ws, int wg, int slot) { return (unsigned*)(ws + CTL_OFF + (size_t)wg * 512 + 256 + slot * 4); }
DI void hs_echo(char* ws, int lid, int lane) {
  const unsigned cv = __hip_atomic_load(ctl_chal(ws, lid, lane), RLX_AGENT);
  const unsigned rv = __hip_atomic_load(ctl_resp(ws, (lid & ~7) + lane, lid & 7), RLX_AGENT);
  if (cv != rv) __hip_atomic_store(ctl_resp(ws, (lid & ~7) + lane, lid & 7), cv, RLX_AGENT);
}
DI u32x4 ld1(rsrc_t rs, int voff, int soff) { return __builtin_amdgcn_raw_buffer_load_b128(rs, voff, soff, AUX_NT); }
DI void ld8(u32x4 (&r)[8], rsrc_t rs, int voff, int soff, int sa, int sb) {
#pragma unroll
  for (int j = 0; j < 8; ++j) r[j] = ld1(rs, voff, soff + (j & 3) * sa + (j >> 2) * sb);
}

DI void dma1(rsrc_t rs, int voff, int soff, int ldsaddr) {
  asm volatile("s_mov_b32 m0, %0\n\ts_nop 0\n\tbuffer_load_dwordx4 %1, %2, %3 offen " QPOL " lds" :: "s"(ldsaddr), "v"(voff), "s"(rs), "s"(soff) : "memory");
}
DI void dma56(rsrc_t rs, int voff, int soff, int ldsaddr) {
  asm volatile("s_mov_b32 m0, %0\n\ts_mov_b32 exec_hi, 0xffffff\n\tbuffer_load_dwordx4 %1, %2, %3 offen " OPOL " lds\n\ts_mov_b32 exec_hi, -1"
               :: "s"(ldsaddr), "v"(voff), "s"(rs), "s"(soff) : "memory");
}
#define WAITV(N) do { SBAR(); asm volatile("s_waitcnt vmcnt(" #N ")" ::: "memory"); SBAR(); } while (0)
template <int NF> struct PFrag { bf16x4 lo[NF], hi[NF]; bf16x8 b[4]; };
template <int RS> DI int pabase(int f0) {
  const int lane = lane_id(), g = lane >> 4, q = (lane & 15) >> 2, p = lane & 3;
  return (16 * (g >> 1) + 4 * (g & 1) + q) * RS + f0 * 32 + p * 8;
}
DI int pbbase(int bimg) { const int lane = lane_id(), g = lane >> 4, c = lane & 15; return bimg + g * 256 + ((c ^ g) << 4); }
DI int pbaddr(int bb, int ks) { return (bb ^ ((ks & 1) << 6)) + ks * 4096; }
template <int NF, int RS, int K> DI void pread(PFrag<NF>& f, LAS char* smem, int aa, int ba) {
  if constexpr (K < 4) f.b[K] = asbf(lds_r128(smem + ba + K * 1024));
  else if constexpr (K < 4 + 2 * NF) {
    if constexpr ((K & 1) == 0) f.lo[(K - 4) >> 1] = lds_tr(smem + aa + ((K - 4) >> 1) * 32);
    else f.hi[(K - 4) >> 1] = lds_tr(smem + aa + ((K - 4) >> 1) * 32 + 8 * RS);
  }
}
template <int NF, int RS>
DI void pstep(f32x4 (&acc)[NF][4], const PFrag<NF>& cur, PFrag<NF>& nxt, LAS char* smem, int aa, int ba) {
  static_for<NF * 4>([&](auto nc) {
    constexpr int n = decltype(nc)::value, i = n >> 2, rt = n & 3;
    const bf16x8 a = __builtin_shufflevector(cur.lo[i], cur.hi[i], 0, 1, 2, 3, 4, 5, 6, 7);
    acc[i][rt] = MFMA(a, cur.b[rt], acc[i][rt]);
    pread<NF, RS, n>(nxt, smem, aa, ba);
    SBAR();
  });
}
template <int NF> DI u32x4 ppack(const f32x4 (&acc)[NF][4], int i, int rp) {
  auto lo = __builtin_amdgcn_permlane16_swap(pk2(acc[i][2 * rp][0], acc[i][2 * rp][1]), pk2(acc[i][2 * rp + 1][0], acc[i][2 * rp + 1][1]), false, false);
  auto hi = __builtin_amdgcn_permlane16_swap(pk2(acc[i][2 * rp][2], acc[i][2 * rp][3]), pk2(acc[i][2 * rp + 1][2], acc[i][2 * rp + 1][3]), false, false);
  return u32x4{lo[0], hi[0], lo[1], hi[1]};
}
DI void q_handover(const f32x4 (&acc)[8][4], LAS char* smem, int w) {
  LAS char* base = smem + ((w & 1) ? LDS_QP : LDS_QT) + (w >> 1) * 16384 + lane_id() * 16;
#pragma unroll
  for (int i = 0; i < 8; ++i)
#pragma unroll
    for (int rp = 0; rp < 2; ++rp) lds_w128(base + rp * 8192 + i * 1024, ppack<8>(acc, i, rp));
}
template <int NF> DI void publish(const f32x4 (&acc)[NF][4], char* ws, int lid, int f0, bool ok) {
  const int lane = lane_id(), g = lane >> 4, c = lane & 15;
  const rsrc_t rp = mk_rsrc(ws + (size_t)(lid >> 3) * 64 * SLAB, 64 * SLAB);
  const int voff = (((g & 1) * 2 + (c >> 3)) * 8 + (lid & 7)) * SLAB + f0 * 256 + (g >> 1) * 128 + (c & 7) * 16;
#pragma unroll
  for (int i = 0; i < NF; ++i)
#pragma unroll
    for (int r2 = 0; r2 < 2; ++r2) __builtin_amdgcn_raw_buffer_store_b128(ppack<NF>(acc, i, r2), rp, voff, r2 * 32 * SLAB + i * 256, AUX_SC1);
  asm volatile("s_waitcnt vmcnt(0)" ::: "memory");
  for (unsigned spins = 0; !ok && spins < SPIN_CAP; ++spins) {
    if (lane < 8) hs_echo(ws, lid, lane);
    const unsigned cv = __hip_atomic_load(ctl_chal(ws, (lid & ~7) + (lane & 7), lid & 7), RLX_AGENT);
    const unsigned rv = __hip_atomic_load(ctl_resp(ws, lid, lane & 7), RLX_AGENT);
    ok = __all(cv == rv);
    __builtin_amdgcn_s_sleep(8);
  }
  if (lane < 8) __hip_atomic_fetch_add((unsigned*)(ws + CTL_OFF + (size_t)((lid & ~7) + lane) * 512), 1u, RLX_AGENT);
}
constexpr int QRS = 1056, ORS = 928;
DI int qslot(int i) { return i == 0 ? 92160 : (i == 1 ? 126976 : 0); }
DI int oslot(int i) { return i == 0 ? LDS_V : (i == 1 ? LDS_QP : LDS_V + 32768); }

__global__ __launch_bounds__(512, 2) void gqa_fused(const char* __restrict__ xA, const char* __restrict__ kA, const char* __restrict__ vA,
                                                    const char* __restrict__ wqA, const char* __restrict__ woA,
                                                    const char* __restrict__ xB, const char* __restrict__ kB, const char* __restrict__ vB,
                                                    const char* __restrict__ wqB, const char* __restrict__ woB,
                                                    char* __restrict__ out, char* ws) {
  __shared__ __attribute__((aligned(1024))) char smem_[LDS_TOTAL];
  LAS char* smem = (LAS char*)smem_;
  const int wid = __builtin_amdgcn_readfirstlane(threadIdx.x >> 6);
  const int bid = blockIdx.x, blk = (bid >> 2) & 1, kvh = (bid & 3) * 2 + (bid >> 7), bb = (bid >> 3) & 15;
  const int lid = (blk * 16 + bb) * 8 + kvh;
  const int kq0 = bb < 14 ? bb : bb - 14;
#define KQ(S) ((kq0 + (S)) >= 14 ? kq0 + (S) - 14 : kq0 + (S))
#define KO(S) (((S) + OROT * bb) & 15)
  STAMP_DECL;
  STAMP(0); STAMPC(13);
  const rsrc_t rc = mk_rsrc(ws + CTL_OFF + (size_t)(lid & ~7) * 512, 8 * 512);
#define HS_REQ() const unsigned hsc_ = __builtin_amdgcn_raw_buffer_load_b32(rc, (lane_id() & 7) * 512 + 128 + (lid & 7) * 4, 0, AUX_SC1), \
                                hsr_ = __builtin_amdgcn_raw_buffer_load_b32(rc, (lid & 7) * 512 + 256 + (lane_id() & 7) * 4, 0, AUX_SC1)
#define HS_OK() __all(hsc_ == hsr_)

  if (wid >= 4) {
    int lane = lane_id();
    const int h = wid - 4;
    bool hs = h == 0 && lane < 8;
    __builtin_amdgcn_raw_buffer_store_b32(0u, rc, hs && lane == 0 ? (lid & 7) * 512 : OOB, 0, AUX_SC1);
    const unsigned hs_r = __builtin_amdgcn_raw_buffer_load_b32(rc, hs ? (lid & 7) * 512 + 256 + lane * 4 : OOB, 0, AUX_SC1);
    const rsrc_t rwq = mk_rsrc((blk ? wqB : wqA) + kvh * 1024, W_BYTES - kvh * 1024);
    const rsrc_t rwo = mk_rsrc((blk ? woB : woA) + (size_t)kvh * 512 * DM * 2, 512 * DM * 2);
    const rsrc_t rk = mk_rsrc((blk ? kB : kA) + (size_t)(bb * HKV + kvh) * KV_BYTES, KV_BYTES);
    const rsrc_t rv = mk_rsrc((blk ? vB : vA) + (size_t)(bb * HKV + kvh) * KV_BYTES, KV_BYTES);
#define voff l16
#define wqv l16
#define wov l16
    int l16 = lane * 16;
    u32x4 R0[8], R1[8], SA[8], SB[8];
    const int irow = 16 * (h >> 1) + 4 * (h & 1);
#define QDMA(S) do { _Pragma("unroll") for (int j = 0; j < 8; ++j) \
      dma1(rwq, wqv, (KQ(S) * 32 + 8 * h + j) * (NQ * 2), qslot((S) % 3) + (irow + 8 * (j >> 2) + (j & 3)) * QRS); SBAR(); } while (0)
#define ODMA(S) do { { _Pragma("unroll") for (int j = 0; j < 8; ++j) \
      dma56(rwo, wov, (KO(S) * 32 + 16 * (h & 1) + 4 * (h >> 1) + 8 * (j >> 2) + (j & 3)) * (DM * 2), oslot((S) % 3) + (irow + 8 * (j >> 2) + (j & 3)) * ORS); } SBAR(); } while (0)
#define LOADU(SET, RS, TILE) do { ld8(SET, RS, voff, (TILE) * 32768 + h * 1024, 4096, 16384); SBAR(); } while (0)
    QDMA(0); QDMA(1); QDMA(2);
    WAITV(16);
    WG_BARRIER();
    STAMP(1);
    WAITV(8);
#pragma unroll 1
    for (int s = 0; s < 11; ++s) {
      WG_BARRIER();
      QDMA(s + 3);
      WAITV(8);
    }
    WG_BARRIER();
    LOADU(R0, rk, 0);
    WAITV(8);
    WG_BARRIER();
    __builtin_amdgcn_raw_buffer_store_b32(hs_r + 1u, rc, hs ? lane * 512 + 128 + (lid & 7) * 4 : OOB, 0, AUX_SC1);
    LOADU(R1, rk, 1);
    WG_BARRIER();
    STAMP(2);
    lane = lane_id(); l16 = lane * 16; hs = h == 0 && lane < 8;
    const int hi = lane >> 5, ch = lane & 31;
    const int row0 = 2 * h + hi;
    const int kw0 = LDS_K + row0 * 512 + ((ch ^ row0) << 4), kw1 = LDS_K + (row0 + 8) * 512 + ((ch ^ row0 ^ 8) << 4);
    const int vw0 = LDS_V + row0 * 512 + ((ch ^ ((row0 & 3) << 2)) << 4);
#define WRITEK(SET, BUF) do { _Pragma("unroll") for (int i = 0; i < 8; ++i) \
      lds_w128(smem + ((i & 1) ? kw1 : kw0) + (BUF) + (i >> 1) * 8192, SET[i]); SBAR(); } while (0)
#define WRITEV(SET, BUF) do { _Pragma("unroll") for (int i = 0; i < 8; ++i) \
      lds_w128(smem + vw0 + (BUF) + i * 4096, SET[i]); SBAR(); } while (0)
    WRITEK(R0, 0); LOADU(R0, rv, 0);
    WG_BARRIER();
    WRITEK(R1, 32768); LOADU(SA, rk, 2); WRITEV(R0, 0); LOADU(SB, rv, 1);
    STAMP(4);
    WG_BARRIER();
#define STEADY(T) do { const int b0_ = ((T) & 1) << 15; WRITEK(SA, b0_); LOADU(SA, rk, (T) + 3); WRITEV(SB, b0_ ^ 32768); LOADU(SB, rv, (T) + 2); } while (0)
#pragma unroll 1
    for (int t = 0; t < 11; ++t) {
      STEADY(t);
      STAMP_IF(t == 4, 5); STAMP_IF(t == 8, 6);
      WG_BARRIER();
    }
    const unsigned hs_c = __builtin_amdgcn_raw_buffer_load_b32(rc, hs ? (lid & 7) * 512 + 128 + lane * 4 : OOB, 0, AUX_SC1);
    SBAR();
    STEADY(11);
    WG_BARRIER();
    STEADY(12);
    __builtin_amdgcn_raw_buffer_store_b32(hs_c, rc, hs ? lane * 512 + 256 + (lid & 7) * 4 : OOB, 0, AUX_SC1);
    STAMP(7);
    WG_BARRIER();
    WRITEK(SA, 32768); WRITEV(SB, 0); LOADU(SB, rv, 15);
    WG_BARRIER();
    WRITEV(SB, 32768);
    STAMP(8);
    WG_BARRIER();
    ODMA(0); ODMA(1);
    WAITV(0);
    WG_BARRIER();
    STAMP(10);
    ODMA(2);
#pragma unroll 1
    for (int s = 0; s < 13; ++s) {
      WG_BARRIER();
      ODMA(s + 3);
      WAITV(8);
    }
    WG_BARRIER();
    WAITV(0);
    WG_BARRIER();
    WG_BARRIER();
    STAMP(11);
    if (h == 0) {
      const unsigned* cnt = (const unsigned*)(ws + CTL_OFF + (size_t)lid * 512);
      for (unsigned spins = 0; spins < SPIN_CAP; ++spins) {
        if (__hip_atomic_load(cnt, RLX_AGENT) >= 32u) break;
        if ((spins & 63u) == 63u) { const int l = lane_id(); if (l < 8) hs_echo(ws, lid, l); }
        __builtin_amdgcn_s_sleep(2);
      }
    }
#undef QDMA
#undef ODMA
#undef STEADY
#undef LOADU
#undef WRITEK
#undef WRITEV
#undef voff
#undef wqv
#undef wov
  } else {
    {
      const int lane = lane_id();
      const rsrc_t rx = mk_rsrc((blk ? xB : xA) + (size_t)bb * SQ * DM * 2, SQ * DM * 2);
      const int xp = ((wid + (kvh >> 1)) & 3) * 14;
      u32x4 xr[14];
#pragma unroll
      for (int f = 0; f < 14; ++f) xr[f] = ld1(rx, lane * 16, (xp + f) * 1024);
      SBAR();
#pragma unroll
      for (int f = 0; f < 14; ++f) {
        const unsigned idx = (xp + f) * 64 + lane, row = idx / 56u, q = idx - row * 56u;
        lds_w128(smem + LDS_X + (q >> 2) * 4096 + (row >> 4) * 1024 + (q & 3) * 256 + (((row & 15) ^ (q & 7)) << 4), xr[f]);
      }
    }
    WG_BARRIER();
    STAMP(1);
    {
      f32x4 acc[8][4]; PFrag<8> F[2];
#pragma unroll
      for (int i = 0; i < 8; ++i)
#pragma unroll
        for (int rt = 0; rt < 4; ++rt) acc[i][rt] = f32x4{0.f, 0.f, 0.f, 0.f};
      const int ab = pabase<QRS>(wid * 8), bbs = pbbase(LDS_X);
      { const int aa = ab + qslot(0), ba = pbaddr(bbs, kq0); static_for<20>([&](auto kc) { pread<8, QRS, decltype(kc)::value>(F[0], smem, aa, ba); }); }
      int sl = 1;
#pragma unroll 1
      for (int s = 0; s < 14; s += 2) {
        WG_BARRIER();
        pstep<8, QRS>(acc, F[0], F[1], smem, ab + qslot(sl), pbaddr(bbs, KQ(s + 1)));
        sl = sl == 2 ? 0 : sl + 1;
        WG_BARRIER();
        pstep<8, QRS>(acc, F[1], F[0], smem, ab + qslot(sl), pbaddr(bbs, KQ(s + 2)));
        sl = sl == 2 ? 0 : sl + 1;
      }
      q_handover(acc, smem, wid);
    }
    STAMP(2);
    {
      const int w = wid, hh = w >> 1, rh = w & 1;
      const int lane = lane_id(), h = lane >> 5, r = lane & 31;
      f32x16 o[8];
#pragma unroll
      for (int dt = 0; dt < 8; ++dt)
#pragma unroll
        for (int i = 0; i < 16; ++i) o[dt][i] = 0.f;
      float mc = 0.f, lsum = 0.f;
      f32x16 s0, s1;
      u32x4 p0[2], p1[2];
      u32x4 qown[8];
      int kb = LDS_K + r * 512 + ((h ^ (r & 15)) << 4);
      int vb = LDS_V + (4 * h + ((lane & 15) >> 2)) * 512 + (((lane & 15) >> 2) << 6) + ((lane >> 4) & 1) * 32 + (lane & 3) * 8;
      LAS char* qp = smem + LDS_QP + w * 8192 + lane * 16;
      constexpr int PD = 4, RN = 6;
      bf16x4 vl[RN], vh[RN]; bf16x8 kf[RN], qf[2];
      float t0, t1, t2, t3, t4, mx;
#define FETCH(N) do { constexpr int n_ = (N), blk_ = n_ >> 4, j_ = n_ & 15; \
        if (blk_ & 1) kf[n_ % RN] = asbf(lds_r128(smem + (kb ^ ((j_ >> 1) << 5)) + (j_ & 1) * 256 + (blk_ >> 1) * 16384)); \
        else { LAS char* a_ = smem + (vb ^ ((j_ & 3) << 6)) + ((j_ >> 2) & 1) * 256 + (blk_ + (j_ >> 3)) * 8192; vl[n_ % RN] = lds_tr(a_); vh[n_ % RN] = lds_tr(a_ + 4096); } } while (0)
#define FETCHQ(J) qf[((J) >> 1) & 1] = asbf(lds_r128(qp + ((J) >> 1) * 1024))
#define JOB_AB(X, j) do { \
        if ((j) < 4) { _Pragma("unroll") for (int i_ = 0; i_ < 4; ++i_) X[4 * (j) + i_] = fmaf(X[4 * (j) + i_], SCL, -mc); } \
        else if ((j) == 4) { t0 = max3(X[0], X[1], X[2]); t1 = max3(X[3], X[4], X[5]); t2 = max3(X[6], X[7], X[8]); } \
        else if ((j) == 5) { t3 = max3(X[9], X[10], X[11]); t4 = max3(X[12], X[13], X[14]); t0 = max3(t0, t1, X[15]); } \
        else if ((j) == 6) { t0 = fmaxf(t0, max3(t2, t3, t4)); } \
        else if ((j) == 7) { mx = hmax(t0); } } while (0)
#define JOB_EXP(X, P, j) do { X[j] = __builtin_amdgcn_exp2f(X[j]); \
        if ((j) > 0) lsum += X[(j) - 1]; \
        if ((j) >= 2 && !((j) & 1)) P[((j) - 2) >> 3][(((j) - 2) >> 1) & 3] = pk2(X[(j) - 2], X[(j) - 1]); \
        if ((j) == 15) { lsum += X[15]; P[1][3] = pk2(X[14], X[15]); } } while (0)
#define RESCALE(X) do { if (__any(mx > THR)) { const float d_ = fmaxf(mx, 0.f), al_ = __builtin_amdgcn_exp2f(-d_); mc += d_; lsum *= al_; \
          _Pragma("unroll") for (int i_ = 0; i_ < 16; ++i_) X[i_] -= d_; \
          _Pragma("unroll") for (int dt = 0; dt < 8; ++dt) o[dt] *= al_; } SBAR(); } while (0)
#define FETCH_AHEAD(B, j) switch (j) { \
            case 0: FETCH((B) * 16 + 0 + PD); break; case 1: FETCH((B) * 16 + 1 + PD); break; case 2: FETCH((B) * 16 + 2 + PD); break; case 3: FETCH((B) * 16 + 3 + PD); break; \
            case 4: FETCH((B) * 16 + 4 + PD); break; case 5: FETCH((B) * 16 + 5 + PD); break; case 6: FETCH((B) * 16 + 6 + PD); break; case 7: FETCH((B) * 16 + 7 + PD); break; \
            case 8: FETCH((B) * 16 + 8 + PD); break; case 9: FETCH((B) * 16 + 9 + PD); break; case 10: FETCH((B) * 16 + 10 + PD); break; case 11: FETCH((B) * 16 + 11 + PD); break; \
            case 12: FETCH(((B) * 16 + 12 + PD) & 63); break; case 13: FETCH(((B) * 16 + 13 + PD) & 63); break; case 14: FETCH(((B) * 16 + 14 + PD) & 63); break; default: FETCH(((B) * 16 + 15 + PD) & 63); break; }
#define PVBLOCK(B, P, X, LAST) do { _Pragma("unroll") for (int j = 0; j < 16; ++j) { \
          const bf16x8 a_ = __builtin_shufflevector(vl[((B) * 16 + j) % RN], vh[((B) * 16 + j) % RN], 0, 1, 2, 3, 4, 5, 6, 7); \
          o[j & 7] = MFMA32(a_, asbf(P[j >> 3]), o[j & 7]); \
          if (j + PD < 16 || !(LAST)) { FETCH_AHEAD(B, j) } \
          if (!(LAST) && j == 14) FETCHQ(1); \
          if ((LAST) != 2) JOB_AB(X, j); \
          if (!(LAST)) PACE(); \
          SBAR(); } } while (0)
#define QKBLOCK(B, S, NEXT, JOB, X, P) do { _Pragma("unroll") for (int j = 0; j < 16; ++j) { \
          const bf16x8 q_ = (j & 1) ? qf[(j >> 1) & 1] : asbf(qown[j >> 1]); \
          if (j == 0) { f32x16 z_; _Pragma("unroll") for (int i_ = 0; i_ < 16; ++i_) z_[i_] = 0.f; S = MFMA32(kf[((B) * 16 + j) % RN], q_, z_); } \
          else S = MFMA32(kf[((B) * 16 + j) % RN], q_, S); \
          if (j + PD < 16 || (NEXT)) { FETCH_AHEAD(B, j) } \
          if (!(j & 1) && j + 3 < 16) FETCHQ(j + 3); \
          if (JOB) JOB_EXP(X, P, j); \
          PACE(); \
          SBAR(); } } while (0)
#define FETCH4(N) do { FETCH(N); FETCH((N) + 1); FETCH((N) + 2); FETCH((N) + 3); } while (0)
      static_assert(PD == 4, "FETCH4");
      int tp = -1; (void)tp;
      WG_BARRIER();
      STAMP(3);
#pragma unroll
      for (int f = 0; f < 8; ++f) qown[f] = lds_r128(smem + LDS_QT + w * 8192 + f * 1024 + lane * 16);
      FETCH4(16); FETCHQ(1); SBAR();
      QKBLOCK(1, s0, 0, 0, s0, p0);
      FETCH4(48); FETCHQ(1); SBAR();
#pragma unroll
      for (int j = 0; j < 8; ++j) JOB_AB(s0, j);
      mc = mx;
#pragma unroll
      for (int i = 0; i < 16; ++i) s0[i] -= mx;
      mx = 0.f;
      SBAR();
#pragma unroll 1
      for (int t = 0; t < 15; ++t) {
        tp = t;
        RESCALE(s0);
        QKBLOCK(3, s1, 0, 1, s0, p0);
        kb ^= 32768;
        WG_BARRIER();
        STAMP_IF(t == 0, 4); STAMP_IF(t == 4, 5); STAMP_IF(t == 8, 6); STAMP_IF(t == 12, 7);
        FETCH4(0); SBAR();
        PVBLOCK(0, p0, s1, 0);
        RESCALE(s1);
        QKBLOCK(1, s0, 1, 1, s1, p1);
        PVBLOCK(2, p1, s0, 0);
        vb ^= 32768;
      }
      tp = 15;
      RESCALE(s0);
      QKBLOCK(3, s1, 0, 1, s0, p0);
#pragma unroll
      for (int j = 0; j < 8; ++j) JOB_AB(s1, j);
      if (__any(mx > THR)) {
        const float d_ = fmaxf(mx, 0.f), al_ = __builtin_amdgcn_exp2f(-d_);
        mc += d_; lsum *= al_;
#pragma unroll
        for (int i = 0; i < 16; ++i) s1[i] -= d_;
#pragma unroll
        for (int dt = 0; dt < 8; ++dt) o[dt] *= al_;
#pragma unroll
        for (int k = 0; k < 2; ++k)
#pragma unroll
          for (int i = 0; i < 4; ++i) p0[k][i] = pk2(__uint_as_float(p0[k][i] << 16) * al_, __uint_as_float(p0[k][i] & 0xffff0000u) * al_);
      }
      SBAR();
#pragma unroll
      for (int j = 0; j < 16; ++j) JOB_EXP(s1, p1, j);
      const float iv = 1.f / hsum(lsum);
      WG_BARRIER();
      STAMP(8);
      FETCH4(0); SBAR();
      PVBLOCK(0, p0, s1, 2);
      FETCH4(32); SBAR();
      PVBLOCK(2, p1, s0, 2);
#undef FETCH
#undef FETCHQ
#undef FETCH4
#undef FETCH_AHEAD
#undef JOB_AB
#undef JOB_EXP
#undef RESCALE
#undef PVBLOCK
#undef QKBLOCK
      STAMP(9);
      const int lane2 = lane_id(), h2 = lane2 >> 5, r2 = lane2 & 31;
      LAS char* ow = smem + LDS_K + (hh * 8 * 4 + rh * 2 + (r2 >> 4)) * 1024 + 2 * h2 * 256;
#pragma unroll
      for (int dt = 0; dt < 8; ++dt)
#pragma unroll
        for (int pp = 0; pp < 2; ++pp)
          lds_w128(ow + dt * 4096 + pp * 256 + (((r2 & 15) ^ (2 * h2 + pp) ^ ((dt & 1) * 4)) << 4),
                   u32x4{pk2(o[dt][8 * pp] * iv, o[dt][8 * pp + 1] * iv), pk2(o[dt][8 * pp + 2] * iv, o[dt][8 * pp + 3] * iv),
                         pk2(o[dt][8 * pp + 4] * iv, o[dt][8 * pp + 5] * iv), pk2(o[dt][8 * pp + 6] * iv, o[dt][8 * pp + 7] * iv)});
    }
    WG_BARRIER();
    STAMP(10);
    {
      HS_REQ();
      f32x4 acc[7][4]; PFrag<7> F[2];
#pragma unroll
      for (int i = 0; i < 7; ++i)
#pragma unroll
        for (int rt = 0; rt < 4; ++rt) acc[i][rt] = f32x4{0.f, 0.f, 0.f, 0.f};
      const int ab = pabase<ORS>(wid * 7), bbs = pbbase(LDS_K);
      { const int aa = ab + oslot(0), ba = pbaddr(bbs, KO(0)); static_for<18>([&](auto kc) { pread<7, ORS, decltype(kc)::value>(F[0], smem, aa, ba); }); }
      int sl = 1;
#pragma unroll 1
      for (int s = 0; s < 16; s += 2) {
        WG_BARRIER();
        pstep<7, ORS>(acc, F[0], F[1], smem, ab + oslot(sl), pbaddr(bbs, KO(s + 1)));
        sl = sl == 2 ? 0 : sl + 1;
        WG_BARRIER();
        pstep<7, ORS>(acc, F[1], F[0], smem, ab + oslot(sl), pbaddr(bbs, KO(s + 2)));
        sl = sl == 2 ? 0 : sl + 1;
      }
      STAMP(11);
      publish<7>(acc, ws, lid, wid * 7, HS_OK());
    }
  }
  WG_BARRIER();
  STAMP(15);
  if (wid < 7) {
    const int lane = lane_id();
    const rsrc_t rp = mk_rsrc(ws + ((size_t)(lid >> 3) * 64 + kvh * 8) * SLAB, 8 * SLAB);
    u32x4 v[8];
#pragma unroll
    for (int s = 0; s < 8; ++s) v[s] = __builtin_amdgcn_raw_buffer_load_b128(rp, wid * 1024 + lane * 16, s * SLAB, AUX_SC1);
    float a[8];
#pragma unroll
    for (int k = 0; k < 4; ++k) { a[2 * k] = __uint_as_float(v[0][k] << 16); a[2 * k + 1] = __uint_as_float(v[0][k] & 0xffff0000u); }
#pragma unroll
    for (int s = 1; s < 8; ++s)
#pragma unroll
      for (int k = 0; k < 4; ++k) { a[2 * k] += __uint_as_float(v[s][k] << 16); a[2 * k + 1] += __uint_as_float(v[s][k] & 0xffff0000u); }
    char* o = out + ((size_t)(bb * SQ + kvh * 8 + (lane & 7)) * (2 * DM) + blk * DM + wid * 64 + (lane >> 3) * 8) * 2;
    *(u32x4*)o = u32x4{pk2(a[0], a[1]), pk2(a[2], a[3]), pk2(a[4], a[5]), pk2(a[6], a[7])};
  }
  STAMP(12); STAMPC(14); STAMP_OUT();
}
}

extern "C" void kernel_launch(void* const* d_in, const int* in_sizes, int n_in, void* d_out, int out_size, void* d_ws, size_t ws_size,
                              hipStream_t stream) {
  (void)in_sizes; (void)n_in; (void)out_size; (void)ws_size;
  gqa_fused<<<256, 512, 0, stream>>>((const char*)d_in[0], (const char*)d_in[1], (const char*)d_in[2], (const char*)d_in[3], (const char*)d_in[4],
                                     (const char*)d_in[5], (const char*)d_in[6], (const char*)d_in[7], (const char*)d_in[8], (const char*)d_in[9],
                                     (char*)d_out, (char*)d_ws);
}
```

```cpp
#include <hip/hip_runtime.h>
#include <hip/hip_bf16.h>
#include <utility>
#include <type_traits>

#define LAS __attribute__((address_space(3)))
#define DI __device__ __forceinline__
typedef __attribute__((ext_vector_type(8))) __bf16 bf16x8;
typedef __attribute__((ext_vector_type(4))) __bf16 bf16x4;
typedef __attribute__((ext_vector_type(2))) __bf16 bf16x2;
typedef __attribute__((ext_vector_type(16))) float f32x16;
typedef __attribute__((ext_vector_type(4))) float f32x4;
typedef __attribute__((ext_vector_type(2))) float f32x2;
typedef __attribute__((ext_vector_type(4))) unsigned u32x4;

namespace {
constexpr int SQ = 64, DM = 448, HKV = 8, SK = 1024, DH = 256;
constexpr int NQ = 4096;
constexpr int KV_BYTES = SK * DH * 2;
constexpr int W_BYTES = DM * NQ * 2;
constexpr int LDS_K = 0, LDS_V = 65536, LDS_QP = 131072, LDS_TOTAL = 163840;
constexpr int LDS_X = 34816, LDS_QT = LDS_V + 32768;
constexpr int XRS = 912;
constexpr int SLAB = 7168;
constexpr size_t CTL_OFF = (size_t)16 << 20;
constexpr float SCL = 0.0625f * 1.4426950408889634f;
constexpr float THR = 8.f;
constexpr int AUX_NT = 2, AUX_SC1 = 16;
#define AUX_W 2
#define OPOL ""
#define QPOL "nt"
#define WARM_AUX 2
#define OROT 1
constexpr int OOB = 0x40000000;
constexpr unsigned SPIN_CAP = 1u << 21;

#define SBAR() __builtin_amdgcn_sched_barrier(0)
#define WG_BARRIER() do { SBAR(); asm volatile("s_waitcnt lgkmcnt(0)\n\ts_barrier" ::: "memory"); SBAR(); } while (0)
#define STAMP(idx) do {} while (0)
#define STAMP_IF(c, idx) do {} while (0)
#define STAMPC(idx) do {} while (0)
#define STAMP_DECL do {} while (0)
#define STAMP_OUT() do {} while (0)
#define PACE(PN) do { if ((PN) >= 0) asm volatile("s_nop %0" :: "i"((PN) < 0 ? 0 : (PN))); } while (0)
#define AP_TH 328
#define AP_P 7
#define PACEN -1
#define NAP() do {} while (0)
#define MFMA(a, b, c) __builtin_amdgcn_mfma_f32_16x16x32_bf16((a), (b), (c), 0, 0, 0)
#define MFMA32(a, b, c) __builtin_amdgcn_mfma_f32_32x32x16_bf16((a), (b), (c), 0, 0, 0)
#define RLX_AGENT __ATOMIC_RELAXED, __HIP_MEMORY_SCOPE_AGENT

DI unsigned pk2(float a, float b) { f32x2 f = {a, b}; bf16x2 r = __builtin_convertvector(f, bf16x2); return __builtin_bit_cast(unsigned, r); }
DI bf16x8 asbf(u32x4 v) { return __builtin_bit_cast(bf16x8, v); }
DI u32x4 lds_r128(LAS char* p) { return *(LAS u32x4*)p; }
DI void lds_w128(LAS char* p, u32x4 v) { *(LAS u32x4*)p = v; }
DI bf16x4 lds_tr(LAS char* p) { return __builtin_amdgcn_ds_read_tr16_b64_v4bf16((LAS bf16x4*)p); }
DI unsigned perm2(int e, unsigned hi, unsigned lo) { return __builtin_amdgcn_perm(hi, lo, e ? 0x07060302u : 0x05040100u); }
DI float max3(float a, float b, float c) { return fmaxf(fmaxf(a, b), c); }
DI float hmax(float v) {
  auto b = __builtin_amdgcn_permlane32_swap(__float_as_uint(v), __float_as_uint(v), false, false);
  return fmaxf(__uint_as_float(b[0]), __uint_as_float(b[1]));
}
DI float hsum(float v) {
  auto b = __builtin_amdgcn_permlane32_swap(__float_as_uint(v), __float_as_uint(v), false, false);
  return __uint_as_float(b[0]) + __uint_as_float(b[1]);
}
DI int lane_id() {
  int l; asm volatile("v_mbcnt_lo_u32_b32 %0, -1, 0\n\tv_mbcnt_hi_u32_b32 %0, -1, %0" : "=v"(l)); return l; }
typedef __amdgpu_buffer_rsrc_t rsrc_t;
DI rsrc_t mk_rsrc(const void* p, int bytes) { return __builtin_amdgcn_make_buffer_rsrc((void*)p, 0, bytes, 0x00020000); }
template <class F, int... I> DI void static_for_(F&& f, std::integer_sequence<int, I...>) { (f(std::integral_constant<int, I>{}), ...); }
template <int N, class F> DI void static_for(F&& f) { static_for_(f, std::make_integer_sequence<int, N>{}); }

DI unsigned* ctl_chal(char* ws, int wg, int slot) { return (unsigned*)(ws + CTL_OFF + (size_t)wg * 512 + 128 + slot * 4); }
DI unsigned* ctl_resp(char* ws, int wg, int slot) { return (unsigned*)(ws + CTL_OFF + (size_t)wg * 512 + 256 + slot * 4); }
DI void hs_echo(char* ws, int lid, int lane) {
  const unsigned cv = __hip_atomic_load(ctl_chal(ws, lid, lane), RLX_AGENT);
  const unsigned rv = __hip_atomic_load(ctl_resp(ws, (lid & ~7) + lane, lid & 7), RLX_AGENT);
  if (cv != rv) __hip_atomic_store(ctl_resp(ws, (lid & ~7) + lane, lid & 7), cv, RLX_AGENT);
}
DI u32x4 ld1(rsrc_t rs, int voff, int soff) { return __builtin_amdgcn_raw_buffer_load_b128(rs, voff, soff, AUX_NT); }
DI void ld8(u32x4 (&r)[8], rsrc_t rs, int voff, int soff, int sa, int sb) {
#pragma unroll
  for (int j = 0; j < 8; ++j) r[j] = ld1(rs, voff, soff + (j & 3) * sa + (j >> 2) * sb);
}

DI void dma1(rsrc_t rs, int voff, int soff, int ldsaddr) {
  asm volatile("s_mov_b32 m0, %0\n\ts_nop 0\n\tbuffer_load_dwordx4 %1, %2, %3 offen " QPOL " lds" :: "s"(ldsaddr), "v"(voff), "s"(rs), "s"(soff) : "memory");
}
DI void dma56nt(rsrc_t rs, int voff, int soff, int ldsaddr) {
  asm volatile("s_mov_b32 m0, %0\n\ts_mov_b32 exec_hi, 0xffffff\n\tbuffer_load_dwordx4 %1, %2, %3 offen nt lds\n\ts_mov_b32 exec_hi, -1"
               :: "s"(ldsaddr), "v"(voff), "s"(rs), "s"(soff) : "memory");
}
DI void dma56(rsrc_t rs, int voff, int soff, int ldsaddr) {
  asm volatile("s_mov_b32 m0, %0\n\ts_mov_b32 exec_hi, 0xffffff\n\tbuffer_load_dwordx4 %1, %2, %3 offen " OPOL " lds\n\ts_mov_b32 exec_hi, -1"
               :: "s"(ldsaddr), "v"(voff), "s"(rs), "s"(soff) : "memory");
}
#define WAITV(N) do { SBAR(); asm volatile("s_waitcnt vmcnt(" #N ")" ::: "memory"); SBAR(); } while (0)
template <int NF> struct PFrag { bf16x4 lo[NF], hi[NF]; bf16x8 b[4]; };
template <int RS> DI int pabase(int f0) {
  const int lane = lane_id(), g = lane >> 4, q = (lane & 15) >> 2, p = lane & 3;
  return (16 * (g >> 1) + 4 * (g & 1) + q) * RS + f0 * 32 + p * 8;
}
DI int pbbase(int bimg) { const int lane = lane_id(), g = lane >> 4, c = lane & 15; return bimg + g * 256 + ((c ^ g) << 4); }
DI int pbaddr(int bb, int ks) { return (bb ^ ((ks & 1) << 6)) + ks * 4096; }
template <int NF, int RS, int K> DI void pread(PFrag<NF>& f, LAS char* smem, int aa, int ba) {
  if constexpr (K < 4) f.b[K] = asbf(lds_r128(smem + ba + K * 1024));
  else if constexpr (K < 4 + 2 * NF) {
    if constexpr ((K & 1) == 0) f.lo[(K - 4) >> 1] = lds_tr(smem + aa + ((K - 4) >> 1) * 32);
    else f.hi[(K - 4) >> 1] = lds_tr(smem + aa + ((K - 4) >> 1) * 32 + 8 * RS);
  }
}
template <int NF, int RS>
DI void pstep(f32x4 (&acc)[NF][4], const PFrag<NF>& cur, PFrag<NF>& nxt, LAS char* smem, int aa, int ba) {
  static_for<NF * 4>([&](auto nc) {
    constexpr int n = decltype(nc)::value, i = n >> 2, rt = n & 3;
    const bf16x8 a = __builtin_shufflevector(cur.lo[i], cur.hi[i], 0, 1, 2, 3, 4, 5, 6, 7);
    acc[i][rt] = MFMA(a, cur.b[rt], acc[i][rt]);
    pread<NF, RS, n>(nxt, smem, aa, ba);
    SBAR();
  });
}
template <int NF> DI u32x4 ppack(const f32x4 (&acc)[NF][4], int i, int rp) {
  auto lo = __builtin_amdgcn_permlane16_swap(pk2(acc[i][2 * rp][0], acc[i][2 * rp][1]), pk2(acc[i][2 * rp + 1][0], acc[i][2 * rp + 1][1]), false, false);
  auto hi = __builtin_amdgcn_permlane16_swap(pk2(acc[i][2 * rp][2], acc[i][2 * rp][3]), pk2(acc[i][2 * rp + 1][2], acc[i][2 * rp + 1][3]), false, false);
  return u32x4{lo[0], hi[0], lo[1], hi[1]};
}
DI void q_handover(const f32x4 (&acc)[8][4], LAS char* smem, int w) {
  LAS char* base = smem + ((w & 1) ? LDS_QP : LDS_QT) + (w >> 1) * 16384 + lane_id() * 16;
#pragma unroll
  for (int i = 0; i < 8; ++i)
#pragma unroll
    for (int rp = 0; rp < 2; ++rp) lds_w128(base + rp * 8192 + i * 1024, ppack<8>(acc, i, rp));
}
template <int NF> DI void publish(const f32x4 (&acc)[NF][4], char* ws, int lid, int f0, bool ok) {
  const int lane = lane_id(), g = lane >> 4, c = lane & 15;
  const rsrc_t rp = mk_rsrc(ws + (size_t)(lid >> 3) * 64 * SLAB, 64 * SLAB);
  const int voff = (((g & 1) * 2 + (c >> 3)) * 8 + (lid & 7)) * SLAB + f0 * 256 + (g >> 1) * 128 + (c & 7) * 16;
#pragma unroll
  for (int i = 0; i < NF; ++i)
#pragma unroll
    for (int r2 = 0; r2 < 2; ++r2) __builtin_amdgcn_raw_buffer_store_b128(ppack<NF>(acc, i, r2), rp, voff, r2 * 32 * SLAB + i * 256, AUX_SC1);
  asm volatile("s_waitcnt vmcnt(0)" ::: "memory");
  for (unsigned spins = 0; !ok && spins < SPIN_CAP; ++spins) {
    if (lane < 8) hs_echo(ws, lid, lane);
    const unsigned cv = __hip_atomic_load(ctl_chal(ws, (lid & ~7) + (lane & 7), lid & 7), RLX_AGENT);
    const unsigned rv = __hip_atomic_load(ctl_resp(ws, lid, lane & 7), RLX_AGENT);
    ok = __all(cv == rv);
    __builtin_amdgcn_s_sleep(8);
  }
  if (lane < 8) __hip_atomic_fetch_add((unsigned*)(ws + CTL_OFF + (size_t)((lid & ~7) + lane) * 512), 1u, RLX_AGENT);
}
constexpr int QRS = 1056, ORS = 928;
DI int qslot(int i) { return i == 0 ? 92160 : (i == 1 ? 126976 : 0); }
DI int oslot(int i) { return i == 0 ? LDS_V : (i == 1 ? LDS_QP : LDS_V + 32768); }

__global__ __launch_bounds__(512, 2) void gqa_fused(const char* __restrict__ xA, const char* __restrict__ kA, const char* __restrict__ vA,
                                                    const char* __restrict__ wqA, const char* __restrict__ woA,
                                                    const char* __restrict__ xB, const char* __restrict__ kB, const char* __restrict__ vB,
                                                    const char* __restrict__ wqB, const char* __restrict__ woB,
                                                    char* __restrict__ out, char* ws) {
  __shared__ __attribute__((aligned(1024))) char smem_[LDS_TOTAL];
  LAS char* smem = (LAS char*)smem_;
  const int wid = __builtin_amdgcn_readfirstlane(threadIdx.x >> 6);
  const int bid = blockIdx.x, blk = (bid >> 2) & 1, kvh = (bid & 3) * 2 + (bid >> 7), bb = (bid >> 3) & 15;
  const int lid = (blk * 16 + bb) * 8 + kvh;
  const int kq0 = bb < 14 ? bb : bb - 14;
#define KQ(S) ((kq0 + (S)) >= 14 ? kq0 + (S) - 14 : kq0 + (S))
#define KO(S) (((S) + OROT * bb) & 15)
  STAMP_DECL;
  STAMP(0); STAMPC(13);
  const rsrc_t rc = mk_rsrc(ws + CTL_OFF + (size_t)(lid & ~7) * 512, 8 * 512);
#define HS_REQ() const unsigned hsc_ = __builtin_amdgcn_raw_buffer_load_b32(rc, (lane_id() & 7) * 512 + 128 + (lid & 7) * 4, 0, AUX_SC1), \
                                hsr_ = __builtin_amdgcn_raw_buffer_load_b32(rc, (lid & 7) * 512 + 256 + (lane_id() & 7) * 4, 0, AUX_SC1)
#define HS_OK() __all(hsc_ == hsr_)

  if (wid >= 4) {
    int lane = lane_id();
    const int h = wid - 4;
    bool hs = h == 0 && lane < 8;
    __builtin_amdgcn_raw_buffer_store_b32(0u, rc, hs && lane == 0 ? (lid & 7) * 512 : OOB, 0, AUX_SC1);
    const unsigned hs_r = __builtin_amdgcn_raw_buffer_load_b32(rc, hs ? (lid & 7) * 512 + 256 + lane * 4 : OOB, 0, AUX_SC1);
    const rsrc_t rwq = mk_rsrc((blk ? wqB : wqA) + kvh * 1024, W_BYTES - kvh * 1024);
    const rsrc_t rwo = mk_rsrc((blk ? woB : woA) + (size_t)kvh * 512 * DM * 2, 512 * DM * 2);
    const rsrc_t rk = mk_rsrc((blk ? kB : kA) + (size_t)(bb * HKV + kvh) * KV_BYTES, KV_BYTES);
    const rsrc_t rv = mk_rsrc((blk ? vB : vA) + (size_t)(bb * HKV + kvh) * KV_BYTES, KV_BYTES);
#define voff l16
#define wqv l16
#define wov l16
    int l16 = lane * 16;
    u32x4 R0[8], R1[8], SA[8], SB[8];
    const int irow = 16 * (h >> 1) + 4 * (h & 1);
#define QDMA(S) do { _Pragma("unroll") for (int j = 0; j < 8; ++j) \
      dma1(rwq, wqv, (KQ(S) * 32 + 8 * h + j) * (NQ * 2), qslot((S) % 3) + (irow + 8 * (j >> 2) + (j & 3)) * QRS); SBAR(); } while (0)
#define ODMA(S) ODMAX(dma56, S)
#define ODMAX(FN, S) do { { _Pragma("unroll") for (int j = 0; j < 8; ++j) \
      FN(rwo, wov, (KO(S) * 32 + 16 * (h & 1) + 4 * (h >> 1) + 8 * (j >> 2) + (j & 3)) * (DM * 2), oslot((S) % 3) + (irow + 8 * (j >> 2) + (j & 3)) * ORS); } SBAR(); } while (0)
#define LOADU(SET, RS, TILE) do { ld8(SET, RS, voff, (TILE) * 32768 + h * 1024, 4096, 16384); SBAR(); } while (0)
    QDMA(0); QDMA(1); QDMA(2);
    WAITV(16);
    WG_BARRIER();
    STAMP(1);
    WAITV(8);
#pragma unroll 1
    for (int s = 0; s < 11; ++s) {
      WG_BARRIER();
      QDMA(s + 3);
      WAITV(8);
    }
    WG_BARRIER();
    LOADU(R0, rk, 0);
    WAITV(8);
    WG_BARRIER();
    __builtin_amdgcn_raw_buffer_store_b32(hs_r + 1u, rc, hs ? lane * 512 + 128 + (lid & 7) * 4 : OOB, 0, AUX_SC1);
    LOADU(R1, rk, 1);
    WG_BARRIER();
    STAMP(2);
    lane = lane_id(); l16 = lane * 16; hs = h == 0 && lane < 8;
    const int hi = lane >> 5, ch = lane & 31;
    const int row0 = 2 * h + hi;
    const int kw0 = LDS_K + row0 * 512 + ((ch ^ row0) << 4), kw1 = LDS_K + (row0 + 8) * 512 + ((ch ^ row0 ^ 8) << 4);
    const int vw0 = LDS_V + row0 * 512 + ((ch ^ ((row0 & 3) << 2)) << 4);
#define WRITEK(SET, BUF) do { _Pragma("unroll") for (int i = 0; i < 8; ++i) \
      lds_w128(smem + ((i & 1) ? kw1 : kw0) + (BUF) + (i >> 1) * 8192, SET[i]); SBAR(); } while (0)
#define WRITEV(SET, BUF) do { _Pragma("unroll") for (int i = 0; i < 8; ++i) \
      lds_w128(smem + vw0 + (BUF) + i * 4096, SET[i]); SBAR(); } while (0)
    WRITEK(R0, 0); LOADU(R0, rv, 0);
    WG_BARRIER();
    WRITEK(R1, 32768); LOADU(SA, rk, 2); WRITEV(R0, 0); LOADU(SB, rv, 1);
    STAMP(4);
    WG_BARRIER();
#define HOLD(U) do {} while (0)
#define STEADY(T) do { const int b0_ = ((T) & 1) << 15; WRITEK(SA, b0_); HOLD(2 * (T)); LOADU(SA, rk, (T) + 3); WRITEV(SB, b0_ ^ 32768); HOLD(2 * (T) + 1); LOADU(SB, rv, (T) + 2); } while (0)
#pragma unroll 1
    for (int t = 0; t < 11; ++t) {
      STEADY(t);
      STAMP_IF(t == 4, 5); STAMP_IF(t == 8, 6);
      WG_BARRIER();
    }
    const unsigned hs_c = __builtin_amdgcn_raw_buffer_load_b32(rc, hs ? (lid & 7) * 512 + 128 + lane * 4 : OOB, 0, AUX_SC1);
    SBAR();
    STEADY(11);
    WG_BARRIER();
    STEADY(12);
    __builtin_amdgcn_raw_buffer_store_b32(hs_c, rc, hs ? lane * 512 + 256 + (lid & 7) * 4 : OOB, 0, AUX_SC1);
    STAMP(7);
    WG_BARRIER();
    WRITEK(SA, 32768); WRITEV(SB, 0); LOADU(SB, rv, 15);
    WG_BARRIER();
    WRITEV(SB, 32768);
    STAMP(8);
    WG_BARRIER();
    ODMA(0); ODMA(1);
    WAITV(0);
    WG_BARRIER();
    STAMP(10);
    ODMA(2);
#pragma unroll 1
    for (int s = 0; s < 13; ++s) {
      WG_BARRIER();
      ODMA(s + 3);
      WAITV(8);
    }
    WG_BARRIER();
    WAITV(0);
    WG_BARRIER();
    WG_BARRIER();
    STAMP(11);
    if (h == 0) {
      const unsigned* cnt = (const unsigned*)(ws + CTL_OFF + (size_t)lid * 512);
      for (unsigned spins = 0; spins < SPIN_CAP; ++spins) {
        if (__hip_atomic_load(cnt, RLX_AGENT) >= 32u) break;
        if ((spins & 63u) == 63u) { const int l = lane_id(); if (l < 8) hs_echo(ws, lid, l); }
        __builtin_amdgcn_s_sleep(2);
      }
    }
#undef QDMA
#undef ODMA
#undef STEADY
#undef LOADU
#undef WRITEK
#undef WRITEV
#undef voff
#undef wqv
#undef wov
  } else {
    const unsigned c0_ = (unsigned)__builtin_amdgcn_s_memtime(), r0_ = (unsigned)__builtin_amdgcn_s_memrealtime();
    {
      const int lane = lane_id();
      const rsrc_t rx = mk_rsrc((blk ? xB : xA) + (size_t)bb * SQ * DM * 2, SQ * DM * 2);
      const int xp = ((wid + (kvh >> 1)) & 3) * 14;
      u32x4 xr[14];
#pragma unroll
      for (int f = 0; f < 14; ++f) xr[f] = ld1(rx, lane * 16, (xp + f) * 1024);
      SBAR();
#pragma unroll
      for (int f = 0; f < 14; ++f) {
        const unsigned idx = (xp + f) * 64 + lane, row = idx / 56u, q = idx - row * 56u;
        lds_w128(smem + LDS_X + (q >> 2) * 4096 + (row >> 4) * 1024 + (q & 3) * 256 + (((row & 15) ^ (q & 7)) << 4), xr[f]);
      }
    }
    WG_BARRIER();
    const int fastclk = __builtin_amdgcn_readfirstlane((int)(((unsigned)__builtin_amdgcn_s_memtime() - c0_) * 16u - ((unsigned)__builtin_amdgcn_s_memrealtime() - r0_) * (unsigned)AP_TH));
    __builtin_amdgcn_s_waitcnt(0xC07F); SBAR();
    STAMP(1);
    {
      f32x4 acc[8][4]; PFrag<8> F[2];
#pragma unroll
      for (int i = 0; i < 8; ++i)
#pragma unroll
        for (int rt = 0; rt < 4; ++rt) acc[i][rt] = f32x4{0.f, 0.f, 0.f, 0.f};
      const int ab = pabase<QRS>(wid * 8), bbs = pbbase(LDS_X);
      { const int aa = ab + qslot(0), ba = pbaddr(bbs, kq0); static_for<20>([&](auto kc) { pread<8, QRS, decltype(kc)::value>(F[0], smem, aa, ba); }); }
      int sl = 1;
#pragma unroll 1
      for (int s = 0; s < 14; s += 2) {
        WG_BARRIER();
        pstep<8, QRS>(acc, F[0], F[1], smem, ab + qslot(sl), pbaddr(bbs, KQ(s + 1)));
        sl = sl == 2 ? 0 : sl + 1;
        WG_BARRIER();
        pstep<8, QRS>(acc, F[1], F[0], smem, ab + qslot(sl), pbaddr(bbs, KQ(s + 2)));
        sl = sl == 2 ? 0 : sl + 1;
      }
      q_handover(acc, smem, wid);
    }
    STAMP(2);
    {
      const int w = wid, hh = w >> 1, rh = w & 1;
      const int lane = lane_id(), h = lane >> 5, r = lane & 31;
      f32x16 o[8];
#pragma unroll
      for (int dt = 0; dt < 8; ++dt)
#pragma unroll
        for (int i = 0; i < 16; ++i) o[dt][i] = 0.f;
      float mc = 0.f, lsum = 0.f;
      f32x16 s0, s1;
      u32x4 p0[2], p1[2];
      u32x4 qown[8];
      int kb = LDS_K + r * 512 + ((h ^ (r & 15)) << 4);
      int vb = LDS_V + (4 * h + ((lane & 15) >> 2)) * 512 + (((lane & 15) >> 2) << 6) + ((lane >> 4) & 1) * 32 + (lane & 3) * 8;
      LAS char* qp = smem + LDS_QP + w * 8192 + lane * 16;
      constexpr int PD = 4, RN = 6;
      bf16x4 vl[RN], vh[RN]; bf16x8 kf[RN], qf[2];
      float t0, t1, t2, t3, t4, mx;
#define FETCH(N) do { constexpr int n_ = (N), blk_ = n_ >> 4, j_ = n_ & 15; \
        if (blk_ & 1) kf[n_ % RN] = asbf(lds_r128(smem + (kb ^ ((j_ >> 1) << 5)) + (j_ & 1) * 256 + (blk_ >> 1) * 16384)); \
        else { LAS char* a_ = smem + (vb ^ ((j_ & 3) << 6)) + ((j_ >> 2) & 1) * 256 + (blk_ + (j_ >> 3)) * 8192; vl[n_ % RN] = lds_tr(a_); vh[n_ % RN] = lds_tr(a_ + 4096); } } while (0)
#define FETCHQ(J) qf[((J) >> 1) & 1] = asbf(lds_r128(qp + ((J) >> 1) * 1024))
#define JOB_AB(X, j) do { \
        if ((j) < 4) { _Pragma("unroll") for (int i_ = 0; i_ < 4; ++i_) X[4 * (j) + i_] = fmaf(X[4 * (j) + i_], SCL, -mc); } \
        else if ((j) == 4) { t0 = max3(X[0], X[1], X[2]); t1 = max3(X[3], X[4], X[5]); t2 = max3(X[6], X[7], X[8]); } \
        else if ((j) == 5) { t3 = max3(X[9], X[10], X[11]); t4 = max3(X[12], X[13], X[14]); t0 = max3(t0, t1, X[15]); } \
        else if ((j) == 6) { t0 = fmaxf(t0, max3(t2, t3, t4)); } \
        else if ((j) == 7) { mx = hmax(t0); } } while (0)
#define JOB_EXP(X, P, j) do { X[j] = __builtin_amdgcn_exp2f(X[j]); \
        if ((j) > 0) lsum += X[(j) - 1]; \
        if ((j) >= 2 && !((j) & 1)) P[((j) - 2) >> 3][(((j) - 2) >> 1) & 3] = pk2(X[(j) - 2], X[(j) - 1]); \
        if ((j) == 15) { lsum += X[15]; P[1][3] = pk2(X[14], X[15]); } } while (0)
#define RESCALE(X) do { if (__any(mx > THR)) { const float d_ = fmaxf(mx, 0.f), al_ = __builtin_amdgcn_exp2f(-d_); mc += d_; lsum *= al_; \
          _Pragma("unroll") for (int i_ = 0; i_ < 16; ++i_) X[i_] -= d_; \
          _Pragma("unroll") for (int dt = 0; dt < 8; ++dt) o[dt] *= al_; } SBAR(); } while (0)
#define FETCH_AHEAD(B, j) switch (j) { \
            case 0: FETCH((B) * 16 + 0 + PD); break; case 1: FETCH((B) * 16 + 1 + PD); break; case 2: FETCH((B) * 16 + 2 + PD); break; case 3: FETCH((B) * 16 + 3 + PD); break; \
            case 4: FETCH((B) * 16 + 4 + PD); break; case 5: FETCH((B) * 16 + 5 + PD); break; case 6: FETCH((B) * 16 + 6 + PD); break; case 7: FETCH((B) * 16 + 7 + PD); break; \
            case 8: FETCH((B) * 16 + 8 + PD); break; case 9: FETCH((B) * 16 + 9 + PD); break; case 10: FETCH((B) * 16 + 10 + PD); break; case 11: FETCH((B) * 16 + 11 + PD); break; \
            case 12: FETCH(((B) * 16 + 12 + PD) & 63); break; case 13: FETCH(((B) * 16 + 13 + PD) & 63); break; case 14: FETCH(((B) * 16 + 14 + PD) & 63); break; default: FETCH(((B) * 16 + 15 + PD) & 63); break; }
#define PVBLOCK(B, P, X, LAST, PN) do { _Pragma("unroll") for (int j = 0; j < 16; ++j) { \
          const bf16x8 a_ = __builtin_shufflevector(vl[((B) * 16 + j) % RN], vh[((B) * 16 + j) % RN], 0, 1, 2, 3, 4, 5, 6, 7); \
          o[j & 7] = MFMA32(a_, asbf(P[j >> 3]), o[j & 7]); \
          if (j + PD < 16 || !(LAST)) { FETCH_AHEAD(B, j) } \
          if (!(LAST) && j == 14) FETCHQ(1); \
          if ((LAST) != 2) JOB_AB(X, j); \
          PACE(PN); \
          SBAR(); } } while (0)
#define QKBLOCK(B, S, NEXT, JOB, X, P, PN) do { _Pragma("unroll") for (int j = 0; j < 16; ++j) { \
          const bf16x8 q_ = (j & 1) ? qf[(j >> 1) & 1] : asbf(qown[j >> 1]); \
          if (j == 0) { f32x16 z_; _Pragma("unroll") for (int i_ = 0; i_ < 16; ++i_) z_[i_] = 0.f; S = MFMA32(kf[((B) * 16 + j) % RN], q_, z_); } \
          else S = MFMA32(kf[((B) * 16 + j) % RN], q_, S); \
          if (j + PD < 16 || (NEXT)) { FETCH_AHEAD(B, j) } \
          if (!(j & 1) && j + 3 < 16) FETCHQ(j + 3); \
          if (JOB) JOB_EXP(X, P, j); \
          PACE(PN); \
          SBAR(); } } while (0)
#define FETCH4(N) do { FETCH(N); FETCH((N) + 1); FETCH((N) + 2); FETCH((N) + 3); } while (0)
      static_assert(PD == 4, "FETCH4");
      WG_BARRIER();
      STAMP(3);
#pragma unroll
      for (int f = 0; f < 8; ++f) qown[f] = lds_r128(smem + LDS_QT + w * 8192 + f * 1024 + lane * 16);
      FETCH4(16); FETCHQ(1); SBAR();
      QKBLOCK(1, s0, 0, 0, s0, p0, PACEN);
      FETCH4(48); FETCHQ(1); SBAR();
#pragma unroll
      for (int j = 0; j < 8; ++j) JOB_AB(s0, j);
      mc = mx;
#pragma unroll
      for (int i = 0; i < 16; ++i) s0[i] -= mx;
      mx = 0.f;
      SBAR();
      RESCALE(s0);
      QKBLOCK(3, s1, 0, 1, s0, p0, PACEN);
      kb ^= 32768;
#define INTERVAL(PN) do { \
        FETCH4(0); SBAR(); \
        PVBLOCK(0, p0, s1, 0, PN); \
        RESCALE(s1); \
        QKBLOCK(1, s0, 1, 1, s1, p1, PN); \
        PVBLOCK(2, p1, s0, 0, PN); \
        RESCALE(s0); \
        QKBLOCK(3, s1, 0, 1, s0, p0, PN); } while (0)
#define LSTAMP() do { STAMP_IF(t == 0, 4); STAMP_IF(t == 4, 5); STAMP_IF(t == 8, 6); STAMP_IF(t == 12, 7); } while (0)
#define ATT_LOOP(PN) _Pragma("unroll 1") for (int t = 0; t < 15; ++t) { \
        WG_BARRIER();     \
        LSTAMP(); \
        INTERVAL(PN); \
        kb ^= 32768; vb ^= 32768; }
      int fast2; asm volatile("s_mov_b32 %0, %1" : "=s"(fast2) : "s"(fastclk));
      if (fastclk > 0) ATT_LOOP(AP_P)
      if (fast2 <= 0) ATT_LOOP(-1)
#undef ATT_LOOP
#undef LSTAMP
#undef INTERVAL
#pragma unroll
      for (int j = 0; j < 8; ++j) JOB_AB(s1, j);
      if (__any(mx > THR)) {
        const float d_ = fmaxf(mx, 0.f), al_ = __builtin_amdgcn_exp2f(-d_);
        mc += d_; lsum *= al_;
#pragma unroll
        for (int i = 0; i < 16; ++i) s1[i] -= d_;
#pragma unroll
        for (int dt = 0; dt < 8; ++dt) o[dt] *= al_;
#pragma unroll
        for (int k = 0; k < 2; ++k)
#pragma unroll
          for (int i = 0; i < 4; ++i) p0[k][i] = pk2(__uint_as_float(p0[k][i] << 16) * al_, __uint_as_float(p0[k][i] & 0xffff0000u) * al_);
      }
      SBAR();
#pragma unroll
      for (int j = 0; j < 16; ++j) JOB_EXP(s1, p1, j);
      const float iv = 1.f / hsum(lsum);
      WG_BARRIER();
      STAMP(8);
      FETCH4(0); SBAR();
      PVBLOCK(0, p0, s1, 2, -1);
      FETCH4(32); SBAR();
      PVBLOCK(2, p1, s0, 2, -1);
#undef FETCH
#undef FETCHQ
#undef FETCH4
#undef FETCH_AHEAD
#undef JOB_AB
#undef JOB_EXP
#undef RESCALE
#undef PVBLOCK
#undef QKBLOCK
      STAMP(9);
      const int lane2 = lane_id(), h2 = lane2 >> 5, r2 = lane2 & 31;
      LAS char* ow = smem + LDS_K + (hh * 8 * 4 + rh * 2 + (r2 >> 4)) * 1024 + 2 * h2 * 256;
#pragma unroll
      for (int dt = 0; dt < 8; ++dt)
#pragma unroll
        for (int pp = 0; pp < 2; ++pp)
          lds_w128(ow + dt * 4096 + pp * 256 + (((r2 & 15) ^ (2 * h2 + pp) ^ ((dt & 1) * 4)) << 4),
                   u32x4{pk2(o[dt][8 * pp] * iv, o[dt][8 * pp + 1] * iv), pk2(o[dt][8 * pp + 2] * iv, o[dt][8 * pp + 3] * iv),
                         pk2(o[dt][8 * pp + 4] * iv, o[dt][8 * pp + 5] * iv), pk2(o[dt][8 * pp + 6] * iv, o[dt][8 * pp + 7] * iv)});
    }
    WG_BARRIER();
    STAMP(10);
    {
      HS_REQ();
      f32x4 acc[7][4]; PFrag<7> F[2];
#pragma unroll
      for (int i = 0; i < 7; ++i)
#pragma unroll
        for (int rt = 0; rt < 4; ++rt) acc[i][rt] = f32x4{0.f, 0.f, 0.f, 0.f};
      const int ab = pabase<ORS>(wid * 7), bbs = pbbase(LDS_K);
      { const int aa = ab + oslot(0), ba = pbaddr(bbs, KO(0)); static_for<18>([&](auto kc) { pread<7, ORS, decltype(kc)::value>(F[0], smem, aa, ba); }); }
      int sl = 1;
#pragma unroll 1
      for (int s = 0; s < 16; s += 2) {
        WG_BARRIER();
        pstep<7, ORS>(acc, F[0], F[1], smem, ab + oslot(sl), pbaddr(bbs, KO(s + 1)));
        sl = sl == 2 ? 0 : sl + 1;
        WG_BARRIER();
        pstep<7, ORS>(acc, F[1], F[0], smem, ab + oslot(sl), pbaddr(bbs, KO(s + 2)));
        sl = sl == 2 ? 0 : sl + 1;
      }
      STAMP(11);
      publish<7>(acc, ws, lid, wid * 7, HS_OK());
    }
  }
  WG_BARRIER();
  STAMP(15);
  if (wid < 7) {
    const int lane = lane_id();
    const rsrc_t rp = mk_rsrc(ws + ((size_t)(lid >> 3) * 64 + kvh * 8) * SLAB, 8 * SLAB);
    u32x4 v[8];
#pragma unroll
    for (int s = 0; s < 8; ++s) v[s] = __builtin_amdgcn_raw_buffer_load_b128(rp, wid * 1024 + lane * 16, s * SLAB, AUX_SC1);
    float a[8];
#pragma unroll
    for (int k = 0; k < 4; ++k) { a[2 * k] = __uint_as_float(v[0][k] << 16); a[2 * k + 1] = __uint_as_float(v[0][k] & 0xffff0000u); }
#pragma unroll
    for (int s = 1; s < 8; ++s)
#pragma unroll
      for (int k = 0; k < 4; ++k) { a[2 * k] += __uint_as_float(v[s][k] << 16); a[2 * k + 1] += __uint_as_float(v[s][k] & 0xffff0000u); }
    char* o = out + ((size_t)(bb * SQ + kvh * 8 + (lane & 7)) * (2 * DM) + blk * DM + wid * 64 + (lane >> 3) * 8) * 2;
    *(u32x4*)o = u32x4{pk2(a[0], a[1]), pk2(a[2], a[3]), pk2(a[4], a[5]), pk2(a[6], a[7])};
  }
  STAMP(12); STAMPC(14); STAMP_OUT();
}
}

extern "C" void kernel_launch(void* const* d_in, const int* in_sizes, int n_in, void* d_out, int out_size, void* d_ws, size_t ws_size,
                              hipStream_t stream) {
  (void)in_sizes; (void)n_in; (void)out_size; (void)ws_size;
  gqa_fused<<<256, 512, 0, stream>>>((const char*)d_in[0], (const char*)d_in[1], (const char*)d_in[2], (const char*)d_in[3], (const char*)d_in[4],
                                     (const char*)d_in[5], (const char*)d_in[6], (const char*)d_in[7], (const char*)d_in[8], (const char*)d_in[9],
                                     (char*)d_out, (char*)d_ws);
}
```

```cpp
#define AP_TH 312
#define AP_P 5
#include <hip/hip_runtime.h>
#include <hip/hip_bf16.h>
#include <utility>
#include <type_traits>

#define LAS __attribute__((address_space(3)))
#define DI __device__ __forceinline__
typedef __attribute__((ext_vector_type(8))) __bf16 bf16x8;
typedef __attribute__((ext_vector_type(4))) __bf16 bf16x4;
typedef __attribute__((ext_vector_type(2))) __bf16 bf16x2;
typedef __attribute__((ext_vector_type(16))) float f32x16;
typedef __attribute__((ext_vector_type(4))) float f32x4;
typedef __attribute__((ext_vector_type(2))) float f32x2;
typedef __attribute__((ext_vector_type(4))) unsigned u32x4;

namespace {
constexpr int SQ = 64, DM = 448, HKV = 8, SK = 1024, DH = 256;
constexpr int NQ = 4096;
constexpr int KV_BYTES = SK * DH * 2;
constexpr int W_BYTES = DM * NQ * 2;
constexpr int LDS_K = 0, LDS_V = 65536, LDS_QP = 131072, LDS_TOTAL = 163840;
constexpr int LDS_X = 34816, LDS_QT = LDS_V + 32768;
constexpr int XRS = 912;
constexpr int SLAB = 7168;
constexpr size_t CTL_OFF = (size_t)16 << 20;
constexpr float SCL = 0.0625f * 1.4426950408889634f;
constexpr float THR = 8.f;
constexpr int AUX_NT = 2, AUX_SC1 = 16;
#define AUX_W 2
#define OPOL ""
#define QPOL "nt"
#define WARM_AUX 2
#define OROT 1
#define PROBE_TH 350
constexpr int OOB = 0x40000000;
constexpr unsigned SPIN_CAP = 1u << 21;

#define SBAR() __builtin_amdgcn_sched_barrier(0)
#define WG_BARRIER() do { SBAR(); asm volatile("s_waitcnt lgkmcnt(0)\n\ts_barrier" ::: "memory"); SBAR(); } while (0)
#define STAMP(idx) do {} while (0)
#define STAMP_IF(c, idx) do {} while (0)
#define STAMPC(idx) do {} while (0)
#define STAMP_DECL do {} while (0)
#define STAMP_OUT() do {} while (0)
#define PACE(PN) do { if ((PN) >= 0) asm volatile("s_nop %0" :: "i"((PN) < 0 ? 0 : (PN))); } while (0)
#define PACEN -1
#define NAP() do {} while (0)
#define MFMA(a, b, c) __builtin_amdgcn_mfma_f32_16x16x32_bf16((a), (b), (c), 0, 0, 0)
#define MFMA32(a, b, c) __builtin_amdgcn_mfma_f32_32x32x16_bf16((a), (b), (c), 0, 0, 0)
#define RLX_AGENT __ATOMIC_RELAXED, __HIP_MEMORY_SCOPE_AGENT

DI unsigned pk2(float a, float b) { f32x2 f = {a, b}; bf16x2 r = __builtin_convertvector(f, bf16x2); return __builtin_bit_cast(unsigned, r); }
DI bf16x8 asbf(u32x4 v) { return __builtin_bit_cast(bf16x8, v); }
DI u32x4 lds_r128(LAS char* p) { return *(LAS u32x4*)p; }
DI void lds_w128(LAS char* p, u32x4 v) { *(LAS u32x4*)p = v; }
DI bf16x4 lds_tr(LAS char* p) { return __builtin_amdgcn_ds_read_tr16_b64_v4bf16((LAS bf16x4*)p); }
DI unsigned perm2(int e, unsigned hi, unsigned lo) { return __builtin_amdgcn_perm(hi, lo, e ? 0x07060302u : 0x05040100u); }
DI float max3(float a, float b, float c) { return fmaxf(fmaxf(a, b), c); }
DI float hmax(float v) {
  auto b = __builtin_amdgcn_permlane32_swap(__float_as_uint(v), __float_as_uint(v), false, false);
  return fmaxf(__uint_as_float(b[0]), __uint_as_float(b[1]));
}
DI float hsum(float v) {
  auto b = __builtin_amdgcn_permlane32_swap(__float_as_uint(v), __float_as_uint(v), false, false);
  return __uint_as_float(b[0]) + __uint_as_float(b[1]);
}
DI int lane_id() {
  int l; asm volatile("v_mbcnt_lo_u32_b32 %0, -1, 0\n\tv_mbcnt_hi_u32_b32 %0, -1, %0" : "=v"(l)); return l; }
typedef __amdgpu_buffer_rsrc_t rsrc_t;
DI rsrc_t mk_rsrc(const void* p, int bytes) { return __builtin_amdgcn_make_buffer_rsrc((void*)p, 0, bytes, 0x00020000); }
template <class F, int... I> DI void static_for_(F&& f, std::integer_sequence<int, I...>) { (f(std::integral_constant<int, I>{}), ...); }
template <int N, class F> DI void static_for(F&& f) { static_for_(f, std::make_integer_sequence<int, N>{}); }

DI unsigned* ctl_chal(char* ws, int wg, int slot) { return (unsigned*)(ws + CTL_OFF + (size_t)wg * 512 + 128 + slot * 4); }
DI unsigned* ctl_resp(char* ws, int wg, int slot) { return (unsigned*)(ws + CTL_OFF + (size_t)wg * 512 + 256 + slot * 4); }
DI void hs_echo(char* ws, int lid, int lane) {
  const unsigned cv = __hip_atomic_load(ctl_chal(ws, lid, lane), RLX_AGENT);
  const unsigned rv = __hip_atomic_load(ctl_resp(ws, (lid & ~7) + lane, lid & 7), RLX_AGENT);
  if (cv != rv) __hip_atomic_store(ctl_resp(ws, (lid & ~7) + lane, lid & 7), cv, RLX_AGENT);
}
DI u32x4 ld1(rsrc_t rs, int voff, int soff) { return __builtin_amdgcn_raw_buffer_load_b128(rs, voff, soff, AUX_NT); }
DI void ld8(u32x4 (&r)[8], rsrc_t rs, int voff, int soff, int sa, int sb) {
#pragma unroll
  for (int j = 0; j < 8; ++j) r[j] = ld1(rs, voff, soff + (j & 3) * sa + (j >> 2) * sb);
}

DI void dma1(rsrc_t rs, int voff, int soff, int ldsaddr) {
  asm volatile("s_mov_b32 m0, %0\n\ts_nop 0\n\tbuffer_load_dwordx4 %1, %2, %3 offen " QPOL " lds" :: "s"(ldsaddr), "v"(voff), "s"(rs), "s"(soff) : "memory");
}
DI void dma56nt(rsrc_t rs, int voff, int soff, int ldsaddr) {
  asm volatile("s_mov_b32 m0, %0\n\ts_mov_b32 exec_hi, 0xffffff\n\tbuffer_load_dwordx4 %1, %2, %3 offen nt lds\n\ts_mov_b32 exec_hi, -1"
               :: "s"(ldsaddr), "v"(voff), "s"(rs), "s"(soff) : "memory");
}
DI void dma56(rsrc_t rs, int voff, int soff, int ldsaddr) {
  asm volatile("s_mov_b32 m0, %0\n\ts_mov_b32 exec_hi, 0xffffff\n\tbuffer_load_dwordx4 %1, %2, %3 offen " OPOL " lds\n\ts_mov_b32 exec_hi, -1"
               :: "s"(ldsaddr), "v"(voff), "s"(rs), "s"(soff) : "memory");
}
#define WAITV(N) do { SBAR(); asm volatile("s_waitcnt vmcnt(" #N ")" ::: "memory"); SBAR(); } while (0)
template <int NF> struct PFrag { bf16x4 lo[NF], hi[NF]; bf16x8 b[4]; };
template <int RS> DI int pabase(int f0) {
  const int lane = lane_id(), g = lane >> 4, q = (lane & 15) >> 2, p = lane & 3;
  return (16 * (g >> 1) + 4 * (g & 1) + q) * RS + f0 * 32 + p * 8;
}
DI int pbbase(int bimg) { const int lane = lane_id(), g = lane >> 4, c = lane & 15; return bimg + g * 256 + ((c ^ g) << 4); }
DI int pbaddr(int bb, int ks) { return (bb ^ ((ks & 1) << 6)) + ks * 4096; }
template <int NF, int RS, int K> DI void pread(PFrag<NF>& f, LAS char* smem, int aa, int ba) {
  if constexpr (K < 4) f.b[K] = asbf(lds_r128(smem + ba + K * 1024));
  else if constexpr (K < 4 + 2 * NF) {
    if constexpr ((K & 1) == 0) f.lo[(K - 4) >> 1] = lds_tr(smem + aa + ((K - 4) >> 1) * 32);
    else f.hi[(K - 4) >> 1] = lds_tr(smem + aa + ((K - 4) >> 1) * 32 + 8 * RS);
  }
}
template <int NF, int RS>
DI void pstep(f32x4 (&acc)[NF][4], const PFrag<NF>& cur, PFrag<NF>& nxt, LAS char* smem, int aa, int ba) {
  static_for<NF * 4>([&](auto nc) {
    constexpr int n = decltype(nc)::value, i = n >> 2, rt = n & 3;
    const bf16x8 a = __builtin_shufflevector(cur.lo[i], cur.hi[i], 0, 1, 2, 3, 4, 5, 6, 7);
    acc[i][rt] = MFMA(a, cur.b[rt], acc[i][rt]);
    pread<NF, RS, n>(nxt, smem, aa, ba);
    SBAR();
  });
}
template <int NF> DI u32x4 ppack(const f32x4 (&acc)[NF][4], int i, int rp) {
  auto lo = __builtin_amdgcn_permlane16_swap(pk2(acc[i][2 * rp][0], acc[i][2 * rp][1]), pk2(acc[i][2 * rp + 1][0], acc[i][2 * rp + 1][1]), false, false);
  auto hi = __builtin_amdgcn_permlane16_swap(pk2(acc[i][2 * rp][2], acc[i][2 * rp][3]), pk2(acc[i][2 * rp + 1][2], acc[i][2 * rp + 1][3]), false, false);
  return u32x4{lo[0], hi[0], lo[1], hi[1]};
}
DI void q_handover(const f32x4 (&acc)[8][4], LAS char* smem, int w) {
  LAS char* base = smem + ((w & 1) ? LDS_QP : LDS_QT) + (w >> 1) * 16384 + lane_id() * 16;
#pragma unroll
  for (int i = 0; i < 8; ++i)
#pragma unroll
    for (int rp = 0; rp < 2; ++rp) lds_w128(base + rp * 8192 + i * 1024, ppack<8>(acc, i, rp));
}
template <int NF> DI void publish(const f32x4 (&acc)[NF][4], char* ws, int lid, int f0, bool ok) {
  const int lane = lane_id(), g = lane >> 4, c = lane & 15;
  const rsrc_t rp = mk_rsrc(ws + (size_t)(lid >> 3) * 64 * SLAB, 64 * SLAB);
  const int voff = (((g & 1) * 2 + (c >> 3)) * 8 + (lid & 7)) * SLAB + f0 * 256 + (g >> 1) * 128 + (c & 7) * 16;
#pragma unroll
  for (int i = 0; i < NF; ++i)
#pragma unroll
    for (int r2 = 0; r2 < 2; ++r2) __builtin_amdgcn_raw_buffer_store_b128(ppack<NF>(acc, i, r2), rp, voff, r2 * 32 * SLAB + i * 256, AUX_SC1);
  asm volatile("s_waitcnt vmcnt(0)" ::: "memory");
  for (unsigned spins = 0; !ok && spins < SPIN_CAP; ++spins) {
    if (lane < 8) hs_echo(ws, lid, lane);
    const unsigned cv = __hip_atomic_load(ctl_chal(ws, (lid & ~7) + (lane & 7), lid & 7), RLX_AGENT);
    const unsigned rv = __hip_atomic_load(ctl_resp(ws, lid, lane & 7), RLX_AGENT);
    ok = __all(cv == rv);
    __builtin_amdgcn_s_sleep(8);
  }
  if (lane < 8) __hip_atomic_fetch_add((unsigned*)(ws + CTL_OFF + (size_t)((lid & ~7) + lane) * 512), 1u, RLX_AGENT);
}
constexpr int QRS = 1056, ORS = 928;
DI int qslot(int i) { return i == 0 ? 92160 : (i == 1 ? 126976 : 0); }
DI int oslot(int i) { return i == 0 ? LDS_V : (i == 1 ? LDS_QP : LDS_V + 32768); }

__global__ __launch_bounds__(512, 2) void gqa_fused(const char* __restrict__ xA, const char* __restrict__ kA, const char* __restrict__ vA,
                                                    const char* __restrict__ wqA, const char* __restrict__ woA,
                                                    const char* __restrict__ xB, const char* __restrict__ kB, const char* __restrict__ vB,
                                                    const char* __restrict__ wqB, const char* __restrict__ woB,
                                                    char* __restrict__ out, char* ws) {
  __shared__ __attribute__((aligned(1024))) char smem_[LDS_TOTAL];
  LAS char* smem = (LAS char*)smem_;
  const int wid = __builtin_amdgcn_readfirstlane(threadIdx.x >> 6);
  const int bid = blockIdx.x, blk = (bid >> 2) & 1, kvh = (bid & 3) * 2 + (bid >> 7), bb = (bid >> 3) & 15;
  const int lid = (blk * 16 + bb) * 8 + kvh;
  const int kq0 = bb < 14 ? bb : bb - 14;
#define KQ(S) ((kq0 + (S)) >= 14 ? kq0 + (S) - 14 : kq0 + (S))
#define KO(S) (((S) + OROT * bb) & 15)
  STAMP_DECL;
  STAMP(0); STAMPC(13);
  const rsrc_t rc = mk_rsrc(ws + CTL_OFF + (size_t)(lid & ~7) * 512, 8 * 512);
#define HS_REQ() const unsigned hsc_ = __builtin_amdgcn_raw_buffer_load_b32(rc, (lane_id() & 7) * 512 + 128 + (lid & 7) * 4, 0, AUX_SC1), \
                                hsr_ = __builtin_amdgcn_raw_buffer_load_b32(rc, (lid & 7) * 512 + 256 + (lane_id() & 7) * 4, 0, AUX_SC1)
#define HS_OK() __all(hsc_ == hsr_)

  if (wid >= 4) {
    int lane = lane_id();
    const int h = wid - 4;
    bool hs = h == 0 && lane < 8;
    __builtin_amdgcn_raw_buffer_store_b32(0u, rc, hs && lane == 0 ? (lid & 7) * 512 : OOB, 0, AUX_SC1);
    const unsigned hs_r = __builtin_amdgcn_raw_buffer_load_b32(rc, hs ? (lid & 7) * 512 + 256 + lane * 4 : OOB, 0, AUX_SC1);
    const rsrc_t rwq = mk_rsrc((blk ? wqB : wqA) + kvh * 1024, W_BYTES - kvh * 1024);
    const rsrc_t rwo = mk_rsrc((blk ? woB : woA) + (size_t)kvh * 512 * DM * 2, 512 * DM * 2);
    const rsrc_t rk = mk_rsrc((blk ? kB : kA) + (size_t)(bb * HKV + kvh) * KV_BYTES, KV_BYTES);
    const rsrc_t rv = mk_rsrc((blk ? vB : vA) + (size_t)(bb * HKV + kvh) * KV_BYTES, KV_BYTES);
#define voff l16
#define wqv l16
#define wov l16
    int l16 = lane * 16;
    const int irow = 16 * (h >> 1) + 4 * (h & 1);
#define QDMA(S) do { _Pragma("unroll") for (int j = 0; j < 8; ++j) \
      dma1(rwq, wqv, (KQ(S) * 32 + 8 * h + j) * (NQ * 2), qslot((S) % 3) + (irow + 8 * (j >> 2) + (j & 3)) * QRS); SBAR(); } while (0)
#define ODMA(S) ODMAX(dma56, S)
#define ODMAX(FN, S) do { { _Pragma("unroll") for (int j = 0; j < 8; ++j) \
      FN(rwo, wov, (KO(S) * 32 + 16 * (h & 1) + 4 * (h >> 1) + 8 * (j >> 2) + (j & 3)) * (DM * 2), oslot((S) % 3) + (irow + 8 * (j >> 2) + (j & 3)) * ORS); } SBAR(); } while (0)
#define LOADP(SET, RS, TILE, AUX) do { _Pragma("unroll") for (int j = 0; j < 8; ++j) \
      SET[j] = __builtin_amdgcn_raw_buffer_load_b128(RS, voff, (TILE) * 32768 + h * 1024 + j * 4096, AUX); SBAR(); } while (0)
#define LOADK(SET, TILE) LOADP(SET, rk, TILE, KVAUX)
#define LOADV(SET, TILE) LOADP(SET, rv, TILE, KVAUX)
    QDMA(0); QDMA(1); QDMA(2);
    WAITV(16);
    WG_BARRIER();
    STAMP(1);
    WAITV(8);
#pragma unroll 1
    for (int s = 0; s < 11; ++s) {
      WG_BARRIER();
      QDMA(s + 3);
      WAITV(8);
    }
    u32x4 R0[8], R1[8];
    WG_BARRIER();
    WAITV(0);
    const unsigned tb0 = (unsigned)__builtin_amdgcn_s_memrealtime(); __builtin_amdgcn_s_waitcnt(0xC07F); SBAR();
    LOADP(R0, rk, 0, AUX_NT);
    WG_BARRIER();
    WG_BARRIER();
    WAITV(0);
    const unsigned lat = (unsigned)__builtin_amdgcn_s_memrealtime() - tb0;
    __builtin_amdgcn_raw_buffer_store_b32(hs_r + 1u, rc, hs ? lane * 512 + 128 + (lid & 7) * 4 : OOB, 0, AUX_SC1);
    LOADP(R1, rk, 1, AUX_NT);
    if (lane == 0) *(volatile LAS unsigned*)(smem + 32768 + 1024 + h * 4) = lat;
    STAMP(2);
    lane = lane_id(); l16 = lane * 16; hs = h == 0 && lane < 8;
    const int hi = lane >> 5, ch = lane & 31;
    const int row0 = 2 * h + hi;
    const int kw0 = LDS_K + row0 * 512 + ((ch ^ row0) << 4), kw1 = LDS_K + (row0 + 8) * 512 + ((ch ^ row0 ^ 8) << 4);
    const int vw0 = LDS_V + row0 * 512 + ((ch ^ ((row0 & 3) << 2)) << 4);
#define WRITEK(SET, BUF) do { _Pragma("unroll") for (int i = 0; i < 8; ++i) \
      lds_w128(smem + ((i & 1) ? kw1 : kw0) + (BUF) + (i >> 1) * 8192, SET[i]); SBAR(); } while (0)
#define WRITEV(SET, BUF) do { _Pragma("unroll") for (int i = 0; i < 8; ++i) \
      lds_w128(smem + vw0 + (BUF) + i * 4096, SET[i]); SBAR(); } while (0)
    WRITEK(R0, 0); LOADP(R0, rv, 0, AUX_NT);
    WG_BARRIER();
    const u32x4 lat4 = lds_r128(smem + 32768 + 1024);
    const int res = __builtin_amdgcn_readfirstlane((int)(lat4[0] + lat4[1] + lat4[2] + lat4[3] < (unsigned)PROBE_TH));
    SBAR(); __builtin_amdgcn_s_barrier(); SBAR();
    auto stream = [&](auto auxc) {
    constexpr int KVAUX = decltype(auxc)::value;
    u32x4 SA[8], SB[8];
    WRITEK(R1, 32768); LOADK(SA, 2); WRITEV(R0, 0); LOADV(SB, 1);
    STAMP(4);
    WG_BARRIER();
#define STEADY(T) do { const int b0_ = ((T) & 1) << 15; WRITEK(SA, b0_); LOADK(SA, (T) + 3); WRITEV(SB, b0_ ^ 32768); LOADV(SB, (T) + 2); } while (0)
#pragma unroll 1
    for (int t = 0; t < 11; ++t) {
      STEADY(t);
      STAMP_IF(t == 4, 5); STAMP_IF(t == 8, 6);
      WG_BARRIER();
    }
    const unsigned hs_c = __builtin_amdgcn_raw_buffer_load_b32(rc, hs ? (lid & 7) * 512 + 128 + lane * 4 : OOB, 0, AUX_SC1);
    SBAR();
    STEADY(11);
    WG_BARRIER();
    STEADY(12);
    __builtin_amdgcn_raw_buffer_store_b32(hs_c, rc, hs ? lane * 512 + 256 + (lid & 7) * 4 : OOB, 0, AUX_SC1);
    STAMP(7);
    WG_BARRIER();
    WRITEK(SA, 32768); WRITEV(SB, 0); LOADV(SB, 15);
    WG_BARRIER();
    WRITEV(SB, 32768);
    };
    int res2; asm volatile("s_mov_b32 %0, %1" : "=s"(res2) : "s"(res));
    if (res) stream(std::integral_constant<int, 0>{});
    if (!res2) stream(std::integral_constant<int, AUX_NT>{});
    STAMP(8);
    WG_BARRIER();
    ODMA(0); ODMA(1);
    WAITV(0);
    WG_BARRIER();
    STAMP(10);
    ODMA(2);
#pragma unroll 1
    for (int s = 0; s < 13; ++s) {
      WG_BARRIER();
      ODMA(s + 3);
      WAITV(8);
    }
    WG_BARRIER();
    WAITV(0);
    WG_BARRIER();
    WG_BARRIER();
    STAMP(11);
    if (h == 0) {
      const unsigned* cnt = (const unsigned*)(ws + CTL_OFF + (size_t)lid * 512);
      for (unsigned spins = 0; spins < SPIN_CAP; ++spins) {
        if (__hip_atomic_load(cnt, RLX_AGENT) >= 32u) break;
        if ((spins & 63u) == 63u) { const int l = lane_id(); if (l < 8) hs_echo(ws, lid, l); }
        __builtin_amdgcn_s_sleep(2);
      }
    }
#undef QDMA
#undef ODMA
#undef STEADY
#undef LOADP
#undef LOADK
#undef LOADV
#undef WRITEK
#undef WRITEV
#undef voff
#undef wqv
#undef wov
  } else {
    const unsigned c0_ = (unsigned)__builtin_amdgcn_s_memtime(), r0_ = (unsigned)__builtin_amdgcn_s_memrealtime();
    {
      const int lane = lane_id();
      const rsrc_t rx = mk_rsrc((blk ? xB : xA) + (size_t)bb * SQ * DM * 2, SQ * DM * 2);
      const int xp = ((wid + (kvh >> 1)) & 3) * 14;
      u32x4 xr[14];
#pragma unroll
      for (int f = 0; f < 14; ++f) xr[f] = ld1(rx, lane * 16, (xp + f) * 1024);
      SBAR();
#pragma unroll
      for (int f = 0; f < 14; ++f) {
        const unsigned idx = (xp + f) * 64 + lane, row = idx / 56u, q = idx - row * 56u;
        lds_w128(smem + LDS_X + (q >> 2) * 4096 + (row >> 4) * 1024 + (q & 3) * 256 + (((row & 15) ^ (q & 7)) << 4), xr[f]);
      }
    }
    WG_BARRIER();
    const int fastclk = __builtin_amdgcn_readfirstlane((int)(((unsigned)__builtin_amdgcn_s_memtime() - c0_) * 16u - ((unsigned)__builtin_amdgcn_s_memrealtime() - r0_) * (unsigned)AP_TH));
    __builtin_amdgcn_s_waitcnt(0xC07F); SBAR();
    STAMP(1);
    {
      f32x4 acc[8][4]; PFrag<8> F[2];
#pragma unroll
      for (int i = 0; i < 8; ++i)
#pragma unroll
        for (int rt = 0; rt < 4; ++rt) acc[i][rt] = f32x4{0.f, 0.f, 0.f, 0.f};
      const int ab = pabase<QRS>(wid * 8), bbs = pbbase(LDS_X);
      { const int aa = ab + qslot(0), ba = pbaddr(bbs, kq0); static_for<20>([&](auto kc) { pread<8, QRS, decltype(kc)::value>(F[0], smem, aa, ba); }); }
      int sl = 1;
#pragma unroll 1
      for (int s = 0; s < 14; s += 2) {
        WG_BARRIER();
        pstep<8, QRS>(acc, F[0], F[1], smem, ab + qslot(sl), pbaddr(bbs, KQ(s + 1)));
        sl = sl == 2 ? 0 : sl + 1;
        WG_BARRIER();
        pstep<8, QRS>(acc, F[1], F[0], smem, ab + qslot(sl), pbaddr(bbs, KQ(s + 2)));
        sl = sl == 2 ? 0 : sl + 1;
      }
      q_handover(acc, smem, wid);
    }
    STAMP(2);
    {
      const int w = wid, hh = w >> 1, rh = w & 1;
      const int lane = lane_id(), h = lane >> 5, r = lane & 31;
      f32x16 o[8];
#pragma unroll
      for (int dt = 0; dt < 8; ++dt)
#pragma unroll
        for (int i = 0; i < 16; ++i) o[dt][i] = 0.f;
      float mc = 0.f, lsum = 0.f;
      f32x16 s0, s1;
      u32x4 p0[2], p1[2];
      u32x4 qown[8];
      int kb = LDS_K + r * 512 + ((h ^ (r & 15)) << 4);
      int vb = LDS_V + (4 * h + ((lane & 15) >> 2)) * 512 + (((lane & 15) >> 2) << 6) + ((lane >> 4) & 1) * 32 + (lane & 3) * 8;
      LAS char* qp = smem + LDS_QP + w * 8192 + lane * 16;
      constexpr int PD = 4, RN = 6;
      bf16x4 vl[RN], vh[RN]; bf16x8 kf[RN], qf[2];
      float t0, t1, t2, t3, t4, mx;
#define FETCH(N) do { constexpr int n_ = (N), blk_ = n_ >> 4, j_ = n_ & 15; \
        if (blk_ & 1) kf[n_ % RN] = asbf(lds_r128(smem + (kb ^ ((j_ >> 1) << 5)) + (j_ & 1) * 256 + (blk_ >> 1) * 16384)); \
        else { LAS char* a_ = smem + (vb ^ ((j_ & 3) << 6)) + ((j_ >> 2) & 1) * 256 + (blk_ + (j_ >> 3)) * 8192; vl[n_ % RN] = lds_tr(a_); vh[n_ % RN] = lds_tr(a_ + 4096); } } while (0)
#define FETCHQ(J) qf[((J) >> 1) & 1] = asbf(lds_r128(qp + ((J) >> 1) * 1024))
#define JOB_AB(X, j) do { \
        if ((j) < 4) { _Pragma("unroll") for (int i_ = 0; i_ < 4; ++i_) X[4 * (j) + i_] = fmaf(X[4 * (j) + i_], SCL, -mc); } \
        else if ((j) == 4) { t0 = max3(X[0], X[1], X[2]); t1 = max3(X[3], X[4], X[5]); t2 = max3(X[6], X[7], X[8]); } \
        else if ((j) == 5) { t3 = max3(X[9], X[10], X[11]); t4 = max3(X[12], X[13], X[14]); t0 = max3(t0, t1, X[15]); } \
        else if ((j) == 6) { t0 = fmaxf(t0, max3(t2, t3, t4)); } \
        else if ((j) == 7) { mx = hmax(t0); } } while (0)
#define JOB_EXP(X, P, j) do { X[j] = __builtin_amdgcn_exp2f(X[j]); \
        if ((j) > 0) lsum += X[(j) - 1]; \
        if ((j) >= 2 && !((j) & 1)) P[((j) - 2) >> 3][(((j) - 2) >> 1) & 3] = pk2(X[(j) - 2], X[(j) - 1]); \
        if ((j) == 15) { lsum += X[15]; P[1][3] = pk2(X[14], X[15]); } } while (0)
#define RESCALE(X) do { if (__any(mx > THR)) { const float d_ = fmaxf(mx, 0.f), al_ = __builtin_amdgcn_exp2f(-d_); mc += d_; lsum *= al_; \
          _Pragma("unroll") for (int i_ = 0; i_ < 16; ++i_) X[i_] -= d_; \
          _Pragma("unroll") for (int dt = 0; dt < 8; ++dt) o[dt] *= al_; } SBAR(); } while (0)
#define FETCH_AHEAD(B, j) switch (j) { \
            case 0: FETCH((B) * 16 + 0 + PD); break; case 1: FETCH((B) * 16 + 1 + PD); break; case 2: FETCH((B) * 16 + 2 + PD); break; case 3: FETCH((B) * 16 + 3 + PD); break; \
            case 4: FETCH((B) * 16 + 4 + PD); break; case 5: FETCH((B) * 16 + 5 + PD); break; case 6: FETCH((B) * 16 + 6 + PD); break; case 7: FETCH((B) * 16 + 7 + PD); break; \
            case 8: FETCH((B) * 16 + 8 + PD); break; case 9: FETCH((B) * 16 + 9 + PD); break; case 10: FETCH((B) * 16 + 10 + PD); break; case 11: FETCH((B) * 16 + 11 + PD); break; \
            case 12: FETCH(((B) * 16 + 12 + PD) & 63); break; case 13: FETCH(((B) * 16 + 13 + PD) & 63); break; case 14: FETCH(((B) * 16 + 14 + PD) & 63); break; default: FETCH(((B) * 16 + 15 + PD) & 63); break; }
#define PVBLOCK(B, P, X, LAST, PN) do { _Pragma("unroll") for (int j = 0; j < 16; ++j) { \
          const bf16x8 a_ = __builtin_shufflevector(vl[((B) * 16 + j) % RN], vh[((B) * 16 + j) % RN], 0, 1, 2, 3, 4, 5, 6, 7); \
          o[j & 7] = MFMA32(a_, asbf(P[j >> 3]), o[j & 7]); \
          if (j + PD < 16 || !(LAST)) { FETCH_AHEAD(B, j) } \
          if (!(LAST) && j == 14) FETCHQ(1); \
          if ((LAST) != 2) JOB_AB(X, j); \
          PACE(PN); \
          SBAR(); } } while (0)
#define QKBLOCK(B, S, NEXT, JOB, X, P, PN) do { _Pragma("unroll") for (int j = 0; j < 16; ++j) { \
          const bf16x8 q_ = (j & 1) ? qf[(j >> 1) & 1] : asbf(qown[j >> 1]); \
          if (j == 0) { f32x16 z_; _Pragma("unroll") for (int i_ = 0; i_ < 16; ++i_) z_[i_] = 0.f; S = MFMA32(kf[((B) * 16 + j) % RN], q_, z_); } \
          else S = MFMA32(kf[((B) * 16 + j) % RN], q_, S); \
          if (j + PD < 16 || (NEXT)) { FETCH_AHEAD(B, j) } \
          if (!(j & 1) && j + 3 < 16) FETCHQ(j + 3); \
          if (JOB) JOB_EXP(X, P, j); \
          PACE(PN); \
          SBAR(); } } while (0)
#define FETCH4(N) do { FETCH(N); FETCH((N) + 1); FETCH((N) + 2); FETCH((N) + 3); } while (0)
      static_assert(PD == 4, "FETCH4");
      WG_BARRIER();
      STAMP(3);
      SBAR(); __builtin_amdgcn_s_barrier(); SBAR();
#pragma unroll
      for (int f = 0; f < 8; ++f) qown[f] = lds_r128(smem + LDS_QT + w * 8192 + f * 1024 + lane * 16);
      FETCH4(16); FETCHQ(1); SBAR();
      QKBLOCK(1, s0, 0, 0, s0, p0, PACEN);
      FETCH4(48); FETCHQ(1); SBAR();
#pragma unroll
      for (int j = 0; j < 8; ++j) JOB_AB(s0, j);
      mc = mx;
#pragma unroll
      for (int i = 0; i < 16; ++i) s0[i] -= mx;
      mx = 0.f;
      SBAR();
      RESCALE(s0);
      QKBLOCK(3, s1, 0, 1, s0, p0, PACEN);
      kb ^= 32768;
#define INTERVAL(PN) do { \
        FETCH4(0); SBAR(); \
        PVBLOCK(0, p0, s1, 0, PN); \
        RESCALE(s1); \
        QKBLOCK(1, s0, 1, 1, s1, p1, PN); \
        PVBLOCK(2, p1, s0, 0, PN); \
        RESCALE(s0); \
        QKBLOCK(3, s1, 0, 1, s0, p0, PN); } while (0)
#define LSTAMP() do { STAMP_IF(t == 0, 4); STAMP_IF(t == 4, 5); STAMP_IF(t == 8, 6); STAMP_IF(t == 12, 7); } while (0)
#define ATT_LOOP(PN) _Pragma("unroll 1") for (int t = 0; t < 15; ++t) { \
        WG_BARRIER();     \
        LSTAMP(); \
        INTERVAL(PN); \
        kb ^= 32768; vb ^= 32768; }
      int fast2; asm volatile("s_mov_b32 %0, %1" : "=s"(fast2) : "s"(fastclk));
      if (fastclk > 0) ATT_LOOP(AP_P)
      if (fast2 <= 0) ATT_LOOP(-1)
#undef ATT_LOOP
#undef LSTAMP
#undef INTERVAL
#pragma unroll
      for (int j = 0; j < 8; ++j) JOB_AB(s1, j);
      if (__any(mx > THR)) {
        const float d_ = fmaxf(mx, 0.f), al_ = __builtin_amdgcn_exp2f(-d_);
        mc += d_; lsum *= al_;
#pragma unroll
        for (int i = 0; i < 16; ++i) s1[i] -= d_;
#pragma unroll
        for (int dt = 0; dt < 8; ++dt) o[dt] *= al_;
#pragma unroll
        for (int k = 0; k < 2; ++k)
#pragma unroll
          for (int i = 0; i < 4; ++i) p0[k][i] = pk2(__uint_as_float(p0[k][i] << 16) * al_, __uint_as_float(p0[k][i] & 0xffff0000u) * al_);
      }
      SBAR();
#pragma unroll
      for (int j = 0; j < 16; ++j) JOB_EXP(s1, p1, j);
      const float iv = 1.f / hsum(lsum);
      WG_BARRIER();
      STAMP(8);
      FETCH4(0); SBAR();
      PVBLOCK(0, p0, s1, 2, -1);
      FETCH4(32); SBAR();
      PVBLOCK(2, p1, s0, 2, -1);
#undef FETCH
#undef FETCHQ
#undef FETCH4
#undef FETCH_AHEAD
#undef JOB_AB
#undef JOB_EXP
#undef RESCALE
#undef PVBLOCK
#undef QKBLOCK
      STAMP(9);
      const int lane2 = lane_id(), h2 = lane2 >> 5, r2 = lane2 & 31;
      LAS char* ow = smem + LDS_K + (hh * 8 * 4 + rh * 2 + (r2 >> 4)) * 1024 + 2 * h2 * 256;
#pragma unroll
      for (int dt = 0; dt < 8; ++dt)
#pragma unroll
        for (int pp = 0; pp < 2; ++pp)
          lds_w128(ow + dt * 4096 + pp * 256 + (((r2 & 15) ^ (2 * h2 + pp) ^ ((dt & 1) * 4)) << 4),
                   u32x4{pk2(o[dt][8 * pp] * iv, o[dt][8 * pp + 1] * iv), pk2(o[dt][8 * pp + 2] * iv, o[dt][8 * pp + 3] * iv),
                         pk2(o[dt][8 * pp + 4] * iv, o[dt][8 * pp + 5] * iv), pk2(o[dt][8 * pp + 6] * iv, o[dt][8 * pp + 7] * iv)});
    }
    WG_BARRIER();
    STAMP(10);
    {
      HS_REQ();
      f32x4 acc[7][4]; PFrag<7> F[2];
#pragma unroll
      for (int i = 0; i < 7; ++i)
#pragma unroll
        for (int rt = 0; rt < 4; ++rt) acc[i][rt] = f32x4{0.f, 0.f, 0.f, 0.f};
      const int ab = pabase<ORS>(wid * 7), bbs = pbbase(LDS_K);
      { const int aa = ab + oslot(0), ba = pbaddr(bbs, KO(0)); static_for<18>([&](auto kc) { pread<7, ORS, decltype(kc)::value>(F[0], smem, aa, ba); }); }
      int sl = 1;
#pragma unroll 1
      for (int s = 0; s < 16; s += 2) {
        WG_BARRIER();
        pstep<7, ORS>(acc, F[0], F[1], smem, ab + oslot(sl), pbaddr(bbs, KO(s + 1)));
        sl = sl == 2 ? 0 : sl + 1;
        WG_BARRIER();
        pstep<7, ORS>(acc, F[1], F[0], smem, ab + oslot(sl), pbaddr(bbs, KO(s + 2)));
        sl = sl == 2 ? 0 : sl + 1;
      }
      STAMP(11);
      publish<7>(acc, ws, lid, wid * 7, HS_OK());
    }
  }
  WG_BARRIER();
  STAMP(15);
  if (wid < 7) {
    const int lane = lane_id();
    const rsrc_t rp = mk_rsrc(ws + ((size_t)(lid >> 3) * 64 + kvh * 8) * SLAB, 8 * SLAB);
    u32x4 v[8];
#pragma unroll
    for (int s = 0; s < 8; ++s) v[s] = __builtin_amdgcn_raw_buffer_load_b128(rp, wid * 1024 + lane * 16, s * SLAB, AUX_SC1);
    float a[8];
#pragma unroll
    for (int k = 0; k < 4; ++k) { a[2 * k] = __uint_as_float(v[0][k] << 16); a[2 * k + 1] = __uint_as_float(v[0][k] & 0xffff0000u); }
#pragma unroll
    for (int s = 1; s < 8; ++s)
#pragma unroll
      for (int k = 0; k < 4; ++k) { a[2 * k] += __uint_as_float(v[s][k] << 16); a[2 * k + 1] += __uint_as_float(v[s][k] & 0xffff0000u); }
    char* o = out + ((size_t)(bb * SQ + kvh * 8 + (lane & 7)) * (2 * DM) + blk * DM + wid * 64 + (lane >> 3) * 8) * 2;
    *(u32x4*)o = u32x4{pk2(a[0], a[1]), pk2(a[2], a[3]), pk2(a[4], a[5]), pk2(a[6], a[7])};
  }
  STAMP(12); STAMPC(14); STAMP_OUT();
}
}

extern "C" void kernel_launch(void* const* d_in, const int* in_sizes, int n_in, void* d_out, int out_size, void* d_ws, size_t ws_size,
                              hipStream_t stream) {
  (void)in_sizes; (void)n_in; (void)out_size; (void)ws_size;
  gqa_fused<<<256, 512, 0, stream>>>((const char*)d_in[0], (const char*)d_in[1], (const char*)d_in[2], (const char*)d_in[3], (const char*)d_in[4],
                                     (const char*)d_in[5], (const char*)d_in[6], (const char*)d_in[7], (const char*)d_in[8], (const char*)d_in[9],
                                     (char*)d_out, (char*)d_ws);
}
```

```cpp
#include <hip/hip_runtime.h>
#include <hip/hip_bf16.h>
#include <utility>
#include <type_traits>

#define LAS __attribute__((address_space(3)))
#define DI __device__ __forceinline__
typedef __attribute__((ext_vector_type(8))) __bf16 bf16x8;
typedef __attribute__((ext_vector_type(4))) __bf16 bf16x4;
typedef __attribute__((ext_vector_type(2))) __bf16 bf16x2;
typedef __attribute__((ext_vector_type(16))) float f32x16;
typedef __attribute__((ext_vector_type(4))) float f32x4;
typedef __attribute__((ext_vector_type(2))) float f32x2;
typedef __attribute__((ext_vector_type(4))) unsigned u32x4;

namespace {
constexpr int SQ = 64, DM = 448, HKV = 8, SK = 1024, DH = 256;
constexpr int NQ = 4096;
constexpr int KV_BYTES = SK * DH * 2;
constexpr int W_BYTES = DM * NQ * 2;
constexpr int LDS_K = 0, LDS_V = 65536, LDS_QP = 131072, LDS_TOTAL = 163840;
constexpr int LDS_X = 34816, LDS_QT = LDS_V + 32768;
constexpr int XRS = 912;
constexpr int SLAB = 7168;
constexpr size_t CTL_OFF = (size_t)16 << 20;
constexpr float SCL = 0.0625f * 1.4426950408889634f;
constexpr float THR = 8.f;
constexpr int AUX_NT = 2, AUX_SC1 = 16;
#define AUX_W 2
#define OPOL ""
#define QPOL "nt"
#define WARM_AUX 2
#define OROT 1
#define XAUX 2
#define OUT_AUX 0
constexpr int OOB = 0x40000000;
constexpr unsigned SPIN_CAP = 1u << 21;

#define SBAR() __builtin_amdgcn_sched_barrier(0)
#define WG_BARRIER() do { SBAR(); asm volatile("s_waitcnt lgkmcnt(0)\n\ts_barrier" ::: "memory"); SBAR(); } while (0)
#define STAMP(idx) do {} while (0)
#define STAMP_IF(c, idx) do {} while (0)
#define STAMPC(idx) do {} while (0)
#define STAMP_DECL do {} while (0)
#define STAMP_OUT() do {} while (0)
#define PACE(PN) do { if ((PN) >= 0) asm volatile("s_nop %0" :: "i"((PN) < 0 ? 0 : (PN))); } while (0)
#define AP_TH 328
#define AP_P 7
#define AP_N 15
#define AP_P2 -1
#define PACEN -1
#define NAP() do {} while (0)
#define MFMA(a, b, c) __builtin_amdgcn_mfma_f32_16x16x32_bf16((a), (b), (c), 0, 0, 0)
#define MFMA32(a, b, c) __builtin_amdgcn_mfma_f32_32x32x16_bf16((a), (b), (c), 0, 0, 0)
#define RLX_AGENT __ATOMIC_RELAXED, __HIP_MEMORY_SCOPE_AGENT

DI unsigned pk2(float a, float b) { f32x2 f = {a, b}; bf16x2 r = __builtin_convertvector(f, bf16x2); return __builtin_bit_cast(unsigned, r); }
DI bf16x8 asbf(u32x4 v) { return __builtin_bit_cast(bf16x8, v); }
DI u32x4 lds_r128(LAS char* p) { return *(LAS u32x4*)p; }
DI void lds_w128(LAS char* p, u32x4 v) { *(LAS u32x4*)p = v; }
DI bf16x4 lds_tr(LAS char* p) { return __builtin_amdgcn_ds_read_tr16_b64_v4bf16((LAS bf16x4*)p); }
DI unsigned perm2(int e, unsigned hi, unsigned lo) { return __builtin_amdgcn_perm(hi, lo, e ? 0x07060302u : 0x05040100u); }
DI float max3(float a, float b, float c) { return fmaxf(fmaxf(a, b), c); }
DI float hmax(float v) {
  auto b = __builtin_amdgcn_permlane32_swap(__float_as_uint(v), __float_as_uint(v), false, false);
  return fmaxf(__uint_as_float(b[0]), __uint_as_float(b[1]));
}
DI float hsum(float v) {
  auto b = __builtin_amdgcn_permlane32_swap(__float_as_uint(v), __float_as_uint(v), false, false);
  return __uint_as_float(b[0]) + __uint_as_float(b[1]);
}
DI int lane_id() {
  int l; asm volatile("v_mbcnt_lo_u32_b32 %0, -1, 0\n\tv_mbcnt_hi_u32_b32 %0, -1, %0" : "=v"(l)); return l; }
typedef __amdgpu_buffer_rsrc_t rsrc_t;
DI rsrc_t mk_rsrc(const void* p, int bytes) { return __builtin_amdgcn_make_buffer_rsrc((void*)p, 0, bytes, 0x00020000); }
template <class F, int... I> DI void static_for_(F&& f, std::integer_sequence<int, I...>) { (f(std::integral_constant<int, I>{}), ...); }
template <int N, class F> DI void static_for(F&& f) { static_for_(f, std::make_integer_sequence<int, N>{}); }

DI unsigned* ctl_chal(char* ws, int wg, int slot) { return (unsigned*)(ws + CTL_OFF + (size_t)wg * 512 + 128 + slot * 4); }
DI unsigned* ctl_resp(char* ws, int wg, int slot) { return (unsigned*)(ws + CTL_OFF + (size_t)wg * 512 + 256 + slot * 4); }
DI void hs_echo(char* ws, int lid, int lane) {
  const unsigned cv = __hip_atomic_load(ctl_chal(ws, lid, lane), RLX_AGENT);
  const unsigned rv = __hip_atomic_load(ctl_resp(ws, (lid & ~7) + lane, lid & 7), RLX_AGENT);
  if (cv != rv) __hip_atomic_store(ctl_resp(ws, (lid & ~7) + lane, lid & 7), cv, RLX_AGENT);
}
DI u32x4 ld1(rsrc_t rs, int voff, int soff) { return __builtin_amdgcn_raw_buffer_load_b128(rs, voff, soff, AUX_NT); }
DI void ld8(u32x4 (&r)[8], rsrc_t rs, int voff, int soff, int sa, int sb) {
#pragma unroll
  for (int j = 0; j < 8; ++j) r[j] = ld1(rs, voff, soff + (j & 3) * sa + (j >> 2) * sb);
}

DI void dma1(rsrc_t rs, int voff, int soff, int ldsaddr) {
  asm volatile("s_mov_b32 m0, %0\n\ts_nop 0\n\tbuffer_load_dwordx4 %1, %2, %3 offen " QPOL " lds" :: "s"(ldsaddr), "v"(voff), "s"(rs), "s"(soff) : "memory");
}
DI void dmakv(rsrc_t rs, int voff, int soff, int ldsaddr) {
  asm volatile("s_mov_b32 m0, %0\n\ts_nop 0\n\tbuffer_load_dwordx4 %1, %2, %3 offen nt lds" :: "s"(ldsaddr), "v"(voff), "s"(rs), "s"(soff) : "memory");
}
DI void dma56nt(rsrc_t rs, int voff, int soff, int ldsaddr) {
  asm volatile("s_mov_b32 m0, %0\n\ts_mov_b32 exec_hi, 0xffffff\n\tbuffer_load_dwordx4 %1, %2, %3 offen nt lds\n\ts_mov_b32 exec_hi, -1"
               :: "s"(ldsaddr), "v"(voff), "s"(rs), "s"(soff) : "memory");
}
DI void dma56(rsrc_t rs, int voff, int soff, int ldsaddr) {
  asm volatile("s_mov_b32 m0, %0\n\ts_mov_b32 exec_hi, 0xffffff\n\tbuffer_load_dwordx4 %1, %2, %3 offen " OPOL " lds\n\ts_mov_b32 exec_hi, -1"
               :: "s"(ldsaddr), "v"(voff), "s"(rs), "s"(soff) : "memory");
}
#define WAITV(N) do { SBAR(); asm volatile("s_waitcnt vmcnt(" #N ")" ::: "memory"); SBAR(); } while (0)
template <int NF> struct PFrag { bf16x4 lo[NF], hi[NF]; bf16x8 b[4]; };
template <int RS> DI int pabase(int f0) {
  const int lane = lane_id(), g = lane >> 4, q = (lane & 15) >> 2, p = lane & 3;
  return (16 * (g >> 1) + 4 * (g & 1) + q) * RS + f0 * 32 + p * 8;
}
DI int pbbase(int bimg) { const int lane = lane_id(), g = lane >> 4, c = lane & 15; return bimg + g * 256 + ((c ^ g) << 4); }
DI int pbaddr(int bb, int ks) { return (bb ^ ((ks & 1) << 6)) + ks * 4096; }
template <int NF, int RS, int K> DI void pread(PFrag<NF>& f, LAS char* smem, int aa, int ba) {
  if constexpr (K < 4) f.b[K] = asbf(lds_r128(smem + ba + K * 1024));
  else if constexpr (K < 4 + 2 * NF) {
    if constexpr ((K & 1) == 0) f.lo[(K - 4) >> 1] = lds_tr(smem + aa + ((K - 4) >> 1) * 32);
    else f.hi[(K - 4) >> 1] = lds_tr(smem + aa + ((K - 4) >> 1) * 32 + 8 * RS);
  }
}
template <int NF, int RS>
DI void pstep(f32x4 (&acc)[NF][4], const PFrag<NF>& cur, PFrag<NF>& nxt, LAS char* smem, int aa, int ba) {
  static_for<NF * 4>([&](auto nc) {
    constexpr int n = decltype(nc)::value, i = n >> 2, rt = n & 3;
    const bf16x8 a = __builtin_shufflevector(cur.lo[i], cur.hi[i], 0, 1, 2, 3, 4, 5, 6, 7);
    acc[i][rt] = MFMA(a, cur.b[rt], acc[i][rt]);
    pread<NF, RS, n>(nxt, smem, aa, ba);
    SBAR();
  });
}
template <int NF> DI u32x4 ppack(const f32x4 (&acc)[NF][4], int i, int rp) {
  auto lo = __builtin_amdgcn_permlane16_swap(pk2(acc[i][2 * rp][0], acc[i][2 * rp][1]), pk2(acc[i][2 * rp + 1][0], acc[i][2 * rp + 1][1]), false, false);
  auto hi = __builtin_amdgcn_permlane16_swap(pk2(acc[i][2 * rp][2], acc[i][2 * rp][3]), pk2(acc[i][2 * rp + 1][2], acc[i][2 * rp + 1][3]), false, false);
  return u32x4{lo[0], hi[0], lo[1], hi[1]};
}
DI void q_handover(const f32x4 (&acc)[8][4], LAS char* smem, int w) {
  LAS char* base = smem + ((w & 1) ? LDS_QP : LDS_QT) + (w >> 1) * 16384 + lane_id() * 16;
#pragma unroll
  for (int i = 0; i < 8; ++i)
#pragma unroll
    for (int rp = 0; rp < 2; ++rp) lds_w128(base + rp * 8192 + i * 1024, ppack<8>(acc, i, rp));
}
template <int NF> DI void publish(const f32x4 (&acc)[NF][4], char* ws, int lid, int f0, bool ok) {
  const int lane = lane_id(), g = lane >> 4, c = lane & 15;
  const rsrc_t rp = mk_rsrc(ws + (size_t)(lid >> 3) * 64 * SLAB, 64 * SLAB);
  const int voff = (((g & 1) * 2 + (c >> 3)) * 8 + (lid & 7)) * SLAB + f0 * 256 + (g >> 1) * 128 + (c & 7) * 16;
#pragma unroll
  for (int i = 0; i < NF; ++i)
#pragma unroll
    for (int r2 = 0; r2 < 2; ++r2) __builtin_amdgcn_raw_buffer_store_b128(ppack<NF>(acc, i, r2), rp, voff, r2 * 32 * SLAB + i * 256, AUX_SC1);
  asm volatile("s_waitcnt vmcnt(0)" ::: "memory");
  for (unsigned spins = 0; !ok && spins < SPIN_CAP; ++spins) {
    if (lane < 8) hs_echo(ws, lid, lane);
    const unsigned cv = __hip_atomic_load(ctl_chal(ws, (lid & ~7) + (lane & 7), lid & 7), RLX_AGENT);
    const unsigned rv = __hip_atomic_load(ctl_resp(ws, lid, lane & 7), RLX_AGENT);
    ok = __all(cv == rv);
    __builtin_amdgcn_s_sleep(8);
  }
  if (lane < 8) __hip_atomic_fetch_add((unsigned*)(ws + CTL_OFF + (size_t)((lid & ~7) + lane) * 512), 1u, RLX_AGENT);
}
constexpr int QRS = 1056, ORS = 928;
DI int qslot(int i) { return i == 0 ? 92160 : (i == 1 ? 126976 : 0); }
DI int oslot(int i) { return i == 0 ? LDS_K : (i == 1 ? LDS_K + 32768 : LDS_V + 32768); }
DI int obaddr(int bb, int ks) { return (bb ^ ((ks & 1) << 6)) + ks * 4096 + (ks >> 3) * 32768; }

__global__ __launch_bounds__(512, 2) void gqa_fused(const char* __restrict__ xA, const char* __restrict__ kA, const char* __restrict__ vA,
                                                    const char* __restrict__ wqA, const char* __restrict__ woA,
                                                    const char* __restrict__ xB, const char* __restrict__ kB, const char* __restrict__ vB,
                                                    const char* __restrict__ wqB, const char* __restrict__ woB,
                                                    char* __restrict__ out, char* ws) {
  __shared__ __attribute__((aligned(1024))) char smem_[LDS_TOTAL];
  LAS char* smem = (LAS char*)smem_;
  const int wid = __builtin_amdgcn_readfirstlane(threadIdx.x >> 6);
  const int bid = blockIdx.x, blk = (bid >> 2) & 1, kvh = (bid & 3) * 2 + (bid >> 7), bb = (bid >> 3) & 15;
  const int lid = (blk * 16 + bb) * 8 + kvh;
  const int kq0 = bb < 14 ? bb : bb - 14;
#define KQ(S) ((kq0 + (S)) >= 14 ? kq0 + (S) - 14 : kq0 + (S))
#define KO(S) (((S) + OROT * bb) & 15)
  STAMP_DECL;
  STAMP(0); STAMPC(13);
  const rsrc_t rc = mk_rsrc(ws + CTL_OFF + (size_t)(lid & ~7) * 512, 8 * 512);
#define HS_REQ() const unsigned hsc_ = __builtin_amdgcn_raw_buffer_load_b32(rc, (lane_id() & 7) * 512 + 128 + (lid & 7) * 4, 0, AUX_SC1), \
                                hsr_ = __builtin_amdgcn_raw_buffer_load_b32(rc, (lid & 7) * 512 + 256 + (lane_id() & 7) * 4, 0, AUX_SC1)
#define HS_OK() __all(hsc_ == hsr_)

  if (wid >= 4) {
    int lane = lane_id();
    const int h = wid - 4;
    bool hs = h == 0 && lane < 8;
    __builtin_amdgcn_raw_buffer_store_b32(0u, rc, hs && lane == 0 ? (lid & 7) * 512 : OOB, 0, AUX_SC1);
    const unsigned hs_r = __builtin_amdgcn_raw_buffer_load_b32(rc, hs ? (lid & 7) * 512 + 256 + lane * 4 : OOB, 0, AUX_SC1);
    const rsrc_t rwq = mk_rsrc((blk ? wqB : wqA) + kvh * 1024, W_BYTES - kvh * 1024);
    const rsrc_t rwo = mk_rsrc((blk ? woB : woA) + (size_t)kvh * 512 * DM * 2, 512 * DM * 2);
    const rsrc_t rk = mk_rsrc((blk ? kB : kA) + (size_t)(bb * HKV + kvh) * KV_BYTES, KV_BYTES);
    const rsrc_t rv = mk_rsrc((blk ? vB : vA) + (size_t)(bb * HKV + kvh) * KV_BYTES, KV_BYTES);
#define voff l16
#define wqv l16
#define wov l16
    int l16 = lane * 16;
    u32x4 R0[8], R1[8], SA[8];
    const int irow = 16 * (h >> 1) + 4 * (h & 1);
#define QDMA(S) do { _Pragma("unroll") for (int j = 0; j < 8; ++j) \
      dma1(rwq, wqv, (KQ(S) * 32 + 8 * h + j) * (NQ * 2), qslot((S) % 3) + (irow + 8 * (j >> 2) + (j & 3)) * QRS); SBAR(); } while (0)
#define ODMA(S) ODMAX(dma56, S)
#define ODMAX(FN, S) do { { _Pragma("unroll") for (int j = 0; j < 8; ++j) \
      FN(rwo, wov, (KO(S) * 32 + 16 * (h & 1) + 4 * (h >> 1) + 8 * (j >> 2) + (j & 3)) * (DM * 2), oslot((S) % 3) + (irow + 8 * (j >> 2) + (j & 3)) * ORS); } SBAR(); } while (0)
#define LOADU(SET, RS, TILE) do { ld8(SET, RS, voff, (TILE) * 32768 + h * 1024, 4096, 16384); SBAR(); } while (0)
    QDMA(0); QDMA(1); QDMA(2);
    WAITV(16);
    WG_BARRIER();
    STAMP(1);
    WAITV(8);
#pragma unroll 1
    for (int s = 0; s < 10; ++s) {
      WG_BARRIER();
      QDMA(s + 3);
      WAITV(8);
    }
    WG_BARRIER();
    QDMA(13);
    LOADU(R0, rk, 0);
    WAITV(16);
    WG_BARRIER();
    WAITV(8);
    LOADU(R1, rk, 1);
    WG_BARRIER();
    __builtin_amdgcn_raw_buffer_store_b32(hs_r + 1u, rc, hs ? lane * 512 + 128 + (lid & 7) * 4 : OOB, 0, AUX_SC1);
    WG_BARRIER();
    LOADU(SA, rv, 0);
    STAMP(2);
    lane = lane_id(); l16 = lane * 16; hs = h == 0 && lane < 8;
    const int hi = lane >> 5, ch = lane & 31;
    const int row0 = 2 * h + hi;
    const int kw0 = LDS_K + row0 * 512 + ((ch ^ row0) << 4), kw1 = LDS_K + (row0 + 8) * 512 + ((ch ^ row0 ^ 8) << 4);
    const int vw0 = LDS_V + row0 * 512 + ((ch ^ ((row0 & 3) << 2)) << 4);
#define WRITEK(SET, BUF) do { _Pragma("unroll") for (int i = 0; i < 8; ++i) \
      lds_w128(smem + ((i & 1) ? kw1 : kw0) + (BUF) + (i >> 1) * 8192, SET[i]); SBAR(); } while (0)
#define WRITEV(SET, BUF) do { _Pragma("unroll") for (int i = 0; i < 8; ++i) \
      lds_w128(smem + vw0 + (BUF) + i * 4096, SET[i]); SBAR(); } while (0)
    const int kv0 = row0 * 512 + ((ch ^ row0) << 4), kv1 = row0 * 512 + ((ch ^ row0 ^ 8) << 4), vv0 = row0 * 512 + ((ch ^ ((row0 & 3) << 2)) << 4);
#define KVO(x) (x)
#define KDMA(TILE, HALF) do { _Pragma("unroll") for (int i = 0; i < 4; ++i) \
      dmakv(rk, KVO((i & 1) ? kv1 : kv0), (TILE) * 32768 + (HALF) * 16384 + i * 4096, LDS_K + ((TILE) & 1) * 32768 + (HALF) * 16384 + i * 4096 + h * 1024); SBAR(); } while (0)
#define VDMA(TILE, HALF) do { _Pragma("unroll") for (int i = 0; i < 4; ++i) \
      dmakv(rv, KVO(vv0), (TILE) * 32768 + (HALF) * 16384 + i * 4096, LDS_V + ((TILE) & 1) * 32768 + (HALF) * 16384 + i * 4096 + h * 1024); SBAR(); } while (0)
    WRITEK(R0, 0);
    WG_BARRIER();
    WRITEK(R1, 32768);
    WG_BARRIER();
    WRITEV(SA, 0);
    STAMP(4);
    VDMA(1, 0); KDMA(2, 0);
    WG_BARRIER();
    WG_BARRIER();
    VDMA(1, 1); KDMA(2, 1);
    WAITV(8);
    const unsigned hs_c0 = 0; (void)hs_c0;
#pragma unroll 1
    for (int n = 1; n < 27; ++n) {
      WG_BARRIER();
      const int tv = (n + 3) >> 1, hf = (n + 3) & 1;
      VDMA(tv, hf); KDMA(tv + 1, hf);
      if (n == 20 && hs) hs_echo(ws, lid, lane);
      WAITV(8);
      STAMP_IF(n == 8, 5); STAMP_IF(n == 16, 6); STAMP_IF(n == 24, 7);
    }
    WG_BARRIER();
    VDMA(15, 0);
    WAITV(4);
    WG_BARRIER();
    VDMA(15, 1);
    ODMA(0);
    WAITV(12);
    WG_BARRIER();
    WAITV(8);
#undef KDMA
#undef VDMA
#undef KVO
    STAMP(8);
    WG_BARRIER();
    ODMA(1);
    WAITV(8);
    WG_BARRIER();
    STAMP(10);
    ODMA(2);
    WAITV(8);
#pragma unroll 1
    for (int s = 0; s < 13; ++s) {
      WG_BARRIER();
      ODMA(s + 3);
      WAITV(8);
    }
    WG_BARRIER();
    WAITV(0);
    WG_BARRIER();
    WG_BARRIER();
    STAMP(11);
    if (h == 0) {
      const unsigned* cnt = (const unsigned*)(ws + CTL_OFF + (size_t)lid * 512);
      for (unsigned spins = 0; spins < SPIN_CAP; ++spins) {
        if (__hip_atomic_load(cnt, RLX_AGENT) >= 32u) break;
        if ((spins & 63u) == 63u) { const int l = lane_id(); if (l < 8) hs_echo(ws, lid, l); }
        __builtin_amdgcn_s_sleep(2);
      }
    }
#undef QDMA
#undef ODMA
#undef LOADU
#undef WRITEK
#undef WRITEV
#undef voff
#undef wqv
#undef wov
  } else {
    const unsigned c0_ = (unsigned)__builtin_amdgcn_s_memtime(), r0_ = (unsigned)__builtin_amdgcn_s_memrealtime();
    {
      const int lane = lane_id();
      const rsrc_t rx = mk_rsrc((blk ? xB : xA) + (size_t)bb * SQ * DM * 2, SQ * DM * 2);
      const int xp = ((wid + (kvh >> 1)) & 3) * 14;
      u32x4 xr[14];
#pragma unroll
      for (int f = 0; f < 14; ++f) xr[f] = __builtin_amdgcn_raw_buffer_load_b128(rx, lane * 16, (xp + f) * 1024, XAUX);
      SBAR();
#pragma unroll
      for (int f = 0; f < 14; ++f) {
        const unsigned idx = (xp + f) * 64 + lane, row = idx / 56u, q = idx - row * 56u;
        lds_w128(smem + LDS_X + (q >> 2) * 4096 + (row >> 4) * 1024 + (q & 3) * 256 + (((row & 15) ^ (q & 7)) << 4), xr[f]);
      }
    }
    WG_BARRIER();
    const int fastclk = __builtin_amdgcn_readfirstlane((int)(((unsigned)__builtin_amdgcn_s_memtime() - c0_) * 16u - ((unsigned)__builtin_amdgcn_s_memrealtime() - r0_) * (unsigned)AP_TH));
    __builtin_amdgcn_s_waitcnt(0xC07F); SBAR();
    STAMP(1);
    {
      f32x4 acc[8][4]; PFrag<8> F[2];
#pragma unroll
      for (int i = 0; i < 8; ++i)
#pragma unroll
        for (int rt = 0; rt < 4; ++rt) acc[i][rt] = f32x4{0.f, 0.f, 0.f, 0.f};
      const int ab = pabase<QRS>(wid * 8), bbs = pbbase(LDS_X);
      { const int aa = ab + qslot(0), ba = pbaddr(bbs, kq0); static_for<20>([&](auto kc) { pread<8, QRS, decltype(kc)::value>(F[0], smem, aa, ba); }); }
      int sl = 1;
#pragma unroll 1
      for (int s = 0; s < 14; s += 2) {
        WG_BARRIER();
        pstep<8, QRS>(acc, F[0], F[1], smem, ab + qslot(sl), pbaddr(bbs, KQ(s + 1)));
        sl = sl == 2 ? 0 : sl + 1;
        WG_BARRIER();
        pstep<8, QRS>(acc, F[1], F[0], smem, ab + qslot(sl), pbaddr(bbs, KQ(s + 2)));
        sl = sl == 2 ? 0 : sl + 1;
      }
      q_handover(acc, smem, wid);
    }
    STAMP(2);
    {
      const int w = wid, hh = w >> 1, rh = w & 1;
      const int lane = lane_id(), h = lane >> 5, r = lane & 31;
      f32x16 o[8];
#pragma unroll
      for (int dt = 0; dt < 8; ++dt)
#pragma unroll
        for (int i = 0; i < 16; ++i) o[dt][i] = 0.f;
      float mc = 0.f, lsum = 0.f;
      f32x16 s0, s1;
      u32x4 p0[2], p1[2];
      u32x4 qown[8];
      int kb = LDS_K + r * 512 + ((h ^ (r & 15)) << 4);
      int vb = LDS_V + (4 * h + ((lane & 15) >> 2)) * 512 + (((lane & 15) >> 2) << 6) + ((lane >> 4) & 1) * 32 + (lane & 3) * 8;
      LAS char* qp = smem + LDS_QP + w * 8192 + lane * 16;
      constexpr int PD = 4, RN = 6;
      bf16x4 vl[RN], vh[RN]; bf16x8 kf[RN], qf[2];
      float t0, t1, t2, t3, t4, mx;
#define FETCH(N) do { constexpr int n_ = (N), blk_ = n_ >> 4, j_ = n_ & 15; \
        if (blk_ & 1) kf[n_ % RN] = asbf(lds_r128(smem + (kb ^ ((j_ >> 1) << 5)) + (j_ & 1) * 256 + (blk_ >> 1) * 16384)); \
        else { LAS char* a_ = smem + (vb ^ ((j_ & 3) << 6)) + ((j_ >> 2) & 1) * 256 + (blk_ + (j_ >> 3)) * 8192; vl[n_ % RN] = lds_tr(a_); vh[n_ % RN] = lds_tr(a_ + 4096); } } while (0)
#define FETCHQ(J) qf[((J) >> 1) & 1] = asbf(lds_r128(qp + ((J) >> 1) * 1024))
#define JOB_AB(X, j) do { \
        if ((j) < 4) { _Pragma("unroll") for (int i_ = 0; i_ < 4; ++i_) X[4 * (j) + i_] = fmaf(X[4 * (j) + i_], SCL, -mc); } \
        else if ((j) == 4) { t0 = max3(X[0], X[1], X[2]); t1 = max3(X[3], X[4], X[5]); t2 = max3(X[6], X[7], X[8]); } \
        else if ((j) == 5) { t3 = max3(X[9], X[10], X[11]); t4 = max3(X[12], X[13], X[14]); t0 = max3(t0, t1, X[15]); } \
        else if ((j) == 6) { t0 = fmaxf(t0, max3(t2, t3, t4)); } \
        else if ((j) == 7) { mx = hmax(t0); } } while (0)
#define JOB_EXP(X, P, j) do { X[j] = __builtin_amdgcn_exp2f(X[j]); \
        if ((j) > 0) lsum += X[(j) - 1]; \
        if ((j) >= 2 && !((j) & 1)) P[((j) - 2) >> 3][(((j) - 2) >> 1) & 3] = pk2(X[(j) - 2], X[(j) - 1]); \
        if ((j) == 15) { lsum += X[15]; P[1][3] = pk2(X[14], X[15]); } } while (0)
#define RESCALE(X) do { if (__any(mx > THR)) { const float d_ = fmaxf(mx, 0.f), al_ = __builtin_amdgcn_exp2f(-d_); mc += d_; lsum *= al_; \
          _Pragma("unroll") for (int i_ = 0; i_ < 16; ++i_) X[i_] -= d_; \
          _Pragma("unroll") for (int dt = 0; dt < 8; ++dt) o[dt] *= al_; } SBAR(); } while (0)
#define FETCH_AHEAD(B, j) switch (j) { \
            case 0: FETCH((B) * 16 + 0 + PD); break; case 1: FETCH((B) * 16 + 1 + PD); break; case 2: FETCH((B) * 16 + 2 + PD); break; case 3: FETCH((B) * 16 + 3 + PD); break; \
            case 4: FETCH((B) * 16 + 4 + PD); break; case 5: FETCH((B) * 16 + 5 + PD); break; case 6: FETCH((B) * 16 + 6 + PD); break; case 7: FETCH((B) * 16 + 7 + PD); break; \
            case 8: FETCH((B) * 16 + 8 + PD); break; case 9: FETCH((B) * 16 + 9 + PD); break; case 10: FETCH((B) * 16 + 10 + PD); break; case 11: FETCH((B) * 16 + 11 + PD); break; \
            case 12: FETCH(((B) * 16 + 12 + PD) & 63); break; case 13: FETCH(((B) * 16 + 13 + PD) & 63); break; case 14: FETCH(((B) * 16 + 14 + PD) & 63); break; default: FETCH(((B) * 16 + 15 + PD) & 63); break; }
#define PVBLOCK(B, P, X, LAST, PN) do { _Pragma("unroll") for (int j = 0; j < 16; ++j) { \
          const bf16x8 a_ = __builtin_shufflevector(vl[((B) * 16 + j) % RN], vh[((B) * 16 + j) % RN], 0, 1, 2, 3, 4, 5, 6, 7); \
          o[j & 7] = MFMA32(a_, asbf(P[j >> 3]), o[j & 7]); \
          if (j + PD < 16 || !(LAST)) { FETCH_AHEAD(B, j) } \
          if (!(LAST) && j == 14) FETCHQ(1); \
          if ((LAST) != 2) JOB_AB(X, j); \
          PACE(PN); \
          SBAR(); } } while (0)
#define QKBLOCK(B, S, NEXT, JOB, X, P, PN) do { _Pragma("unroll") for (int j = 0; j < 16; ++j) { \
          const bf16x8 q_ = (j & 1) ? qf[(j >> 1) & 1] : asbf(qown[j >> 1]); \
          if (j == 0) { f32x16 z_; _Pragma("unroll") for (int i_ = 0; i_ < 16; ++i_) z_[i_] = 0.f; S = MFMA32(kf[((B) * 16 + j) % RN], q_, z_); } \
          else S = MFMA32(kf[((B) * 16 + j) % RN], q_, S); \
          if (j + PD < 16 || (NEXT)) { FETCH_AHEAD(B, j) } \
          if (!(j & 1) && j + 3 < 16) FETCHQ(j + 3); \
          if (JOB) JOB_EXP(X, P, j); \
          PACE(PN); \
          SBAR(); } } while (0)
#define FETCH4(N) do { FETCH(N); FETCH((N) + 1); FETCH((N) + 2); FETCH((N) + 3); } while (0)
      static_assert(PD == 4, "FETCH4");
#define RAWBAR() do { SBAR(); asm volatile("s_barrier" ::: "memory"); SBAR(); } while (0)
      WG_BARRIER();
      STAMP(3);
#pragma unroll
      for (int f = 0; f < 8; ++f) qown[f] = lds_r128(smem + LDS_QT + w * 8192 + f * 1024 + lane * 16);
      FETCH4(16); FETCHQ(1); SBAR();
      QKBLOCK(1, s0, 0, 0, s0, p0, PACEN);
      FETCH4(48); FETCHQ(1); SBAR();
#pragma unroll
      for (int j = 0; j < 8; ++j) JOB_AB(s0, j);
      mc = mx;
#pragma unroll
      for (int i = 0; i < 16; ++i) s0[i] -= mx;
      RAWBAR();
      QKBLOCK(3, s1, 0, 1, s0, p0, PACEN);
      kb ^= 32768;
      RAWBAR();
      FETCH4(0); SBAR();
#define LSTAMP() do { STAMP_IF(t == 0, 4); STAMP_IF(t == 4, 5); STAMP_IF(t == 8, 6); STAMP_IF(t == 12, 7); } while (0)
#define ATT_LOOP(PN, T0, T1) _Pragma("unroll 1") for (int t = (T0); t < (T1); ++t) { \
        RAWBAR();     \
        LSTAMP(); \
        PVBLOCK(0, p0, s1, 0, PN); \
        RESCALE(s1); \
        QKBLOCK(1, s0, 1, 1, s1, p1, PN); \
        RAWBAR();     \
        PVBLOCK(2, p1, s0, 0, PN); \
        vb ^= 32768; \
        RESCALE(s0); \
        QKBLOCK(3, s1, 1, 1, s0, p0, PN); \
        kb ^= 32768; }
      const int nslow = fastclk > 0 ? AP_N : 0;
      ATT_LOOP(AP_P, 0, nslow)
      ATT_LOOP(AP_P2, nslow, 15)
#undef ATT_LOOP
#undef LSTAMP
#pragma unroll
      for (int j = 0; j < 8; ++j) JOB_AB(s1, j);
      if (__any(mx > THR)) {
        const float d_ = fmaxf(mx, 0.f), al_ = __builtin_amdgcn_exp2f(-d_);
        mc += d_; lsum *= al_;
#pragma unroll
        for (int i = 0; i < 16; ++i) s1[i] -= d_;
#pragma unroll
        for (int dt = 0; dt < 8; ++dt) o[dt] *= al_;
#pragma unroll
        for (int k = 0; k < 2; ++k)
#pragma unroll
          for (int i = 0; i < 4; ++i) p0[k][i] = pk2(__uint_as_float(p0[k][i] << 16) * al_, __uint_as_float(p0[k][i] & 0xffff0000u) * al_);
      }
      SBAR();
#pragma unroll
      for (int j = 0; j < 16; ++j) JOB_EXP(s1, p1, j);
      const float iv = 1.f / hsum(lsum);
      PVBLOCK(0, p0, s1, 2, -1);
      RAWBAR();
      STAMP(8);
      FETCH4(32); SBAR();
      PVBLOCK(2, p1, s0, 2, -1);
#undef FETCH
#undef FETCHQ
#undef FETCH4
#undef FETCH_AHEAD
#undef JOB_AB
#undef JOB_EXP
#undef RESCALE
#undef PVBLOCK
#undef QKBLOCK
      STAMP(9);
      const int lane2 = lane_id(), h2 = lane2 >> 5, r2 = lane2 & 31;
      LAS char* ow = smem + LDS_V + hh * 65536 + (rh * 2 + (r2 >> 4)) * 1024 + 2 * h2 * 256;
#pragma unroll
      for (int dt = 0; dt < 8; ++dt)
#pragma unroll
        for (int pp = 0; pp < 2; ++pp)
          lds_w128(ow + dt * 4096 + pp * 256 + (((r2 & 15) ^ (2 * h2 + pp) ^ ((dt & 1) * 4)) << 4),
                   u32x4{pk2(o[dt][8 * pp] * iv, o[dt][8 * pp + 1] * iv), pk2(o[dt][8 * pp + 2] * iv, o[dt][8 * pp + 3] * iv),
                         pk2(o[dt][8 * pp + 4] * iv, o[dt][8 * pp + 5] * iv), pk2(o[dt][8 * pp + 6] * iv, o[dt][8 * pp + 7] * iv)});
    }
    WG_BARRIER();
    STAMP(10);
    {
      HS_REQ();
      f32x4 acc[7][4]; PFrag<7> F[2];
#pragma unroll
      for (int i = 0; i < 7; ++i)
#pragma unroll
        for (int rt = 0; rt < 4; ++rt) acc[i][rt] = f32x4{0.f, 0.f, 0.f, 0.f};
      const int ab = pabase<ORS>(wid * 7), bbs = pbbase(LDS_V);
      { const int aa = ab + oslot(0), ba = obaddr(bbs, KO(0)); static_for<18>([&](auto kc) { pread<7, ORS, decltype(kc)::value>(F[0], smem, aa, ba); }); }
      int sl = 1;
#pragma unroll 1
      for (int s = 0; s < 16; s += 2) {
        WG_BARRIER();
        pstep<7, ORS>(acc, F[0], F[1], smem, ab + oslot(sl), obaddr(bbs, KO(s + 1)));
        sl = sl == 2 ? 0 : sl + 1;
        WG_BARRIER();
        pstep<7, ORS>(acc, F[1], F[0], smem, ab + oslot(sl), obaddr(bbs, KO(s + 2)));
        sl = sl == 2 ? 0 : sl + 1;
      }
      STAMP(11);
      publish<7>(acc, ws, lid, wid * 7, HS_OK());
    }
  }
  WG_BARRIER();
  STAMP(15);
  if (wid < 7) {
    const int lane = lane_id();
    const rsrc_t rp = mk_rsrc(ws + ((size_t)(lid >> 3) * 64 + kvh * 8) * SLAB, 8 * SLAB);
    u32x4 v[8];
#pragma unroll
    for (int s = 0; s < 8; ++s) v[s] = __builtin_amdgcn_raw_buffer_load_b128(rp, wid * 1024 + lane * 16, s * SLAB, AUX_SC1);
    float a[8];
#pragma unroll
    for (int k = 0; k < 4; ++k) { a[2 * k] = __uint_as_float(v[0][k] << 16); a[2 * k + 1] = __uint_as_float(v[0][k] & 0xffff0000u); }
#pragma unroll
    for (int s = 1; s < 8; ++s)
#pragma unroll
      for (int k = 0; k < 4; ++k) { a[2 * k] += __uint_as_float(v[s][k] << 16); a[2 * k + 1] += __uint_as_float(v[s][k] & 0xffff0000u); }
    const rsrc_t ro = mk_rsrc(out + (size_t)(bb * SQ + kvh * 8) * (2 * DM) * 2, 8 * 2 * DM * 2);
    __builtin_amdgcn_raw_buffer_store_b128(u32x4{pk2(a[0], a[1]), pk2(a[2], a[3]), pk2(a[4], a[5]), pk2(a[6], a[7])}, ro,
                                           ((lane & 7) * (2 * DM) + blk * DM + wid * 64 + (lane >> 3) * 8) * 2, 0, OUT_AUX);
  }
  STAMP(12); STAMPC(14); STAMP_OUT();
}
}

extern "C" void kernel_launch(void* const* d_in, const int* in_sizes, int n_in, void* d_out, int out_size, void* d_ws, size_t ws_size,
                              hipStream_t stream) {
  (void)in_sizes; (void)n_in; (void)out_size; (void)ws_size;
  gqa_fused<<<256, 512, 0, stream>>>((const char*)d_in[0], (const char*)d_in[1], (const char*)d_in[2], (const char*)d_in[3], (const char*)d_in[4],
                                     (const char*)d_in[5], (const char*)d_in[6], (const char*)d_in[7], (const char*)d_in[8], (const char*)d_in[9],
                                     (char*)d_out, (char*)d_ws);
}
```

```cpp
#define PRETOUCH
#define AP_TH 328
#define AP_P 7
#include <hip/hip_runtime.h>
#include <hip/hip_bf16.h>
#include <utility>
#include <type_traits>

#define LAS __attribute__((address_space(3)))
#define DI __device__ __forceinline__
typedef __attribute__((ext_vector_type(8))) __bf16 bf16x8;
typedef __attribute__((ext_vector_type(4))) __bf16 bf16x4;
typedef __attribute__((ext_vector_type(2))) __bf16 bf16x2;
typedef __attribute__((ext_vector_type(16))) float f32x16;
typedef __attribute__((ext_vector_type(4))) float f32x4;
typedef __attribute__((ext_vector_type(2))) float f32x2;
typedef __attribute__((ext_vector_type(4))) unsigned u32x4;

namespace {
constexpr int SQ = 64, DM = 448, HKV = 8, SK = 1024, DH = 256;
constexpr int NQ = 4096;
constexpr int KV_BYTES = SK * DH * 2;
constexpr int W_BYTES = DM * NQ * 2;
constexpr int LDS_K = 0, LDS_V = 65536, LDS_QP = 131072, LDS_TOTAL = 163840;
constexpr int LDS_X = 34816, LDS_QT = LDS_V + 32768;
constexpr int XRS = 912;
constexpr int SLAB = 7168;
constexpr size_t CTL_OFF = (size_t)16 << 20;
constexpr float SCL = 0.0625f * 1.4426950408889634f;
constexpr float THR = 8.f;
constexpr int AUX_NT = 2, AUX_SC1 = 16;
#define AUX_W 2
#define OPOL ""
#define QPOL "nt"
#define WARM_AUX 2
#define OROT 1
#define XAUX 2
#define OUT_AUX 0
constexpr int OOB = 0x40000000;
constexpr unsigned SPIN_CAP = 1u << 21;

#define SBAR() __builtin_amdgcn_sched_barrier(0)
#define WG_BARRIER() do { SBAR(); asm volatile("s_waitcnt lgkmcnt(0)\n\ts_barrier" ::: "memory"); SBAR(); } while (0)
#define STAMP(idx) do {} while (0)
#define STAMP_IF(c, idx) do {} while (0)
#define STAMPC(idx) do {} while (0)
#define STAMP_DECL do {} while (0)
#define STAMP_OUT() do {} while (0)
#define PACE(PN) do { if ((PN) >= 0) asm volatile("s_nop %0" :: "i"((PN) < 0 ? 0 : (PN))); } while (0)
#define AP_N 15
#define AP_P2 -1
#define PACEN -1
#define NAP() do {} while (0)
#define MFMA(a, b, c) __builtin_amdgcn_mfma_f32_16x16x32_bf16((a), (b), (c), 0, 0, 0)
#define MFMA32(a, b, c) __builtin_amdgcn_mfma_f32_32x32x16_bf16((a), (b), (c), 0, 0, 0)
#define RLX_AGENT __ATOMIC_RELAXED, __HIP_MEMORY_SCOPE_AGENT

DI unsigned pk2(float a, float b) { f32x2 f = {a, b}; bf16x2 r = __builtin_convertvector(f, bf16x2); return __builtin_bit_cast(unsigned, r); }
DI bf16x8 asbf(u32x4 v) { return __builtin_bit_cast(bf16x8, v); }
DI u32x4 lds_r128(LAS char* p) { return *(LAS u32x4*)p; }
DI void lds_w128(LAS char* p, u32x4 v) { *(LAS u32x4*)p = v; }
DI bf16x4 lds_tr(LAS char* p) { return __builtin_amdgcn_ds_read_tr16_b64_v4bf16((LAS bf16x4*)p); }
DI unsigned perm2(int e, unsigned hi, unsigned lo) { return __builtin_amdgcn_perm(hi, lo, e ? 0x07060302u : 0x05040100u); }
DI float max3(float a, float b, float c) { return fmaxf(fmaxf(a, b), c); }
DI float hmax(float v) {
  auto b = __builtin_amdgcn_permlane32_swap(__float_as_uint(v), __float_as_uint(v), false, false);
  return fmaxf(__uint_as_float(b[0]), __uint_as_float(b[1]));
}
DI float hsum(float v) {
  auto b = __builtin_amdgcn_permlane32_swap(__float_as_uint(v), __float_as_uint(v), false, false);
  return __uint_as_float(b[0]) + __uint_as_float(b[1]);
}
DI int lane_id() {
  int l; asm volatile("v_mbcnt_lo_u32_b32 %0, -1, 0\n\tv_mbcnt_hi_u32_b32 %0, -1, %0" : "=v"(l)); return l; }
typedef __amdgpu_buffer_rsrc_t rsrc_t;
DI rsrc_t mk_rsrc(const void* p, int bytes) { return __builtin_amdgcn_make_buffer_rsrc((void*)p, 0, bytes, 0x00020000); }
template <class F, int... I> DI void static_for_(F&& f, std::integer_sequence<int, I...>) { (f(std::integral_constant<int, I>{}), ...); }
template <int N, class F> DI void static_for(F&& f) { static_for_(f, std::make_integer_sequence<int, N>{}); }

DI unsigned* ctl_chal(char* ws, int wg, int slot) { return (unsigned*)(ws + CTL_OFF + (size_t)wg * 512 + 128 + slot * 4); }
DI unsigned* ctl_resp(char* ws, int wg, int slot) { return (unsigned*)(ws + CTL_OFF + (size_t)wg * 512 + 256 + slot * 4); }
DI void hs_echo(char* ws, int lid, int lane) {
  const unsigned cv = __hip_atomic_load(ctl_chal(ws, lid, lane), RLX_AGENT);
  const unsigned rv = __hip_atomic_load(ctl_resp(ws, (lid & ~7) + lane, lid & 7), RLX_AGENT);
  if (cv != rv) __hip_atomic_store(ctl_resp(ws, (lid & ~7) + lane, lid & 7), cv, RLX_AGENT);
}
DI u32x4 ld1(rsrc_t rs, int voff, int soff) { return __builtin_amdgcn_raw_buffer_load_b128(rs, voff, soff, AUX_NT); }
DI void ld8(u32x4 (&r)[8], rsrc_t rs, int voff, int soff, int sa, int sb) {
#pragma unroll
  for (int j = 0; j < 8; ++j) r[j] = ld1(rs, voff, soff + (j & 3) * sa + (j >> 2) * sb);
}

DI void dma1(rsrc_t rs, int voff, int soff, int ldsaddr) {
  asm volatile("s_mov_b32 m0, %0\n\ts_nop 0\n\tbuffer_load_dwordx4 %1, %2, %3 offen " QPOL " lds" :: "s"(ldsaddr), "v"(voff), "s"(rs), "s"(soff) : "memory");
}
DI void dmakv(rsrc_t rs, int voff, int soff, int ldsaddr) {
  asm volatile("s_mov_b32 m0, %0\n\ts_nop 0\n\tbuffer_load_dwordx4 %1, %2, %3 offen nt lds" :: "s"(ldsaddr), "v"(voff), "s"(rs), "s"(soff) : "memory");
}
DI void dma56nt(rsrc_t rs, int voff, int soff, int ldsaddr) {
  asm volatile("s_mov_b32 m0, %0\n\ts_mov_b32 exec_hi, 0xffffff\n\tbuffer_load_dwordx4 %1, %2, %3 offen nt lds\n\ts_mov_b32 exec_hi, -1"
               :: "s"(ldsaddr), "v"(voff), "s"(rs), "s"(soff) : "memory");
}
DI void dma56(rsrc_t rs, int voff, int soff, int ldsaddr) {
  asm volatile("s_mov_b32 m0, %0\n\ts_mov_b32 exec_hi, 0xffffff\n\tbuffer_load_dwordx4 %1, %2, %3 offen " OPOL " lds\n\ts_mov_b32 exec_hi, -1"
               :: "s"(ldsaddr), "v"(voff), "s"(rs), "s"(soff) : "memory");
}
#define WAITV(N) do { SBAR(); asm volatile("s_waitcnt vmcnt(" #N ")" ::: "memory"); SBAR(); } while (0)
template <int NF> struct PFrag { bf16x4 lo[NF], hi[NF]; bf16x8 b[4]; };
template <int RS> DI int pabase(int f0) {
  const int lane = lane_id(), g = lane >> 4, q = (lane & 15) >> 2, p = lane & 3;
  return (16 * (g >> 1) + 4 * (g & 1) + q) * RS + f0 * 32 + p * 8;
}
DI int pbbase(int bimg) { const int lane = lane_id(), g = lane >> 4, c = lane & 15; return bimg + g * 256 + ((c ^ g) << 4); }
DI int pbaddr(int bb, int ks) { return (bb ^ ((ks & 1) << 6)) + ks * 4096; }
template <int NF, int RS, int K> DI void pread(PFrag<NF>& f, LAS char* smem, int aa, int ba) {
  if constexpr (K < 4) f.b[K] = asbf(lds_r128(smem + ba + K * 1024));
  else if constexpr (K < 4 + 2 * NF) {
    if constexpr ((K & 1) == 0) f.lo[(K - 4) >> 1] = lds_tr(smem + aa + ((K - 4) >> 1) * 32);
    else f.hi[(K - 4) >> 1] = lds_tr(smem + aa + ((K - 4) >> 1) * 32 + 8 * RS);
  }
}
template <int NF, int RS>
DI void pstep(f32x4 (&acc)[NF][4], const PFrag<NF>& cur, PFrag<NF>& nxt, LAS char* smem, int aa, int ba) {
  static_for<NF * 4>([&](auto nc) {
    constexpr int n = decltype(nc)::value, i = n >> 2, rt = n & 3;
    const bf16x8 a = __builtin_shufflevector(cur.lo[i], cur.hi[i], 0, 1, 2, 3, 4, 5, 6, 7);
    acc[i][rt] = MFMA(a, cur.b[rt], acc[i][rt]);
    pread<NF, RS, n>(nxt, smem, aa, ba);
    SBAR();
  });
}
template <int NF> DI u32x4 ppack(const f32x4 (&acc)[NF][4], int i, int rp) {
  auto lo = __builtin_amdgcn_permlane16_swap(pk2(acc[i][2 * rp][0], acc[i][2 * rp][1]), pk2(acc[i][2 * rp + 1][0], acc[i][2 * rp + 1][1]), false, false);
  auto hi = __builtin_amdgcn_permlane16_swap(pk2(acc[i][2 * rp][2], acc[i][2 * rp][3]), pk2(acc[i][2 * rp + 1][2], acc[i][2 * rp + 1][3]), false, false);
  return u32x4{lo[0], hi[0], lo[1], hi[1]};
}
DI void q_handover(const f32x4 (&acc)[8][4], LAS char* smem, int w) {
  LAS char* base = smem + ((w & 1) ? LDS_QP : LDS_QT) + (w >> 1) * 16384 + lane_id() * 16;
#pragma unroll
  for (int i = 0; i < 8; ++i)
#pragma unroll
    for (int rp = 0; rp < 2; ++rp) lds_w128(base + rp * 8192 + i * 1024, ppack<8>(acc, i, rp));
}
template <int NF> DI void publish(const f32x4 (&acc)[NF][4], char* ws, int lid, int f0, bool ok) {
  const int lane = lane_id(), g = lane >> 4, c = lane & 15;
  const rsrc_t rp = mk_rsrc(ws + (size_t)(lid >> 3) * 64 * SLAB, 64 * SLAB);
  const int voff = (((g & 1) * 2 + (c >> 3)) * 8 + (lid & 7)) * SLAB + f0 * 256 + (g >> 1) * 128 + (c & 7) * 16;
#pragma unroll
  for (int i = 0; i < NF; ++i)
#pragma unroll
    for (int r2 = 0; r2 < 2; ++r2) __builtin_amdgcn_raw_buffer_store_b128(ppack<NF>(acc, i, r2), rp, voff, r2 * 32 * SLAB + i * 256, AUX_SC1);
  asm volatile("s_waitcnt vmcnt(0)" ::: "memory");
  for (unsigned spins = 0; !ok && spins < SPIN_CAP; ++spins) {
    if (lane < 8) hs_echo(ws, lid, lane);
    const unsigned cv = __hip_atomic_load(ctl_chal(ws, (lid & ~7) + (lane & 7), lid & 7), RLX_AGENT);
    const unsigned rv = __hip_atomic_load(ctl_resp(ws, lid, lane & 7), RLX_AGENT);
    ok = __all(cv == rv);
    __builtin_amdgcn_s_sleep(8);
  }
  if (lane < 8) __hip_atomic_fetch_add((unsigned*)(ws + CTL_OFF + (size_t)((lid & ~7) + lane) * 512), 1u, RLX_AGENT);
}
constexpr int QRS = 1056, ORS = 928;
DI int qslot(int i) { return i == 0 ? 92160 : (i == 1 ? 126976 : 0); }
DI int oslot(int i) { return i == 0 ? LDS_K : (i == 1 ? LDS_K + 32768 : LDS_V + 32768); }
DI int obaddr(int bb, int ks) { return (bb ^ ((ks & 1) << 6)) + ks * 4096 + (ks >> 3) * 32768; }

__global__ __launch_bounds__(512, 2) void gqa_fused(const char* __restrict__ xA, const char* __restrict__ kA, const char* __restrict__ vA,
                                                    const char* __restrict__ wqA, const char* __restrict__ woA,
                                                    const char* __restrict__ xB, const char* __restrict__ kB, const char* __restrict__ vB,
                                                    const char* __restrict__ wqB, const char* __restrict__ woB,
                                                    char* __restrict__ out, char* ws) {
  __shared__ __attribute__((aligned(1024))) char smem_[LDS_TOTAL];
  LAS char* smem = (LAS char*)smem_;
  const int wid = __builtin_amdgcn_readfirstlane(threadIdx.x >> 6);
  const int bid = blockIdx.x, blk = (bid >> 2) & 1, kvh = (bid & 3) * 2 + (bid >> 7), bb = (bid >> 3) & 15;
  const int lid = (blk * 16 + bb) * 8 + kvh;
  const int kq0 = bb < 14 ? bb : bb - 14;
#define KQ(S) ((kq0 + (S)) >= 14 ? kq0 + (S) - 14 : kq0 + (S))
#define KO(S) (((S) + OROT * bb) & 15)
  STAMP_DECL;
  STAMP(0); STAMPC(13);
  const rsrc_t rc = mk_rsrc(ws + CTL_OFF + (size_t)(lid & ~7) * 512, 8 * 512);
#define HS_REQ() const unsigned hsc_ = __builtin_amdgcn_raw_buffer_load_b32(rc, (lane_id() & 7) * 512 + 128 + (lid & 7) * 4, 0, AUX_SC1), \
                                hsr_ = __builtin_amdgcn_raw_buffer_load_b32(rc, (lid & 7) * 512 + 256 + (lane_id() & 7) * 4, 0, AUX_SC1)
#define HS_OK() __all(hsc_ == hsr_)

  if (wid >= 4) {
    int lane = lane_id();
    const int h = wid - 4;
    bool hs = h == 0 && lane < 8;
    __builtin_amdgcn_raw_buffer_store_b32(0u, rc, hs && lane == 0 ? (lid & 7) * 512 : OOB, 0, AUX_SC1);
    const unsigned hs_r = __builtin_amdgcn_raw_buffer_load_b32(rc, hs ? (lid & 7) * 512 + 256 + lane * 4 : OOB, 0, AUX_SC1);
    const rsrc_t rwq = mk_rsrc((blk ? wqB : wqA) + kvh * 1024, W_BYTES - kvh * 1024);
    const rsrc_t rwo = mk_rsrc((blk ? woB : woA) + (size_t)kvh * 512 * DM * 2, 512 * DM * 2);
    const rsrc_t rk = mk_rsrc((blk ? kB : kA) + (size_t)(bb * HKV + kvh) * KV_BYTES, KV_BYTES);
    const rsrc_t rv = mk_rsrc((blk ? vB : vA) + (size_t)(bb * HKV + kvh) * KV_BYTES, KV_BYTES);
#define voff l16
#define wqv l16
#define wov l16
    int l16 = lane * 16;
    u32x4 R0[8], R1[8], SA[8];
    const int irow = 16 * (h >> 1) + 4 * (h & 1);
#define QDMA(S) do { _Pragma("unroll") for (int j = 0; j < 8; ++j) \
      dma1(rwq, wqv, (KQ(S) * 32 + 8 * h + j) * (NQ * 2), qslot((S) % 3) + (irow + 8 * (j >> 2) + (j & 3)) * QRS); SBAR(); } while (0)
#define ODMA(S) ODMAX(dma56, S)
#define ODMAX(FN, S) do { { _Pragma("unroll") for (int j = 0; j < 8; ++j) \
      FN(rwo, wov, (KO(S) * 32 + 16 * (h & 1) + 4 * (h >> 1) + 8 * (j >> 2) + (j & 3)) * (DM * 2), oslot((S) % 3) + (irow + 8 * (j >> 2) + (j & 3)) * ORS); } SBAR(); } while (0)
#define LOADU(SET, RS, TILE) do { ld8(SET, RS, voff, (TILE) * 32768 + h * 1024, 4096, 16384); SBAR(); } while (0)
    QDMA(0); QDMA(1); QDMA(2);
    WAITV(16);
    WG_BARRIER();
    STAMP(1);
    WAITV(8);
#pragma unroll 1
    for (int s = 0; s < 10; ++s) {
      WG_BARRIER();
      QDMA(s + 3);
      WAITV(8);
    }
    WG_BARRIER();
    QDMA(13);
    LOADU(R0, rk, 0);
    WAITV(16);
    WG_BARRIER();
    WAITV(8);
    LOADU(R1, rk, 1);
    WG_BARRIER();
    __builtin_amdgcn_raw_buffer_store_b32(hs_r + 1u, rc, hs ? lane * 512 + 128 + (lid & 7) * 4 : OOB, 0, AUX_SC1);
    WG_BARRIER();
    LOADU(SA, rv, 0);
    STAMP(2);
    lane = lane_id(); l16 = lane * 16; hs = h == 0 && lane < 8;
    const int hi = lane >> 5, ch = lane & 31;
    const int row0 = 2 * h + hi;
    const int kw0 = LDS_K + row0 * 512 + ((ch ^ row0) << 4), kw1 = LDS_K + (row0 + 8) * 512 + ((ch ^ row0 ^ 8) << 4);
    const int vw0 = LDS_V + row0 * 512 + ((ch ^ ((row0 & 3) << 2)) << 4);
#define WRITEK(SET, BUF) do { _Pragma("unroll") for (int i = 0; i < 8; ++i) \
      lds_w128(smem + ((i & 1) ? kw1 : kw0) + (BUF) + (i >> 1) * 8192, SET[i]); SBAR(); } while (0)
#define WRITEV(SET, BUF) do { _Pragma("unroll") for (int i = 0; i < 8; ++i) \
      lds_w128(smem + vw0 + (BUF) + i * 4096, SET[i]); SBAR(); } while (0)
    const int kv0 = row0 * 512 + ((ch ^ row0) << 4), kv1 = row0 * 512 + ((ch ^ row0 ^ 8) << 4), vv0 = row0 * 512 + ((ch ^ ((row0 & 3) << 2)) << 4);
#define KVO(x) (x)
#define KDMA(TILE, HALF) do { _Pragma("unroll") for (int i = 0; i < 4; ++i) \
      dmakv(rk, KVO((i & 1) ? kv1 : kv0), (TILE) * 32768 + (HALF) * 16384 + i * 4096, LDS_K + ((TILE) & 1) * 32768 + (HALF) * 16384 + i * 4096 + h * 1024); SBAR(); } while (0)
#define VDMA(TILE, HALF) do { _Pragma("unroll") for (int i = 0; i < 4; ++i) \
      dmakv(rv, KVO(vv0), (TILE) * 32768 + (HALF) * 16384 + i * 4096, LDS_V + ((TILE) & 1) * 32768 + (HALF) * 16384 + i * 4096 + h * 1024); SBAR(); } while (0)
    WRITEK(R0, 0);
    WG_BARRIER();
    WRITEK(R1, 32768);
    WG_BARRIER();
    WRITEV(SA, 0);
    STAMP(4);
    VDMA(1, 0); KDMA(2, 0);
    WG_BARRIER();
    WG_BARRIER();
    VDMA(1, 1); KDMA(2, 1);
    WAITV(8);
    const unsigned hs_c0 = 0; (void)hs_c0;
#pragma unroll 1
    for (int n = 1; n < 27; ++n) {
      WG_BARRIER();
      const int tv = (n + 3) >> 1, hf = (n + 3) & 1;
      VDMA(tv, hf); KDMA(tv + 1, hf);
      if (n == 20 && hs) hs_echo(ws, lid, lane);
      WAITV(8);
      STAMP_IF(n == 8, 5); STAMP_IF(n == 16, 6); STAMP_IF(n == 24, 7);
    }
    WG_BARRIER();
    VDMA(15, 0);
    WAITV(4);
    WG_BARRIER();
    VDMA(15, 1);
    ODMA(0);
    WAITV(12);
    WG_BARRIER();
    WAITV(8);
#undef KDMA
#undef VDMA
#undef KVO
    STAMP(8);
    WG_BARRIER();
    ODMA(1);
    WAITV(8);
    WG_BARRIER();
    STAMP(10);
    ODMA(2);
    WAITV(8);
#pragma unroll 1
    for (int s = 0; s < 13; ++s) {
      WG_BARRIER();
      ODMA(s + 3);
      WAITV(8);
    }
    WG_BARRIER();
    WAITV(0);
    WG_BARRIER();
    WG_BARRIER();
    STAMP(11);
    if (h == 0) {
      const unsigned* cnt = (const unsigned*)(ws + CTL_OFF + (size_t)lid * 512);
      for (unsigned spins = 0; spins < SPIN_CAP; ++spins) {
        if (__hip_atomic_load(cnt, RLX_AGENT) >= 32u) break;
        if ((spins & 63u) == 63u) { const int l = lane_id(); if (l < 8) hs_echo(ws, lid, l); }
        __builtin_amdgcn_s_sleep(2);
      }
    }
#undef QDMA
#undef ODMA
#undef LOADU
#undef WRITEK
#undef WRITEV
#undef voff
#undef wqv
#undef wov
  } else {
    const unsigned c0_ = (unsigned)__builtin_amdgcn_s_memtime(), r0_ = (unsigned)__builtin_amdgcn_s_memrealtime();
    {
      const int lane = lane_id();
      const rsrc_t rx = mk_rsrc((blk ? xB : xA) + (size_t)bb * SQ * DM * 2, SQ * DM * 2);
      const int xp = ((wid + (kvh >> 1)) & 3) * 14;
      u32x4 xr[14];
#pragma unroll
      for (int f = 0; f < 14; ++f) xr[f] = __builtin_amdgcn_raw_buffer_load_b128(rx, lane * 16, (xp + f) * 1024, XAUX);
      SBAR();
      {
        const char* p0 = wid == 0 ? (blk ? kB : kA) + (size_t)(bb * HKV + kvh) * KV_BYTES
                       : wid == 1 ? (blk ? woB : woA) + (size_t)kvh * 512 * DM * 2
                       : wid == 2 ? (const char*)ws + (size_t)(lid >> 3) * 64 * SLAB : (const char*)out + (size_t)(bb * SQ + kvh * 8) * (2 * DM) * 2;
        const char* p1 = wid == 0 ? (blk ? vB : vA) + (size_t)(bb * HKV + kvh) * KV_BYTES
                       : wid == 1 ? p0 + 512 * DM * 2 - 64 : wid == 2 ? p0 + 64 * SLAB - 64 : p0 + 8 * (2 * DM) * 2 - 64;
        const unsigned long long a0 = (unsigned long long)p0, a1 = (unsigned long long)p1;
        const unsigned long long u0 = ((unsigned long long)__builtin_amdgcn_readfirstlane((unsigned)(a0 >> 32)) << 32) | (unsigned)__builtin_amdgcn_readfirstlane((unsigned)a0);
        const unsigned long long u1 = ((unsigned long long)__builtin_amdgcn_readfirstlane((unsigned)(a1 >> 32)) << 32) | (unsigned)__builtin_amdgcn_readfirstlane((unsigned)a1);
        unsigned d0, d1;
        asm volatile("s_nop 4\n\ts_load_dword %0, %2, 0x0\n\ts_load_dword %1, %3, 0x0\n\ts_waitcnt lgkmcnt(0)" : "=&s"(d0), "=&s"(d1) : "s"(u0), "s"(u1) : "memory");
      }
#pragma unroll
      for (int f = 0; f < 14; ++f) {
        const unsigned idx = (xp + f) * 64 + lane, row = idx / 56u, q = idx - row * 56u;
        lds_w128(smem + LDS_X + (q >> 2) * 4096 + (row >> 4) * 1024 + (q & 3) * 256 + (((row & 15) ^ (q & 7)) << 4), xr[f]);
      }
    }
    WG_BARRIER();
    const int fastclk = __builtin_amdgcn_readfirstlane((int)(((unsigned)__builtin_amdgcn_s_memtime() - c0_) * 16u - ((unsigned)__builtin_amdgcn_s_memrealtime() - r0_) * (unsigned)AP_TH));
    __builtin_amdgcn_s_waitcnt(0xC07F); SBAR();
    STAMP(1);
    {
      f32x4 acc[8][4]; PFrag<8> F[2];
#pragma unroll
      for (int i = 0; i < 8; ++i)
#pragma unroll
        for (int rt = 0; rt < 4; ++rt) acc[i][rt] = f32x4{0.f, 0.f, 0.f, 0.f};
      const int ab = pabase<QRS>(wid * 8), bbs = pbbase(LDS_X);
      { const int aa = ab + qslot(0), ba = pbaddr(bbs, kq0); static_for<20>([&](auto kc) { pread<8, QRS, decltype(kc)::value>(F[0], smem, aa, ba); }); }
      int sl = 1;
#pragma unroll 1
      for (int s = 0; s < 14; s += 2) {
        WG_BARRIER();
        pstep<8, QRS>(acc, F[0], F[1], smem, ab + qslot(sl), pbaddr(bbs, KQ(s + 1)));
        sl = sl == 2 ? 0 : sl + 1;
        WG_BARRIER();
        pstep<8, QRS>(acc, F[1], F[0], smem, ab + qslot(sl), pbaddr(bbs, KQ(s + 2)));
        sl = sl == 2 ? 0 : sl + 1;
      }
      q_handover(acc, smem, wid);
    }
    STAMP(2);
    {
      const int w = wid, hh = w >> 1, rh = w & 1;
      const int lane = lane_id(), h = lane >> 5, r = lane & 31;
      f32x16 o[8];
#pragma unroll
      for (int dt = 0; dt < 8; ++dt)
#pragma unroll
        for (int i = 0; i < 16; ++i) o[dt][i] = 0.f;
      float mc = 0.f, lsum = 0.f;
      f32x16 s0, s1;
      u32x4 p0[2], p1[2];
      u32x4 qown[8];
      int kb = LDS_K + r * 512 + ((h ^ (r & 15)) << 4);
      int vb = LDS_V + (4 * h + ((lane & 15) >> 2)) * 512 + (((lane & 15) >> 2) << 6) + ((lane >> 4) & 1) * 32 + (lane & 3) * 8;
      LAS char* qp = smem + LDS_QP + w * 8192 + lane * 16;
      constexpr int PD = 4, RN = 6;
      bf16x4 vl[RN], vh[RN]; bf16x8 kf[RN], qf[2];
      float t0, t1, t2, t3, t4, mx;
#define FETCH(N) do { constexpr int n_ = (N), blk_ = n_ >> 4, j_ = n_ & 15; \
        if (blk_ & 1) kf[n_ % RN] = asbf(lds_r128(smem + (kb ^ ((j_ >> 1) << 5)) + (j_ & 1) * 256 + (blk_ >> 1) * 16384)); \
        else { LAS char* a_ = smem + (vb ^ ((j_ & 3) << 6)) + ((j_ >> 2) & 1) * 256 + (blk_ + (j_ >> 3)) * 8192; vl[n_ % RN] = lds_tr(a_); vh[n_ % RN] = lds_tr(a_ + 4096); } } while (0)
#define FETCHQ(J) qf[((J) >> 1) & 1] = asbf(lds_r128(qp + ((J) >> 1) * 1024))
#define JOB_AB(X, j) do { \
        if ((j) < 4) { _Pragma("unroll") for (int i_ = 0; i_ < 4; ++i_) X[4 * (j) + i_] = fmaf(X[4 * (j) + i_], SCL, -mc); } \
        else if ((j) == 4) { t0 = max3(X[0], X[1], X[2]); t1 = max3(X[3], X[4], X[5]); t2 = max3(X[6], X[7], X[8]); } \
        else if ((j) == 5) { t3 = max3(X[9], X[10], X[11]); t4 = max3(X[12], X[13], X[14]); t0 = max3(t0, t1, X[15]); } \
        else if ((j) == 6) { t0 = fmaxf(t0, max3(t2, t3, t4)); } \
        else if ((j) == 7) { mx = hmax(t0); } } while (0)
#define JOB_EXP(X, P, j) do { X[j] = __builtin_amdgcn_exp2f(X[j]); \
        if ((j) > 0) lsum += X[(j) - 1]; \
        if ((j) >= 2 && !((j) & 1)) P[((j) - 2) >> 3][(((j) - 2) >> 1) & 3] = pk2(X[(j) - 2], X[(j) - 1]); \
        if ((j) == 15) { lsum += X[15]; P[1][3] = pk2(X[14], X[15]); } } while (0)
#define RESCALE(X) do { if (__any(mx > THR)) { const float d_ = fmaxf(mx, 0.f), al_ = __builtin_amdgcn_exp2f(-d_); mc += d_; lsum *= al_; \
          _Pragma("unroll") for (int i_ = 0; i_ < 16; ++i_) X[i_] -= d_; \
          _Pragma("unroll") for (int dt = 0; dt < 8; ++dt) o[dt] *= al_; } SBAR(); } while (0)
#define FETCH_AHEAD(B, j) switch (j) { \
            case 0: FETCH((B) * 16 + 0 + PD); break; case 1: FETCH((B) * 16 + 1 + PD); break; case 2: FETCH((B) * 16 + 2 + PD); break; case 3: FETCH((B) * 16 + 3 + PD); break; \
            case 4: FETCH((B) * 16 + 4 + PD); break; case 5: FETCH((B) * 16 + 5 + PD); break; case 6: FETCH((B) * 16 + 6 + PD); break; case 7: FETCH((B) * 16 + 7 + PD); break; \
            case 8: FETCH((B) * 16 + 8 + PD); break; case 9: FETCH((B) * 16 + 9 + PD); break; case 10: FETCH((B) * 16 + 10 + PD); break; case 11: FETCH((B) * 16 + 11 + PD); break; \
            case 12: FETCH(((B) * 16 + 12 + PD) & 63); break; case 13: FETCH(((B) * 16 + 13 + PD) & 63); break; case 14: FETCH(((B) * 16 + 14 + PD) & 63); break; default: FETCH(((B) * 16 + 15 + PD) & 63); break; }
#define PVBLOCK(B, P, X, LAST, PN) do { _Pragma("unroll") for (int j = 0; j < 16; ++j) { \
          const bf16x8 a_ = __builtin_shufflevector(vl[((B) * 16 + j) % RN], vh[((B) * 16 + j) % RN], 0, 1, 2, 3, 4, 5, 6, 7); \
          o[j & 7] = MFMA32(a_, asbf(P[j >> 3]), o[j & 7]); \
          if (j + PD < 16 || !(LAST)) { FETCH_AHEAD(B, j) } \
          if (!(LAST) && j == 14) FETCHQ(1); \
          if ((LAST) != 2) JOB_AB(X, j); \
          PACE(PN); \
          SBAR(); } } while (0)
#define QKBLOCK(B, S, NEXT, JOB, X, P, PN) do { _Pragma("unroll") for (int j = 0; j < 16; ++j) { \
          const bf16x8 q_ = (j & 1) ? qf[(j >> 1) & 1] : asbf(qown[j >> 1]); \
          if (j == 0) { f32x16 z_; _Pragma("unroll") for (int i_ = 0; i_ < 16; ++i_) z_[i_] = 0.f; S = MFMA32(kf[((B) * 16 + j) % RN], q_, z_); } \
          else S = MFMA32(kf[((B) * 16 + j) % RN], q_, S); \
          if (j + PD < 16 || (NEXT)) { FETCH_AHEAD(B, j) } \
          if (!(j & 1) && j + 3 < 16) FETCHQ(j + 3); \
          if (JOB) JOB_EXP(X, P, j); \
          PACE(PN); \
          SBAR(); } } while (0)
#define FETCH4(N) do { FETCH(N); FETCH((N) + 1); FETCH((N) + 2); FETCH((N) + 3); } while (0)
      static_assert(PD == 4, "FETCH4");
#define RAWBAR() do { SBAR(); asm volatile("s_barrier" ::: "memory"); SBAR(); } while (0)
      WG_BARRIER();
      STAMP(3);
#pragma unroll
      for (int f = 0; f < 8; ++f) qown[f] = lds_r128(smem + LDS_QT + w * 8192 + f * 1024 + lane * 16);
      FETCH4(16); FETCHQ(1); SBAR();
      QKBLOCK(1, s0, 0, 0, s0, p0, PACEN);
      FETCH4(48); FETCHQ(1); SBAR();
#pragma unroll
      for (int j = 0; j < 8; ++j) JOB_AB(s0, j);
      mc = mx;
#pragma unroll
      for (int i = 0; i < 16; ++i) s0[i] -= mx;
      RAWBAR();
      QKBLOCK(3, s1, 0, 1, s0, p0, PACEN);
      kb ^= 32768;
      RAWBAR();
      FETCH4(0); SBAR();
#define LSTAMP() do { STAMP_IF(t == 0, 4); STAMP_IF(t == 4, 5); STAMP_IF(t == 8, 6); STAMP_IF(t == 12, 7); } while (0)
#define ATT_LOOP(PN, T0, T1) _Pragma("unroll 1") for (int t = (T0); t < (T1); ++t) { \
        RAWBAR();     \
        LSTAMP(); \
        PVBLOCK(0, p0, s1, 0, PN); \
        RESCALE(s1); \
        QKBLOCK(1, s0, 1, 1, s1, p1, PN); \
        RAWBAR();     \
        PVBLOCK(2, p1, s0, 0, PN); \
        vb ^= 32768; \
        RESCALE(s0); \
        QKBLOCK(3, s1, 1, 1, s0, p0, PN); \
        kb ^= 32768; }
      const int nslow = fastclk > 0 ? AP_N : 0;
      ATT_LOOP(AP_P, 0, nslow)
      ATT_LOOP(AP_P2, nslow, 15)
#undef ATT_LOOP
#undef LSTAMP
#pragma unroll
      for (int j = 0; j < 8; ++j) JOB_AB(s1, j);
      if (__any(mx > THR)) {
        const float d_ = fmaxf(mx, 0.f), al_ = __builtin_amdgcn_exp2f(-d_);
        mc += d_; lsum *= al_;
#pragma unroll
        for (int i = 0; i < 16; ++i) s1[i] -= d_;
#pragma unroll
        for (int dt = 0; dt < 8; ++dt) o[dt] *= al_;
#pragma unroll
        for (int k = 0; k < 2; ++k)
#pragma unroll
          for (int i = 0; i < 4; ++i) p0[k][i] = pk2(__uint_as_float(p0[k][i] << 16) * al_, __uint_as_float(p0[k][i] & 0xffff0000u) * al_);
      }
      SBAR();
#pragma unroll
      for (int j = 0; j < 16; ++j) JOB_EXP(s1, p1, j);
      const float iv = 1.f / hsum(lsum);
      PVBLOCK(0, p0, s1, 2, -1);
      RAWBAR();
      STAMP(8);
      FETCH4(32); SBAR();
      PVBLOCK(2, p1, s0, 2, -1);
#undef FETCH
#undef FETCHQ
#undef FETCH4
#undef FETCH_AHEAD
#undef JOB_AB
#undef JOB_EXP
#undef RESCALE
#undef PVBLOCK
#undef QKBLOCK
      STAMP(9);
      const int lane2 = lane_id(), h2 = lane2 >> 5, r2 = lane2 & 31;
      LAS char* ow = smem + LDS_V + hh * 65536 + (rh * 2 + (r2 >> 4)) * 1024 + 2 * h2 * 256;
#pragma unroll
      for (int dt = 0; dt < 8; ++dt)
#pragma unroll
        for (int pp = 0; pp < 2; ++pp)
          lds_w128(ow + dt * 4096 + pp * 256 + (((r2 & 15) ^ (2 * h2 + pp) ^ ((dt & 1) * 4)) << 4),
                   u32x4{pk2(o[dt][8 * pp] * iv, o[dt][8 * pp + 1] * iv), pk2(o[dt][8 * pp + 2] * iv, o[dt][8 * pp + 3] * iv),
                         pk2(o[dt][8 * pp + 4] * iv, o[dt][8 * pp + 5] * iv), pk2(o[dt][8 * pp + 6] * iv, o[dt][8 * pp + 7] * iv)});
    }
    WG_BARRIER();
    STAMP(10);
    {
      HS_REQ();
      f32x4 acc[7][4]; PFrag<7> F[2];
#pragma unroll
      for (int i = 0; i < 7; ++i)
#pragma unroll
        for (int rt = 0; rt < 4; ++rt) acc[i][rt] = f32x4{0.f, 0.f, 0.f, 0.f};
      const int ab = pabase<ORS>(wid * 7), bbs = pbbase(LDS_V);
      { const int aa = ab + oslot(0), ba = obaddr(bbs, KO(0)); static_for<18>([&](auto kc) { pread<7, ORS, decltype(kc)::value>(F[0], smem, aa, ba); }); }
      int sl = 1;
#pragma unroll 1
      for (int s = 0; s < 16; s += 2) {
        WG_BARRIER();
        pstep<7, ORS>(acc, F[0], F[1], smem, ab + oslot(sl), obaddr(bbs, KO(s + 1)));
        sl = sl == 2 ? 0 : sl + 1;
        WG_BARRIER();
        pstep<7, ORS>(acc, F[1], F[0], smem, ab + oslot(sl), obaddr(bbs, KO(s + 2)));
        sl = sl == 2 ? 0 : sl + 1;
      }
      STAMP(11);
      publish<7>(acc, ws, lid, wid * 7, HS_OK());
    }
  }
  WG_BARRIER();
  STAMP(15);
  if (wid < 7) {
    const int lane = lane_id();
    const rsrc_t rp = mk_rsrc(ws + ((size_t)(lid >> 3) * 64 + kvh * 8) * SLAB, 8 * SLAB);
    u32x4 v[8];
#pragma unroll
    for (int s = 0; s < 8; ++s) v[s] = __builtin_amdgcn_raw_buffer_load_b128(rp, wid * 1024 + lane * 16, s * SLAB, AUX_SC1);
    float a[8];
#pragma unroll
    for (int k = 0; k < 4; ++k) { a[2 * k] = __uint_as_float(v[0][k] << 16); a[2 * k + 1] = __uint_as_float(v[0][k] & 0xffff0000u); }
#pragma unroll
    for (int s = 1; s < 8; ++s)
#pragma unroll
      for (int k = 0; k < 4; ++k) { a[2 * k] += __uint_as_float(v[s][k] << 16); a[2 * k + 1] += __uint_as_float(v[s][k] & 0xffff0000u); }
    const rsrc_t ro = mk_rsrc(out + (size_t)(bb * SQ + kvh * 8) * (2 * DM) * 2, 8 * 2 * DM * 2);
    __builtin_amdgcn_raw_buffer_store_b128(u32x4{pk2(a[0], a[1]), pk2(a[2], a[3]), pk2(a[4], a[5]), pk2(a[6], a[7])}, ro,
                                           ((lane & 7) * (2 * DM) + blk * DM + wid * 64 + (lane >> 3) * 8) * 2, 0, OUT_AUX);
  }
  STAMP(12); STAMPC(14); STAMP_OUT();
}
}

extern "C" void kernel_launch(void* const* d_in, const int* in_sizes, int n_in, void* d_out, int out_size, void* d_ws, size_t ws_size,
                              hipStream_t stream) {
  (void)in_sizes; (void)n_in; (void)out_size; (void)ws_size;
  gqa_fused<<<256, 512, 0, stream>>>((const char*)d_in[0], (const char*)d_in[1], (const char*)d_in[2], (const char*)d_in[3], (const char*)d_in[4],
                                     (const char*)d_in[5], (const char*)d_in[6], (const char*)d_in[7], (const char*)d_in[8], (const char*)d_in[9],
                                     (char*)d_out, (char*)d_ws);
}
```

```cpp
#define PRETOUCH
#define AP_TH 328
#define AP_P 0
#include <hip/hip_runtime.h>
#include <hip/hip_bf16.h>
#include <utility>
#include <type_traits>

#define LAS __attribute__((address_space(3)))
#define DI __device__ __forceinline__
typedef __attribute__((ext_vector_type(8))) __bf16 bf16x8;
typedef __attribute__((ext_vector_type(4))) __bf16 bf16x4;
typedef __attribute__((ext_vector_type(2))) __bf16 bf16x2;
typedef __attribute__((ext_vector_type(16))) float f32x16;
typedef __attribute__((ext_vector_type(4))) float f32x4;
typedef __attribute__((ext_vector_type(2))) float f32x2;
typedef __attribute__((ext_vector_type(4))) unsigned u32x4;

namespace {
constexpr int SQ = 64, DM = 448, HKV = 8, SK = 1024, DH = 256;
constexpr int NQ = 4096;
constexpr int KV_BYTES = SK * DH * 2;
constexpr int W_BYTES = DM * NQ * 2;
constexpr int LDS_K = 0, LDS_V = 65536, LDS_QP = 131072, LDS_TOTAL = 163840;
constexpr int LDS_X = 34816, LDS_QT = LDS_V + 32768;
constexpr int XRS = 912;
constexpr int SLAB = 7168;
constexpr size_t CTL_OFF = (size_t)16 << 20;
constexpr float SCL = 0.0625f * 1.4426950408889634f;
constexpr float THR = 8.f;
constexpr int AUX_NT = 2, AUX_SC1 = 16;
#define AUX_W 2
#define OPOL ""
#define QPOL "nt"
#define WARM_AUX 2
#define OROT 1
#define XAUX 2
#define OUT_AUX 0
constexpr int OOB = 0x40000000;
constexpr unsigned SPIN_CAP = 1u << 21;

#define SBAR() __builtin_amdgcn_sched_barrier(0)
#define WG_BARRIER() do { SBAR(); asm volatile("s_waitcnt lgkmcnt(0)\n\ts_barrier" ::: "memory"); SBAR(); } while (0)
#define STAMP(idx) do {} while (0)
#define STAMP_IF(c, idx) do {} while (0)
#define STAMPC(idx) do {} while (0)
#define STAMP_DECL do {} while (0)
#define STAMP_OUT() do {} while (0)
#define PACE(PN) do { if ((PN) >= 0) asm volatile("s_nop %0" :: "i"((PN) < 0 ? 0 : (PN))); } while (0)
#define AP_N 15
#define AP_P2 -1
#define PACEN -1
#define NAP() do {} while (0)
#define MFMA(a, b, c) __builtin_amdgcn_mfma_f32_16x16x32_bf16((a), (b), (c), 0, 0, 0)
#define MFMA32(a, b, c) __builtin_amdgcn_mfma_f32_32x32x16_bf16((a), (b), (c), 0, 0, 0)
#define RLX_AGENT __ATOMIC_RELAXED, __HIP_MEMORY_SCOPE_AGENT

DI unsigned pk2(float a, float b) { f32x2 f = {a, b}; bf16x2 r = __builtin_convertvector(f, bf16x2); return __builtin_bit_cast(unsigned, r); }
DI bf16x8 asbf(u32x4 v) { return __builtin_bit_cast(bf16x8, v); }
DI u32x4 lds_r128(LAS char* p) { return *(LAS u32x4*)p; }
DI void lds_w128(LAS char* p, u32x4 v) { *(LAS u32x4*)p = v; }
DI bf16x4 lds_tr(LAS char* p) { return __builtin_amdgcn_ds_read_tr16_b64_v4bf16((LAS bf16x4*)p); }
DI unsigned perm2(int e, unsigned hi, unsigned lo) { return __builtin_amdgcn_perm(hi, lo, e ? 0x07060302u : 0x05040100u); }
DI float max3(float a, float b, float c) { return fmaxf(fmaxf(a, b), c); }
DI float hmax(float v) {
  auto b = __builtin_amdgcn_permlane32_swap(__float_as_uint(v), __float_as_uint(v), false, false);
  return fmaxf(__uint_as_float(b[0]), __uint_as_float(b[1]));
}
DI float hsum(float v) {
  auto b = __builtin_amdgcn_permlane32_swap(__float_as_uint(v), __float_as_uint(v), false, false);
  return __uint_as_float(b[0]) + __uint_as_float(b[1]);
}
DI int lane_id() {
  int l; asm volatile("v_mbcnt_lo_u32_b32 %0, -1, 0\n\tv_mbcnt_hi_u32_b32 %0, -1, %0" : "=v"(l)); return l; }
typedef __amdgpu_buffer_rsrc_t rsrc_t;
DI rsrc_t mk_rsrc(const void* p, int bytes) { return __builtin_amdgcn_make_buffer_rsrc((void*)p, 0, bytes, 0x00020000); }
template <class F, int... I> DI void static_for_(F&& f, std::integer_sequence<int, I...>) { (f(std::integral_constant<int, I>{}), ...); }
template <int N, class F> DI void static_for(F&& f) { static_for_(f, std::make_integer_sequence<int, N>{}); }

DI unsigned* ctl_chal(char* ws, int wg, int slot) { return (unsigned*)(ws + CTL_OFF + (size_t)wg * 512 + 128 + slot * 4); }
DI unsigned* ctl_resp(char* ws, int wg, int slot) { return (unsigned*)(ws + CTL_OFF + (size_t)wg * 512 + 256 + slot * 4); }
DI void hs_echo(char* ws, int lid, int lane) {
  const unsigned cv = __hip_atomic_load(ctl_chal(ws, lid, lane), RLX_AGENT);
  const unsigned rv = __hip_atomic_load(ctl_resp(ws, (lid & ~7) + lane, lid & 7), RLX_AGENT);
  if (cv != rv) __hip_atomic_store(ctl_resp(ws, (lid & ~7) + lane, lid & 7), cv, RLX_AGENT);
}
DI u32x4 ld1(rsrc_t rs, int voff, int soff) { return __builtin_amdgcn_raw_buffer_load_b128(rs, voff, soff, AUX_NT); }
DI void ld8(u32x4 (&r)[8], rsrc_t rs, int voff, int soff, int sa, int sb) {
#pragma unroll
  for (int j = 0; j < 8; ++j) r[j] = ld1(rs, voff, soff + (j & 3) * sa + (j >> 2) * sb);
}

DI void dma1(rsrc_t rs, int voff, int soff, int ldsaddr) {
  asm volatile("s_mov_b32 m0, %0\n\ts_nop 0\n\tbuffer_load_dwordx4 %1, %2, %3 offen " QPOL " lds" :: "s"(ldsaddr), "v"(voff), "s"(rs), "s"(soff) : "memory");
}
DI void dmakv(rsrc_t rs, int voff, int soff, int ldsaddr) {
  asm volatile("s_mov_b32 m0, %0\n\ts_nop 0\n\tbuffer_load_dwordx4 %1, %2, %3 offen nt lds" :: "s"(ldsaddr), "v"(voff), "s"(rs), "s"(soff) : "memory");
}
DI void dma56nt(rsrc_t rs, int voff, int soff, int ldsaddr) {
  asm volatile("s_mov_b32 m0, %0\n\ts_mov_b32 exec_hi, 0xffffff\n\tbuffer_load_dwordx4 %1, %2, %3 offen nt lds\n\ts_mov_b32 exec_hi, -1"
               :: "s"(ldsaddr), "v"(voff), "s"(rs), "s"(soff) : "memory");
}
DI void dma56(rsrc_t rs, int voff, int soff, int ldsaddr) {
  asm volatile("s_mov_b32 m0, %0\n\ts_mov_b32 exec_hi, 0xffffff\n\tbuffer_load_dwordx4 %1, %2, %3 offen " OPOL " lds\n\ts_mov_b32 exec_hi, -1"
               :: "s"(ldsaddr), "v"(voff), "s"(rs), "s"(soff) : "memory");
}
#define WAITV(N) do { SBAR(); asm volatile("s_waitcnt vmcnt(" #N ")" ::: "memory"); SBAR(); } while (0)
template <int NF> struct PFrag { bf16x4 lo[NF], hi[NF]; bf16x8 b[4]; };
template <int RS> DI int pabase(int f0) {
  const int lane = lane_id(), g = lane >> 4, q = (lane & 15) >> 2, p = lane & 3;
  return (16 * (g >> 1) + 4 * (g & 1) + q) * RS + f0 * 32 + p * 8;
}
DI int pbbase(int bimg) { const int lane = lane_id(), g = lane >> 4, c = lane & 15; return bimg + g * 256 + ((c ^ g) << 4); }
DI int pbaddr(int bb, int ks) { return (bb ^ ((ks & 1) << 6)) + ks * 4096; }
template <int NF, int RS, int K> DI void pread(PFrag<NF>& f, LAS char* smem, int aa, int ba) {
  if constexpr (K < 4) f.b[K] = asbf(lds_r128(smem + ba + K * 1024));
  else if constexpr (K < 4 + 2 * NF) {
    if constexpr ((K & 1) == 0) f.lo[(K - 4) >> 1] = lds_tr(smem + aa + ((K - 4) >> 1) * 32);
    else f.hi[(K - 4) >> 1] = lds_tr(smem + aa + ((K - 4) >> 1) * 32 + 8 * RS);
  }
}
template <int NF, int RS>
DI void pstep(f32x4 (&acc)[NF][4], const PFrag<NF>& cur, PFrag<NF>& nxt, LAS char* smem, int aa, int ba) {
  static_for<NF * 4>([&](auto nc) {
    constexpr int n = decltype(nc)::value, i = n >> 2, rt = n & 3;
    const bf16x8 a = __builtin_shufflevector(cur.lo[i], cur.hi[i], 0, 1, 2, 3, 4, 5, 6, 7);
    acc[i][rt] = MFMA(a, cur.b[rt], acc[i][rt]);
    pread<NF, RS, n>(nxt, smem, aa, ba);
    SBAR();
  });
}
template <int NF> DI u32x4 ppack(const f32x4 (&acc)[NF][4], int i, int rp) {
  auto lo = __builtin_amdgcn_permlane16_swap(pk2(acc[i][2 * rp][0], acc[i][2 * rp][1]), pk2(acc[i][2 * rp + 1][0], acc[i][2 * rp + 1][1]), false, false);
  auto hi = __builtin_amdgcn_permlane16_swap(pk2(acc[i][2 * rp][2], acc[i][2 * rp][3]), pk2(acc[i][2 * rp + 1][2], acc[i][2 * rp + 1][3]), false, false);
  return u32x4{lo[0], hi[0], lo[1], hi[1]};
}
DI void q_handover(const f32x4 (&acc)[8][4], LAS char* smem, int w) {
  const int lane = lane_id(), g = lane >> 4, c = lane & 15;
  LAS char* base = smem + ((w & 1) ? LDS_QP : LDS_QT) + (w >> 1) * 16384 + (g & 1) * 1024 + (g >> 1) * 256 + c * 16;
#pragma unroll
  for (int i = 0; i < 8; ++i)
#pragma unroll
    for (int rp = 0; rp < 2; ++rp) lds_w128(base + rp * 8192 + (i >> 1) * 2048 + (i & 1) * 512, ppack<8>(acc, i, rp));
}
template <int NF> DI void publish(const f32x4 (&acc)[NF][4], char* ws, int lid, int f0, bool ok) {
  const int lane = lane_id(), g = lane >> 4, c = lane & 15;
  const rsrc_t rp = mk_rsrc(ws + (size_t)(lid >> 3) * 64 * SLAB, 64 * SLAB);
  const int voff = (((g & 1) * 2 + (c >> 3)) * 8 + (lid & 7)) * SLAB + f0 * 256 + (g >> 1) * 128 + (c & 7) * 16;
#pragma unroll
  for (int i = 0; i < NF; ++i)
#pragma unroll
    for (int r2 = 0; r2 < 2; ++r2) __builtin_amdgcn_raw_buffer_store_b128(ppack<NF>(acc, i, r2), rp, voff, r2 * 32 * SLAB + i * 256, AUX_SC1);
  asm volatile("s_waitcnt vmcnt(0)" ::: "memory");
  for (unsigned spins = 0; !ok && spins < SPIN_CAP; ++spins) {
    if (lane < 8) hs_echo(ws, lid, lane);
    const unsigned cv = __hip_atomic_load(ctl_chal(ws, (lid & ~7) + (lane & 7), lid & 7), RLX_AGENT);
    const unsigned rv = __hip_atomic_load(ctl_resp(ws, lid, lane & 7), RLX_AGENT);
    ok = __all(cv == rv);
    __builtin_amdgcn_s_sleep(8);
  }
  if (lane < 8) __hip_atomic_fetch_add((unsigned*)(ws + CTL_OFF + (size_t)((lid & ~7) + lane) * 512), 1u, RLX_AGENT);
}
constexpr int QRS = 1056, ORS = 928;
DI int qslot(int i) { return i == 0 ? 92160 : (i == 1 ? 126976 : 0); }
DI int oslot(int i) { return i == 0 ? LDS_K : (i == 1 ? LDS_K + 32768 : LDS_V + 32768); }
DI int obaddr(int bb, int ks) { return (bb ^ ((ks & 1) << 6)) + ks * 4096 + (ks >> 3) * 32768; }

__global__ __launch_bounds__(512, 2) void gqa_fused(const char* __restrict__ xA, const char* __restrict__ kA, const char* __restrict__ vA,
                                                    const char* __restrict__ wqA, const char* __restrict__ woA,
                                                    const char* __restrict__ xB, const char* __restrict__ kB, const char* __restrict__ vB,
                                                    const char* __restrict__ wqB, const char* __restrict__ woB,
                                                    char* __restrict__ out, char* ws) {
  __shared__ __attribute__((aligned(1024))) char smem_[LDS_TOTAL];
  LAS char* smem = (LAS char*)smem_;
  const int wid = __builtin_amdgcn_readfirstlane(threadIdx.x >> 6);
  const int bid = blockIdx.x, blk = (bid >> 2) & 1, kvh = (bid & 3) * 2 + (bid >> 7), bb = (bid >> 3) & 15;
  const int lid = (blk * 16 + bb) * 8 + kvh;
  const int kq0 = bb < 14 ? bb : bb - 14;
#define KQ(S) ((kq0 + (S)) >= 14 ? kq0 + (S) - 14 : kq0 + (S))
#define KO(S) (((S) + OROT * bb) & 15)
  STAMP_DECL;
  STAMP(0); STAMPC(13);
  const rsrc_t rc = mk_rsrc(ws + CTL_OFF + (size_t)(lid & ~7) * 512, 8 * 512);
#define HS_REQ() const unsigned hsc_ = __builtin_amdgcn_raw_buffer_load_b32(rc, (lane_id() & 7) * 512 + 128 + (lid & 7) * 4, 0, AUX_SC1), \
                                hsr_ = __builtin_amdgcn_raw_buffer_load_b32(rc, (lid & 7) * 512 + 256 + (lane_id() & 7) * 4, 0, AUX_SC1)
#define HS_OK() __all(hsc_ == hsr_)

  if (wid >= 4) {
    int lane = lane_id();
    const int h = wid - 4;
    bool hs = h == 0 && lane < 8;
    __builtin_amdgcn_raw_buffer_store_b32(0u, rc, hs && lane == 0 ? (lid & 7) * 512 : OOB, 0, AUX_SC1);
    const unsigned hs_r = __builtin_amdgcn_raw_buffer_load_b32(rc, hs ? (lid & 7) * 512 + 256 + lane * 4 : OOB, 0, AUX_SC1);
    const rsrc_t rwq = mk_rsrc((blk ? wqB : wqA) + kvh * 1024, W_BYTES - kvh * 1024);
    const rsrc_t rwo = mk_rsrc((blk ? woB : woA) + (size_t)kvh * 512 * DM * 2, 512 * DM * 2);
    const rsrc_t rk = mk_rsrc((blk ? kB : kA) + (size_t)(bb * HKV + kvh) * KV_BYTES, KV_BYTES);
    const rsrc_t rv = mk_rsrc((blk ? vB : vA) + (size_t)(bb * HKV + kvh) * KV_BYTES, KV_BYTES);
#define voff l16
#define wqv l16
#define wov l16
    int l16 = lane * 16;
    u32x4 R0[8], R1[8], SA[8];
    const int irow = 16 * (h >> 1) + 4 * (h & 1);
#define QDMA(S) do { _Pragma("unroll") for (int j = 0; j < 8; ++j) \
      dma1(rwq, wqv, (KQ(S) * 32 + 8 * h + j) * (NQ * 2), qslot((S) % 3) + (irow + 8 * (j >> 2) + (j & 3)) * QRS); SBAR(); } while (0)
#define ODMA(S) ODMAX(dma56, S)
#define ODMAX(FN, S) do { { _Pragma("unroll") for (int j = 0; j < 8; ++j) \
      FN(rwo, wov, (KO(S) * 32 + 4 * h + 16 * (j >> 2) + (j & 3)) * (DM * 2), oslot((S) % 3) + (irow + 8 * (j >> 2) + (j & 3)) * ORS); } SBAR(); } while (0)
#define LOADU(SET, RS, TILE) do { ld8(SET, RS, voff, (TILE) * 32768 + h * 1024, 4096, 16384); SBAR(); } while (0)
    QDMA(0); QDMA(1); QDMA(2);
    WAITV(16);
    WG_BARRIER();
    STAMP(1);
    WAITV(8);
#pragma unroll 1
    for (int s = 0; s < 10; ++s) {
      WG_BARRIER();
      QDMA(s + 3);
      WAITV(8);
    }
    WG_BARRIER();
    QDMA(13);
    LOADU(R0, rk, 0);
    WAITV(16);
    WG_BARRIER();
    WAITV(8);
    LOADU(R1, rk, 1);
    WG_BARRIER();
    __builtin_amdgcn_raw_buffer_store_b32(hs_r + 1u, rc, hs ? lane * 512 + 128 + (lid & 7) * 4 : OOB, 0, AUX_SC1);
    WG_BARRIER();
    LOADU(SA, rv, 0);
    STAMP(2);
    lane = lane_id(); l16 = lane * 16; hs = h == 0 && lane < 8;
    const int hi = lane >> 5, ch = lane & 31;
    const int row0 = 2 * h + hi;
    const int kw0 = LDS_K + row0 * 512 + ((ch ^ row0) << 4), kw1 = LDS_K + (row0 + 8) * 512 + ((ch ^ row0 ^ 8) << 4);
    const int vw0 = LDS_V + row0 * 512 + ((ch ^ (row0 << 1)) << 4);
#define WRITEK(SET, BUF) do { _Pragma("unroll") for (int i = 0; i < 8; ++i) \
      lds_w128(smem + ((i & 1) ? kw1 : kw0) + (BUF) + (i >> 1) * 8192, SET[i]); SBAR(); } while (0)
#define WRITEV(SET, BUF) do { _Pragma("unroll") for (int i = 0; i < 8; ++i) \
      lds_w128(smem + vw0 + (BUF) + i * 4096, SET[i]); SBAR(); } while (0)
    const int kv0 = row0 * 512 + ((ch ^ row0) << 4), kv1 = row0 * 512 + ((ch ^ row0 ^ 8) << 4), vv0 = row0 * 512 + ((ch ^ (row0 << 1)) << 4);
#define KVO(x) (x)
#define KDMA(TILE, HALF) do { _Pragma("unroll") for (int i = 0; i < 4; ++i) \
      dmakv(rk, KVO((i & 1) ? kv1 : kv0), (TILE) * 32768 + (HALF) * 16384 + i * 4096, LDS_K + ((TILE) & 1) * 32768 + (HALF) * 16384 + i * 4096 + h * 1024); SBAR(); } while (0)
#define VDMA(TILE, HALF) do { _Pragma("unroll") for (int i = 0; i < 4; ++i) \
      dmakv(rv, KVO(vv0), (TILE) * 32768 + (HALF) * 16384 + i * 4096, LDS_V + ((TILE) & 1) * 32768 + (HALF) * 16384 + i * 4096 + h * 1024); SBAR(); } while (0)
    WRITEK(R0, 0);
    WG_BARRIER();
    WRITEK(R1, 32768);
    WG_BARRIER();
    WRITEV(SA, 0);
    STAMP(4);
    VDMA(1, 0); KDMA(2, 0);
    WG_BARRIER();
    WG_BARRIER();
    VDMA(1, 1); KDMA(2, 1);
    WAITV(8);
    const unsigned hs_c0 = 0; (void)hs_c0;
#pragma unroll 1
    for (int n = 1; n < 27; ++n) {
      WG_BARRIER();
      const int tv = (n + 3) >> 1, hf = (n + 3) & 1;
      VDMA(tv, hf); KDMA(tv + 1, hf);
      if (n == 20 && hs) hs_echo(ws, lid, lane);
      WAITV(8);
      STAMP_IF(n == 8, 5); STAMP_IF(n == 16, 6); STAMP_IF(n == 24, 7);
    }
    WG_BARRIER();
    VDMA(15, 0);
    WAITV(4);
    WG_BARRIER();
    VDMA(15, 1);
    ODMA(0);
    WAITV(12);
    WG_BARRIER();
    WAITV(8);
#undef KDMA
#undef VDMA
#undef KVO
    STAMP(8);
    WG_BARRIER();
    ODMA(1);
    WAITV(8);
    WG_BARRIER();
    STAMP(10);
    ODMA(2);
    WAITV(8);
#pragma unroll 1
    for (int s = 0; s < 13; ++s) {
      WG_BARRIER();
      ODMA(s + 3);
      WAITV(8);
    }
    WG_BARRIER();
    WAITV(0);
    WG_BARRIER();
    WG_BARRIER();
    STAMP(11);
    if (h == 0) {
      const unsigned* cnt = (const unsigned*)(ws + CTL_OFF + (size_t)lid * 512);
      for (unsigned spins = 0; spins < SPIN_CAP; ++spins) {
        if (__hip_atomic_load(cnt, RLX_AGENT) >= 32u) break;
        if ((spins & 63u) == 63u) { const int l = lane_id(); if (l < 8) hs_echo(ws, lid, l); }
        __builtin_amdgcn_s_sleep(2);
      }
    }
#undef QDMA
#undef ODMA
#undef LOADU
#undef WRITEK
#undef WRITEV
#undef voff
#undef wqv
#undef wov
  } else {
    const unsigned c0_ = (unsigned)__builtin_amdgcn_s_memtime(), r0_ = (unsigned)__builtin_amdgcn_s_memrealtime();
    {
      const int lane = lane_id();
      const rsrc_t rx = mk_rsrc((blk ? xB : xA) + (size_t)bb * SQ * DM * 2, SQ * DM * 2);
      const int xp = ((wid + (kvh >> 1)) & 3) * 14;
      u32x4 xr[14];
#pragma unroll
      for (int f = 0; f < 14; ++f) xr[f] = __builtin_amdgcn_raw_buffer_load_b128(rx, lane * 16, (xp + f) * 1024, XAUX);
      SBAR();
      {
        const char* p0 = wid == 0 ? (blk ? kB : kA) + (size_t)(bb * HKV + kvh) * KV_BYTES
                       : wid == 1 ? (blk ? woB : woA) + (size_t)kvh * 512 * DM * 2
                       : wid == 2 ? (const char*)ws + (size_t)(lid >> 3) * 64 * SLAB : (const char*)out + (size_t)(bb * SQ + kvh * 8) * (2 * DM) * 2;
        const char* p1 = wid == 0 ? (blk ? vB : vA) + (size_t)(bb * HKV + kvh) * KV_BYTES
                       : wid == 1 ? p0 + 512 * DM * 2 - 64 : wid == 2 ? p0 + 64 * SLAB - 64 : p0 + 8 * (2 * DM) * 2 - 64;
        const unsigned long long a0 = (unsigned long long)p0, a1 = (unsigned long long)p1;
        const unsigned long long u0 = ((unsigned long long)__builtin_amdgcn_readfirstlane((unsigned)(a0 >> 32)) << 32) | (unsigned)__builtin_amdgcn_readfirstlane((unsigned)a0);
        const unsigned long long u1 = ((unsigned long long)__builtin_amdgcn_readfirstlane((unsigned)(a1 >> 32)) << 32) | (unsigned)__builtin_amdgcn_readfirstlane((unsigned)a1);
        unsigned d0, d1;
        asm volatile("s_nop 4\n\ts_load_dword %0, %2, 0x0\n\ts_load_dword %1, %3, 0x0\n\ts_waitcnt lgkmcnt(0)" : "=&s"(d0), "=&s"(d1) : "s"(u0), "s"(u1) : "memory");
      }
#pragma unroll
      for (int f = 0; f < 14; ++f) {
        const unsigned idx = (xp + f) * 64 + lane, row = idx / 56u, q = idx - row * 56u;
        lds_w128(smem + LDS_X + (q >> 2) * 4096 + (row >> 4) * 1024 + (q & 3) * 256 + (((row & 15) ^ (q & 7)) << 4), xr[f]);
      }
    }
    WG_BARRIER();
    const int fastclk = __builtin_amdgcn_readfirstlane((int)(((unsigned)__builtin_amdgcn_s_memtime() - c0_) * 16u - ((unsigned)__builtin_amdgcn_s_memrealtime() - r0_) * (unsigned)AP_TH));
    __builtin_amdgcn_s_waitcnt(0xC07F); SBAR();
    STAMP(1);
    {
      f32x4 acc[8][4]; PFrag<8> F[2];
#pragma unroll
      for (int i = 0; i < 8; ++i)
#pragma unroll
        for (int rt = 0; rt < 4; ++rt) acc[i][rt] = f32x4{0.f, 0.f, 0.f, 0.f};
      const int ab = pabase<QRS>(wid * 8), bbs = pbbase(LDS_X);
      { const int aa = ab + qslot(0), ba = pbaddr(bbs, kq0); static_for<20>([&](auto kc) { pread<8, QRS, decltype(kc)::value>(F[0], smem, aa, ba); }); }
      int sl = 1;
#pragma unroll 1
      for (int s = 0; s < 14; s += 2) {
        WG_BARRIER();
        pstep<8, QRS>(acc, F[0], F[1], smem, ab + qslot(sl), pbaddr(bbs, KQ(s + 1)));
        sl = sl == 2 ? 0 : sl + 1;
        WG_BARRIER();
        pstep<8, QRS>(acc, F[1], F[0], smem, ab + qslot(sl), pbaddr(bbs, KQ(s + 2)));
        sl = sl == 2 ? 0 : sl + 1;
      }
      q_handover(acc, smem, wid);
    }
    STAMP(2);
    {
      const int w = wid, hh = w >> 1, rh = w & 1;
      const int lane = lane_id(), g = lane >> 4, c = lane & 15;
      f32x4 o[16][2];
#pragma unroll
      for (int dt = 0; dt < 16; ++dt) { o[dt][0] = f32x4{0.f, 0.f, 0.f, 0.f}; o[dt][1] = f32x4{0.f, 0.f, 0.f, 0.f}; }
      float mc[2] = {0.f, 0.f}, lsum[2] = {0.f, 0.f};
      f32x4 s0[2][2], s1[2][2];
      u32x4 p0[2], p1[2];
      u32x4 qown[4][2];
      int kb = LDS_K + c * 512 + ((g ^ c) << 4);
      int vb = LDS_V + (4 * g + (c >> 2)) * 512 + ((((4 * g + (c >> 2)) & 7)) << 5) + (c & 3) * 8;
      LAS char* qp = smem + LDS_QP + w * 8192 + lane * 16;
      constexpr int PD = 4, RN = 6;
      bf16x4 vl[RN], vh[RN]; bf16x8 kf[RN], qf[2][2];
      float t0, t1, t2, t3, mx;
      int fa = 0;
#define XORI(IMM, V) asm volatile("v_xor_b32 %0, %1, %2" : "=v"(fa) : "i"(IMM), "v"(V))
#define FETCH(N) do { constexpr int n_ = (N), blk_ = n_ >> 4, j_ = n_ & 15; \
        if (blk_ & 1) { if ((j_ & 3) == 0) XORI((j_ >> 2) << 6, kb); \
          kf[n_ % RN] = asbf(lds_r128(smem + fa + ((j_ >> 1) & 1) * 256 + (j_ & 1) * 8192 + (blk_ >> 1) * 16384)); } \
        else { if ((j_ & 1) == 0) XORI((j_ >> 1) << 5, vb); \
          LAS char* a_ = smem + fa + (j_ & 1) * 256 + (blk_ >> 1) * 16384; vl[n_ % RN] = lds_tr(a_); vh[n_ % RN] = lds_tr(a_ + 8192); } } while (0)
#define FETCHQ(V, RT) qf[(V) & 1][RT] = asbf(lds_r128(qp + ((V) * 2 + (RT)) * 1024))
#define XE(X, e) X[(e) >> 3][((e) >> 2) & 1][(e) & 3]
#define JOB_AB(X, j) do { \
        if ((j) < 4) { _Pragma("unroll") for (int i_ = 0; i_ < 4; ++i_) XE(X, 4 * (j) + i_) = fmaf(XE(X, 4 * (j) + i_), SCL, -mc[(j) & 1]); } \
        else if ((j) == 4) { t0 = max3(X[0][0][0], X[0][0][1], X[0][0][2]); t1 = max3(X[0][1][0], X[0][1][1], X[0][1][2]); t2 = max3(X[0][0][3], X[1][0][0], X[1][0][1]); } \
        else if ((j) == 5) { t3 = max3(X[0][1][3], X[1][1][0], X[1][1][1]); t0 = max3(t0, X[1][0][2], X[1][0][3]); t1 = max3(t1, X[1][1][2], X[1][1][3]); } \
        else if ((j) == 6) { t0 = fmaxf(t0, t2); t1 = fmaxf(t1, t3); } \
        else if ((j) == 7) { auto a_ = __builtin_amdgcn_permlane16_swap(__float_as_uint(t0), __float_as_uint(t1), false, false); \
                             t0 = fmaxf(__uint_as_float(a_[0]), __uint_as_float(a_[1])); } \
        else if ((j) == 8) { mx = hmax(t0); } } while (0)
#define JOB_EXP(X, P, j) do { XE(X, j) = __builtin_amdgcn_exp2f(XE(X, j)); \
        if ((j) > 0) lsum[(((j) - 1) >> 2) & 1] += XE(X, (j) - 1); \
        if ((j) >= 2 && !((j) & 1)) P[(((j) - 2) >> 2) & 1][(((j) - 2) >> 3) * 2 + ((((j) - 2) >> 1) & 1)] = pk2(XE(X, (j) - 2), XE(X, (j) - 1)); \
        if ((j) == 15) { lsum[1] += XE(X, 15); P[1][3] = pk2(XE(X, 14), XE(X, 15)); } } while (0)
#define MX2(M) do { auto m_ = __builtin_amdgcn_permlane16_swap(__float_as_uint(mx), __float_as_uint(mx), false, false); \
        M[0] = __uint_as_float(m_[0]); M[1] = __uint_as_float(m_[1]); } while (0)
#define RESCALE(X, PEND) do { if (__any(mx > THR)) { float d_[2], al_[2]; MX2(d_); \
          _Pragma("unroll") for (int r_ = 0; r_ < 2; ++r_) { d_[r_] = fmaxf(d_[r_], 0.f); al_[r_] = __builtin_amdgcn_exp2f(-d_[r_]); mc[r_] += d_[r_]; lsum[r_] *= al_[r_]; \
            X[0][r_] -= d_[r_]; X[1][r_] -= d_[r_]; \
            _Pragma("unroll") for (int dt = 0; dt < 16; ++dt) o[dt][r_] *= al_[r_]; \
            if (PEND) { _Pragma("unroll") for (int i_ = 0; i_ < 4; ++i_) \
              p0[r_][i_] = pk2(__uint_as_float(p0[r_][i_] << 16) * al_[r_], __uint_as_float(p0[r_][i_] & 0xffff0000u) * al_[r_]); } } } SBAR(); } while (0)
#define FETCH_AHEAD(B, j) switch (j) { \
            case 0: FETCH((B) * 16 + 0 + PD); break; case 1: FETCH((B) * 16 + 1 + PD); break; case 2: FETCH((B) * 16 + 2 + PD); break; case 3: FETCH((B) * 16 + 3 + PD); break; \
            case 4: FETCH((B) * 16 + 4 + PD); break; case 5: FETCH((B) * 16 + 5 + PD); break; case 6: FETCH((B) * 16 + 6 + PD); break; case 7: FETCH((B) * 16 + 7 + PD); break; \
            case 8: FETCH((B) * 16 + 8 + PD); break; case 9: FETCH((B) * 16 + 9 + PD); break; case 10: FETCH((B) * 16 + 10 + PD); break; case 11: FETCH((B) * 16 + 11 + PD); break; \
            case 12: FETCH(((B) * 16 + 12 + PD) & 63); break; case 13: FETCH(((B) * 16 + 13 + PD) & 63); break; case 14: FETCH(((B) * 16 + 14 + PD) & 63); break; default: FETCH(((B) * 16 + 15 + PD) & 63); break; }
#define PVBLOCK(B, P, X, LAST, PN) do { _Pragma("unroll") for (int j = 0; j < 16; ++j) { \
          const bf16x8 a_ = __builtin_shufflevector(vl[((B) * 16 + j) % RN], vh[((B) * 16 + j) % RN], 0, 1, 2, 3, 4, 5, 6, 7); \
          o[(j >> 1) + 8 * (j & 1)][0] = MFMA(a_, asbf(P[0]), o[(j >> 1) + 8 * (j & 1)][0]); \
          if (j + PD < 16 || !(LAST)) { FETCH_AHEAD(B, j) } \
          SBAR(); \
          o[(j >> 1) + 8 * (j & 1)][1] = MFMA(a_, asbf(P[1]), o[(j >> 1) + 8 * (j & 1)][1]); \
          if (!(LAST) && j >= 14) FETCHQ(0, j - 14); \
          if (!(LAST)) JOB_AB(X, j); \
          PACE(PN); \
          SBAR(); } } while (0)
#define QKBLOCK(B, S, NEXT, JOB, X, P, PN) do { _Pragma("unroll") for (int j = 0; j < 16; ++j) { \
          const bf16x8 q0_ = (j & 2) ? qf[(j >> 2) & 1][0] : asbf(qown[j >> 2][0]), q1_ = (j & 2) ? qf[(j >> 2) & 1][1] : asbf(qown[j >> 2][1]); \
          const f32x4 z_ = {0.f, 0.f, 0.f, 0.f}; \
          S[j & 1][0] = MFMA(kf[((B) * 16 + j) % RN], q0_, j < 2 ? z_ : S[j & 1][0]); \
          if (j + PD < 16 || (NEXT)) { FETCH_AHEAD(B, j) } \
          SBAR(); \
          S[j & 1][1] = MFMA(kf[((B) * 16 + j) % RN], q1_, j < 2 ? z_ : S[j & 1][1]); \
          if (!(j & 2) && j >= 4) FETCHQ(j >> 2, j & 1); \
          if (JOB) JOB_EXP(X, P, j); \
          PACE(PN); \
          SBAR(); } } while (0)
#define FETCH4(N) do { FETCH(N); FETCH((N) + 1); FETCH((N) + 2); FETCH((N) + 3); } while (0)
      static_assert(PD == 4, "FETCH4");
#define RAWBAR() do { SBAR(); asm volatile("s_barrier" ::: "memory"); SBAR(); } while (0)
      WG_BARRIER();
      STAMP(3);
#pragma unroll
      for (int f = 0; f < 8; ++f) qown[f >> 1][f & 1] = lds_r128(smem + LDS_QT + w * 8192 + f * 1024 + lane * 16);
      FETCH4(16); FETCHQ(0, 0); FETCHQ(0, 1); SBAR();
      QKBLOCK(1, s0, 0, 0, s0, p0, PACEN);
      FETCH4(48); FETCHQ(0, 0); FETCHQ(0, 1); SBAR();
#pragma unroll
      for (int j = 0; j < 9; ++j) JOB_AB(s0, j);
      MX2(mc);
#pragma unroll
      for (int r = 0; r < 2; ++r) { s0[0][r] -= mc[r]; s0[1][r] -= mc[r]; }
      RAWBAR();
      QKBLOCK(3, s1, 0, 1, s0, p0, PACEN);
      kb ^= 32768;
      RAWBAR();
      FETCH4(0); SBAR();
#define LSTAMP() do { STAMP_IF(t == 0, 4); STAMP_IF(t == 4, 5); STAMP_IF(t == 8, 6); STAMP_IF(t == 12, 7); } while (0)
#define ATT_LOOP(PN, T0, T1) _Pragma("unroll 1") for (int t = (T0); t < (T1); ++t) { \
        RAWBAR();     \
        LSTAMP(); \
        PVBLOCK(0, p0, s1, 0, PN); \
        RESCALE(s1, 0); \
        QKBLOCK(1, s0, 1, 1, s1, p1, PN); \
        RAWBAR();     \
        PVBLOCK(2, p1, s0, 0, PN); \
        vb ^= 32768; \
        RESCALE(s0, 0); \
        QKBLOCK(3, s1, 1, 1, s0, p0, PN); \
        kb ^= 32768; }
      const int nslow = fastclk > 0 ? AP_N : 0;
      ATT_LOOP(AP_P, 0, nslow)
      ATT_LOOP(AP_P2, nslow, 15)
#undef ATT_LOOP
#undef LSTAMP
#pragma unroll
      for (int j = 0; j < 9; ++j) JOB_AB(s1, j);
      RESCALE(s1, 1);
#pragma unroll
      for (int j = 0; j < 16; ++j) JOB_EXP(s1, p1, j);
      float iv[2];
#pragma unroll
      for (int r = 0; r < 2; ++r) {
        auto a = __builtin_amdgcn_permlane16_swap(__float_as_uint(lsum[r]), __float_as_uint(lsum[r]), false, false);
        iv[r] = 1.f / hsum(__uint_as_float(a[0]) + __uint_as_float(a[1]));
      }
      SBAR();
      PVBLOCK(0, p0, s1, 1, -1);
      RAWBAR();
      STAMP(8);
      FETCH4(32); SBAR();
      PVBLOCK(2, p1, s0, 1, -1);
#undef FETCH
#undef XORI
#undef FETCHQ
#undef FETCH4
#undef FETCH_AHEAD
#undef JOB_AB
#undef JOB_EXP
#undef RESCALE
#undef MX2
#undef XE
#undef PVBLOCK
#undef QKBLOCK
      STAMP(9);
      const int lane2 = lane_id(), g2 = lane2 >> 4, c2 = lane2 & 15;
      LAS char* ow = smem + LDS_V + hh * 65536 + rh * 2048 + g2 * 256;
#pragma unroll
      for (int kk = 0; kk < 8; ++kk)
#pragma unroll
        for (int r = 0; r < 2; ++r) {
          const f32x4 a = o[2 * kk][r] * iv[r], b = o[2 * kk + 1][r] * iv[r];
          lds_w128(ow + kk * 4096 + r * 1024 + ((c2 ^ g2 ^ ((kk & 1) * 4)) << 4), u32x4{pk2(a[0], a[1]), pk2(a[2], a[3]), pk2(b[0], b[1]), pk2(b[2], b[3])});
        }
    }
    WG_BARRIER();
    STAMP(10);
    {
      HS_REQ();
      f32x4 acc[7][4]; PFrag<7> F[2];
#pragma unroll
      for (int i = 0; i < 7; ++i)
#pragma unroll
        for (int rt = 0; rt < 4; ++rt) acc[i][rt] = f32x4{0.f, 0.f, 0.f, 0.f};
      const int ab = pabase<ORS>(wid * 7), bbs = pbbase(LDS_V);
      { const int aa = ab + oslot(0), ba = obaddr(bbs, KO(0)); static_for<18>([&](auto kc) { pread<7, ORS, decltype(kc)::value>(F[0], smem, aa, ba); }); }
      int sl = 1;
#pragma unroll 1
      for (int s = 0; s < 16; s += 2) {
        WG_BARRIER();
        pstep<7, ORS>(acc, F[0], F[1], smem, ab + oslot(sl), obaddr(bbs, KO(s + 1)));
        sl = sl == 2 ? 0 : sl + 1;
        WG_BARRIER();
        pstep<7, ORS>(acc, F[1], F[0], smem, ab + oslot(sl), obaddr(bbs, KO(s + 2)));
        sl = sl == 2 ? 0 : sl + 1;
      }
      STAMP(11);
      publish<7>(acc, ws, lid, wid * 7, HS_OK());
    }
  }
  WG_BARRIER();
  STAMP(15);
  if (wid < 7) {
    const int lane = lane_id();
    const rsrc_t rp = mk_rsrc(ws + ((size_t)(lid >> 3) * 64 + kvh * 8) * SLAB, 8 * SLAB);
    u32x4 v[8];
#pragma unroll
    for (int s = 0; s < 8; ++s) v[s] = __builtin_amdgcn_raw_buffer_load_b128(rp, wid * 1024 + lane * 16, s * SLAB, AUX_SC1);
    float a[8];
#pragma unroll
    for (int k = 0; k < 4; ++k) { a[2 * k] = __uint_as_float(v[0][k] << 16); a[2 * k + 1] = __uint_as_float(v[0][k] & 0xffff0000u); }
#pragma unroll
    for (int s = 1; s < 8; ++s)
#pragma unroll
      for (int k = 0; k < 4; ++k) { a[2 * k] += __uint_as_float(v[s][k] << 16); a[2 * k + 1] += __uint_as_float(v[s][k] & 0xffff0000u); }
    const rsrc_t ro = mk_rsrc(out + (size_t)(bb * SQ + kvh * 8) * (2 * DM) * 2, 8 * 2 * DM * 2);
    __builtin_amdgcn_raw_buffer_store_b128(u32x4{pk2(a[0], a[1]), pk2(a[2], a[3]), pk2(a[4], a[5]), pk2(a[6], a[7])}, ro,
                                           ((lane & 7) * (2 * DM) + blk * DM + wid * 64 + (lane >> 3) * 8) * 2, 0, OUT_AUX);
  }
  STAMP(12); STAMPC(14); STAMP_OUT();
}
}

extern "C" void kernel_launch(void* const* d_in, const int* in_sizes, int n_in, void* d_out, int out_size, void* d_ws, size_t ws_size,
                              hipStream_t stream) {
  (void)in_sizes; (void)n_in; (void)out_size; (void)ws_size;
  gqa_fused<<<256, 512, 0, stream>>>((const char*)d_in[0], (const char*)d_in[1], (const char*)d_in[2], (const char*)d_in[3], (const char*)d_in[4],
                                     (const char*)d_in[5], (const char*)d_in[6], (const char*)d_in[7], (const char*)d_in[8], (const char*)d_in[9],
                                     (char*)d_out, (char*)d_ws);
}
```

```cpp
#include <hip/hip_runtime.h>
#include <hip/hip_bf16.h>
#include <utility>
#include <type_traits>

#define LAS __attribute__((address_space(3)))
#define DI __device__ __forceinline__
typedef __attribute__((ext_vector_type(8))) __bf16 bf16x8;
typedef __attribute__((ext_vector_type(4))) __bf16 bf16x4;
typedef __attribute__((ext_vector_type(2))) __bf16 bf16x2;
typedef __attribute__((ext_vector_type(4))) float f32x4;
typedef __attribute__((ext_vector_type(2))) float f32x2;
typedef __attribute__((ext_vector_type(4))) unsigned u32x4;

namespace {
constexpr int SQ = 64, DM = 448, HKV = 8, SK = 1024, DH = 256;
constexpr int NQ = 4096;
constexpr int KV_BYTES = SK * DH * 2;
constexpr int W_BYTES = DM * NQ * 2;
constexpr int LDS_K = 0, LDS_V = 65536, LDS_QP = 131072, LDS_TOTAL = 163840;
constexpr int LDS_X = 34816, LDS_QT = LDS_V + 32768;
constexpr int SLAB = 7168;
constexpr size_t CTL_OFF = (size_t)16 << 20;
constexpr float SCL = 0.0625f * 1.4426950408889634f;
constexpr float THR = 8.f;
constexpr int AUX_NT = 2, AUX_SC1 = 16;
constexpr int OOB = 0x40000000;
constexpr unsigned SPIN_CAP = 1u << 21;

#define SBAR() __builtin_amdgcn_sched_barrier(0)
#define WG_BARRIER() do { SBAR(); asm volatile("s_waitcnt lgkmcnt(0)\n\ts_barrier" ::: "memory"); SBAR(); } while (0)
#define PACE(PN) do { if ((PN) >= 0) asm volatile("s_nop %0" :: "i"((PN) < 0 ? 0 : (PN))); } while (0)
constexpr int AP_TH = 328;
constexpr int AP_P = 3;
#define MFMA(a, b, c) __builtin_amdgcn_mfma_f32_16x16x32_bf16((a), (b), (c), 0, 0, 0)
#define RLX_AGENT __ATOMIC_RELAXED, __HIP_MEMORY_SCOPE_AGENT

DI unsigned pk2(float a, float b) { f32x2 f = {a, b}; bf16x2 r = __builtin_convertvector(f, bf16x2); return __builtin_bit_cast(unsigned, r); }
DI bf16x8 asbf(u32x4 v) { return __builtin_bit_cast(bf16x8, v); }
DI u32x4 lds_r128(LAS char* p) { return *(LAS u32x4*)p; }
DI void lds_w128(LAS char* p, u32x4 v) { *(LAS u32x4*)p = v; }
DI bf16x4 lds_tr(LAS char* p) { return __builtin_amdgcn_ds_read_tr16_b64_v4bf16((LAS bf16x4*)p); }
DI float max3(float a, float b, float c) { return fmaxf(fmaxf(a, b), c); }
DI float hmax(float v) {
  auto b = __builtin_amdgcn_permlane32_swap(__float_as_uint(v), __float_as_uint(v), false, false);
  return fmaxf(__uint_as_float(b[0]), __uint_as_float(b[1]));
}
DI float hsum(float v) {
  auto b = __builtin_amdgcn_permlane32_swap(__float_as_uint(v), __float_as_uint(v), false, false);
  return __uint_as_float(b[0]) + __uint_as_float(b[1]);
}
DI int lane_id() {
  unsigned m = ~0u; asm volatile("" : "+s"(m)); return (int)__builtin_amdgcn_mbcnt_hi(m, __builtin_amdgcn_mbcnt_lo(m, 0u)); }
typedef __amdgpu_buffer_rsrc_t rsrc_t;
DI rsrc_t mk_rsrc(const void* p, int bytes) { return __builtin_amdgcn_make_buffer_rsrc((void*)p, 0, bytes, 0x00020000); }
template <class F, int... I> DI void static_for_(F&& f, std::integer_sequence<int, I...>) { (f(std::integral_constant<int, I>{}), ...); }
template <int N, class F> DI void static_for(F&& f) { static_for_(f, std::make_integer_sequence<int, N>{}); }

DI unsigned* ctl_chal(char* ws, int wg, int slot) { return (unsigned*)(ws + CTL_OFF + (size_t)wg * 512 + 128 + slot * 4); }
DI unsigned* ctl_resp(char* ws, int wg, int slot) { return (unsigned*)(ws + CTL_OFF + (size_t)wg * 512 + 256 + slot * 4); }
DI void hs_echo(char* ws, int lid, int lane) {
  const unsigned cv = __hip_atomic_load(ctl_chal(ws, lid, lane), RLX_AGENT);
  const unsigned rv = __hip_atomic_load(ctl_resp(ws, (lid & ~7) + lane, lid & 7), RLX_AGENT);
  if (cv != rv) __hip_atomic_store(ctl_resp(ws, (lid & ~7) + lane, lid & 7), cv, RLX_AGENT);
}
DI u32x4 ld1(rsrc_t rs, int voff, int soff) { return __builtin_amdgcn_raw_buffer_load_b128(rs, voff, soff, AUX_NT); }
DI void ld8(u32x4 (&r)[8], rsrc_t rs, int voff, int soff, int sa, int sb) {
#pragma unroll
  for (int j = 0; j < 8; ++j) r[j] = ld1(rs, voff, soff + (j & 3) * sa + (j >> 2) * sb);
}

DI void dma_nt(rsrc_t rs, int voff, int soff, int ldsaddr) {
  asm volatile("s_mov_b32 m0, %0\n\ts_nop 0\n\tbuffer_load_dwordx4 %1, %2, %3 offen nt lds" :: "s"(ldsaddr), "v"(voff), "s"(rs), "s"(soff) : "memory");
}
DI void dma56(rsrc_t rs, int voff, int soff, int ldsaddr) {
  asm volatile("s_mov_b32 m0, %0\n\ts_mov_b32 exec_hi, 0xffffff\n\tbuffer_load_dwordx4 %1, %2, %3 offen lds\n\ts_mov_b32 exec_hi, -1"
               :: "s"(ldsaddr), "v"(voff), "s"(rs), "s"(soff) : "memory");
}
#define WAITV(N) do { SBAR(); asm volatile("s_waitcnt vmcnt(" #N ")" ::: "memory"); SBAR(); } while (0)
template <int NF> struct PFrag { bf16x4 lo[NF], hi[NF]; bf16x8 b[4]; };
template <int RS> DI int pabase(int f0) {
  const int lane = lane_id(), g = lane >> 4, q = (lane & 15) >> 2, p = lane & 3;
  return (16 * (g >> 1) + 4 * (g & 1) + q) * RS + f0 * 32 + p * 8;
}
DI int pbbase(int bimg) { const int lane = lane_id(), g = lane >> 4, c = lane & 15; return bimg + g * 256 + ((c ^ g) << 4); }
DI int pbaddr(int bb, int ks) { return (bb ^ ((ks & 1) << 6)) + ks * 4096; }
template <int NF, int RS, int K> DI void pread(PFrag<NF>& f, LAS char* smem, int aa, int ba) {
  if constexpr (K < 4) f.b[K] = asbf(lds_r128(smem + ba + K * 1024));
  else if constexpr (K < 4 + 2 * NF) {
    if constexpr ((K & 1) == 0) f.lo[(K - 4) >> 1] = lds_tr(smem + aa + ((K - 4) >> 1) * 32);
    else f.hi[(K - 4) >> 1] = lds_tr(smem + aa + ((K - 4) >> 1) * 32 + 8 * RS);
  }
}
template <int NF, int RS>
DI void pstep(f32x4 (&acc)[NF][4], const PFrag<NF>& cur, PFrag<NF>& nxt, LAS char* smem, int aa, int ba) {
  static_for<NF * 4>([&](auto nc) {
    constexpr int n = decltype(nc)::value, i = n >> 2, rt = n & 3;
    const bf16x8 a = __builtin_shufflevector(cur.lo[i], cur.hi[i], 0, 1, 2, 3, 4, 5, 6, 7);
    acc[i][rt] = MFMA(a, cur.b[rt], acc[i][rt]);
    pread<NF, RS, n>(nxt, smem, aa, ba);
    SBAR();
  });
}
template <int NF> DI u32x4 ppack(const f32x4 (&acc)[NF][4], int i, int rp) {
  auto lo = __builtin_amdgcn_permlane16_swap(pk2(acc[i][2 * rp][0], acc[i][2 * rp][1]), pk2(acc[i][2 * rp + 1][0], acc[i][2 * rp + 1][1]), false, false);
  auto hi = __builtin_amdgcn_permlane16_swap(pk2(acc[i][2 * rp][2], acc[i][2 * rp][3]), pk2(acc[i][2 * rp + 1][2], acc[i][2 * rp + 1][3]), false, false);
  return u32x4{lo[0], hi[0], lo[1], hi[1]};
}
DI void q_handover(const f32x4 (&acc)[8][4], LAS char* smem, int w) {
  const int lane = lane_id(), g = lane >> 4, c = lane & 15;
  LAS char* base = smem + ((w & 1) ? LDS_QP : LDS_QT) + (w >> 1) * 16384 + (g & 1) * 1024 + (g >> 1) * 256 + c * 16;
#pragma unroll
  for (int i = 0; i < 8; ++i)
#pragma unroll
    for (int rp = 0; rp < 2; ++rp) lds_w128(base + rp * 8192 + (i >> 1) * 2048 + (i & 1) * 512, ppack<8>(acc, i, rp));
}
template <int NF> DI void publish(const f32x4 (&acc)[NF][4], char* ws, int lid, int f0, bool ok) {
  const int lane = lane_id(), g = lane >> 4, c = lane & 15;
  const rsrc_t rp = mk_rsrc(ws + (size_t)(lid >> 3) * 64 * SLAB, 64 * SLAB);
  const int voff = (((g & 1) * 2 + (c >> 3)) * 8 + (lid & 7)) * SLAB + f0 * 256 + (g >> 1) * 128 + (c & 7) * 16;
#pragma unroll
  for (int i = 0; i < NF; ++i)
#pragma unroll
    for (int r2 = 0; r2 < 2; ++r2) __builtin_amdgcn_raw_buffer_store_b128(ppack<NF>(acc, i, r2), rp, voff, r2 * 32 * SLAB + i * 256, AUX_SC1);
  asm volatile("s_waitcnt vmcnt(0)" ::: "memory");
  for (unsigned spins = 0; !ok && spins < SPIN_CAP; ++spins) {
    if (lane < 8) hs_echo(ws, lid, lane);
    const unsigned cv = __hip_atomic_load(ctl_chal(ws, (lid & ~7) + (lane & 7), lid & 7), RLX_AGENT);
    const unsigned rv = __hip_atomic_load(ctl_resp(ws, lid, lane & 7), RLX_AGENT);
    ok = __all(cv == rv);
    __builtin_amdgcn_s_sleep(8);
  }
  if (lane < 8) __hip_atomic_fetch_add((unsigned*)(ws + CTL_OFF + (size_t)((lid & ~7) + lane) * 512), 1u, RLX_AGENT);
}
constexpr int QRS = 1056, ORS = 928;
DI int qslot(int i) { return i == 0 ? 92160 : (i == 1 ? 126976 : 0); }
DI int oslot(int i) { return i == 0 ? LDS_K : (i == 1 ? LDS_K + 32768 : LDS_V + 32768); }
DI int obaddr(int bb, int ks) { return (bb ^ ((ks & 1) << 6)) + ks * 4096 + (ks >> 3) * 32768; }

__global__ __launch_bounds__(512, 2) void gqa_fused(const char* __restrict__ xA, const char* __restrict__ kA, const char* __restrict__ vA,
                                                    const char* __restrict__ wqA, const char* __restrict__ woA,
                                                    const char* __restrict__ xB, const char* __restrict__ kB, const char* __restrict__ vB,
                                                    const char* __restrict__ wqB, const char* __restrict__ woB,
                                                    char* __restrict__ out, char* ws) {
  __shared__ __attribute__((aligned(1024))) char smem_[LDS_TOTAL];
  LAS char* smem = (LAS char*)smem_;
  const int wid = __builtin_amdgcn_readfirstlane(threadIdx.x >> 6);
  const int bid = blockIdx.x, blk = (bid >> 2) & 1, kvh = (bid & 3) * 2 + (bid >> 7), bb = (bid >> 3) & 15;
  const int lid = (blk * 16 + bb) * 8 + kvh;
  const int kq0 = bb < 14 ? bb : bb - 14;
#define KQ(S) ((kq0 + (S)) >= 14 ? kq0 + (S) - 14 : kq0 + (S))
#define KO(S) (((S) + bb) & 15)
  const rsrc_t rc = mk_rsrc(ws + CTL_OFF + (size_t)(lid & ~7) * 512, 8 * 512);
#define HS_REQ() const unsigned hsc_ = __builtin_amdgcn_raw_buffer_load_b32(rc, (lane_id() & 7) * 512 + 128 + (lid & 7) * 4, 0, AUX_SC1), \
                                hsr_ = __builtin_amdgcn_raw_buffer_load_b32(rc, (lid & 7) * 512 + 256 + (lane_id() & 7) * 4, 0, AUX_SC1)
#define HS_OK() __all(hsc_ == hsr_)

  if (wid >= 4) {
    int lane = lane_id();
    const int h = wid - 4;
    bool hs = h == 0 && lane < 8;
    __builtin_amdgcn_raw_buffer_store_b32(0u, rc, hs && lane == 0 ? (lid & 7) * 512 : OOB, 0, AUX_SC1);
    const unsigned hs_r = __builtin_amdgcn_raw_buffer_load_b32(rc, hs ? (lid & 7) * 512 + 256 + lane * 4 : OOB, 0, AUX_SC1);
    const rsrc_t rwq = mk_rsrc((blk ? wqB : wqA) + kvh * 1024, W_BYTES - kvh * 1024);
    const rsrc_t rwo = mk_rsrc((blk ? woB : woA) + (size_t)kvh * 512 * DM * 2, 512 * DM * 2);
    const rsrc_t rk = mk_rsrc((blk ? kB : kA) + (size_t)(bb * HKV + kvh) * KV_BYTES, KV_BYTES);
    const rsrc_t rv = mk_rsrc((blk ? vB : vA) + (size_t)(bb * HKV + kvh) * KV_BYTES, KV_BYTES);
    int l16 = lane * 16;
    u32x4 R0[8], R1[8], SA[8];
    const int irow = 16 * (h >> 1) + 4 * (h & 1);
#define QDMA(S) do { _Pragma("unroll") for (int j = 0; j < 8; ++j) \
      dma_nt(rwq, l16, (KQ(S) * 32 + 8 * h + j) * (NQ * 2), qslot((S) % 3) + (irow + 8 * (j >> 2) + (j & 3)) * QRS); SBAR(); } while (0)
#define ODMA(S) do { _Pragma("unroll") for (int j = 0; j < 8; ++j) \
      dma56(rwo, l16, (KO(S) * 32 + 4 * h + 16 * (j >> 2) + (j & 3)) * (DM * 2), oslot((S) % 3) + (irow + 8 * (j >> 2) + (j & 3)) * ORS); SBAR(); } while (0)
#define LOADU(SET, RS, TILE) do { ld8(SET, RS, l16, (TILE) * 32768 + h * 1024, 4096, 16384); SBAR(); } while (0)
    QDMA(0); QDMA(1); QDMA(2);
    WAITV(16);
    WG_BARRIER();
    WAITV(8);
#pragma unroll 1
    for (int s = 0; s < 10; ++s) {
      WG_BARRIER();
      QDMA(s + 3);
      WAITV(8);
    }
    WG_BARRIER();
    QDMA(13);
    LOADU(R0, rk, 0);
    WAITV(16);
    WG_BARRIER();
    WAITV(8);
    LOADU(R1, rk, 1);
    WG_BARRIER();
    __builtin_amdgcn_raw_buffer_store_b32(hs_r + 1u, rc, hs ? lane * 512 + 128 + (lid & 7) * 4 : OOB, 0, AUX_SC1);
    WG_BARRIER();
    LOADU(SA, rv, 0);
    lane = lane_id(); l16 = lane * 16; hs = h == 0 && lane < 8;
    const int hi = lane >> 5, ch = lane & 31;
    const int row0 = 2 * h + hi;
    const int kw0 = LDS_K + row0 * 512 + ((ch ^ row0) << 4), kw1 = LDS_K + (row0 + 8) * 512 + ((ch ^ row0 ^ 8) << 4);
    const int vw0 = LDS_V + row0 * 512 + ((ch ^ (row0 << 1)) << 4);
#define WRITEK(SET, BUF) do { _Pragma("unroll") for (int i = 0; i < 8; ++i) \
      lds_w128(smem + ((i & 1) ? kw1 : kw0) + (BUF) + (i >> 1) * 8192, SET[i]); SBAR(); } while (0)
#define WRITEV(SET, BUF) do { _Pragma("unroll") for (int i = 0; i < 8; ++i) \
      lds_w128(smem + vw0 + (BUF) + i * 4096, SET[i]); SBAR(); } while (0)
    const int kv0 = row0 * 512 + ((ch ^ row0) << 4), kv1 = row0 * 512 + ((ch ^ row0 ^ 8) << 4), vv0 = row0 * 512 + ((ch ^ (row0 << 1)) << 4);
#define KDMA(TILE, HALF) do { _Pragma("unroll") for (int i = 0; i < 4; ++i) \
      dma_nt(rk, (i & 1) ? kv1 : kv0, (TILE) * 32768 + (HALF) * 16384 + i * 4096, LDS_K + ((TILE) & 1) * 32768 + (HALF) * 16384 + i * 4096 + h * 1024); SBAR(); } while (0)
#define VDMA(TILE, HALF) do { _Pragma("unroll") for (int i = 0; i < 4; ++i) \
      dma_nt(rv, vv0, (TILE) * 32768 + (HALF) * 16384 + i * 4096, LDS_V + ((TILE) & 1) * 32768 + (HALF) * 16384 + i * 4096 + h * 1024); SBAR(); } while (0)
    WRITEK(R0, 0);
    WG_BARRIER();
    WRITEK(R1, 32768);
    WG_BARRIER();
    WRITEV(SA, 0);
    VDMA(1, 0); KDMA(2, 0);
    WG_BARRIER();
    WG_BARRIER();
    VDMA(1, 1); KDMA(2, 1);
    WAITV(8);
#pragma unroll 1
    for (int n = 1; n < 27; ++n) {
      WG_BARRIER();
      const int tv = (n + 3) >> 1, hf = (n + 3) & 1;
      VDMA(tv, hf); KDMA(tv + 1, hf);
      if (n == 20 && hs) hs_echo(ws, lid, lane);
      WAITV(8);
    }
    WG_BARRIER();
    VDMA(15, 0);
    WAITV(4);
    WG_BARRIER();
    VDMA(15, 1);
    ODMA(0);
    WAITV(12);
    WG_BARRIER();
    WAITV(8);
#undef KDMA
#undef VDMA
    WG_BARRIER();
    ODMA(1);
    WAITV(8);
    WG_BARRIER();
    ODMA(2);
    WAITV(8);
#pragma unroll 1
    for (int s = 0; s < 13; ++s) {
      WG_BARRIER();
      ODMA(s + 3);
      WAITV(8);
    }
    WG_BARRIER();
    WAITV(0);
    WG_BARRIER();
    WG_BARRIER();
    if (h == 0) {
      const unsigned* cnt = (const unsigned*)(ws + CTL_OFF + (size_t)lid * 512);
      for (unsigned spins = 0; spins < SPIN_CAP; ++spins) {
        if (__hip_atomic_load(cnt, RLX_AGENT) >= 32u) break;
        if ((spins & 63u) == 63u) { const int l = lane_id(); if (l < 8) hs_echo(ws, lid, l); }
        __builtin_amdgcn_s_sleep(2);
      }
    }
#undef QDMA
#undef ODMA
#undef LOADU
#undef WRITEK
#undef WRITEV
  } else {
    const unsigned c0_ = (unsigned)__builtin_amdgcn_s_memtime(), r0_ = (unsigned)__builtin_amdgcn_s_memrealtime();
    {
      const int lane = lane_id();
      const rsrc_t rx = mk_rsrc((blk ? xB : xA) + (size_t)bb * SQ * DM * 2, SQ * DM * 2);
      const int xp = ((wid + (kvh >> 1)) & 3) * 14;
      u32x4 xr[14];
#pragma unroll
      for (int f = 0; f < 14; ++f) xr[f] = __builtin_amdgcn_raw_buffer_load_b128(rx, lane * 16, (xp + f) * 1024, AUX_NT);
      SBAR();
#pragma unroll
      for (int f = 0; f < 14; ++f) {
        const unsigned idx = (xp + f) * 64 + lane, row = idx / 56u, q = idx - row * 56u;
        lds_w128(smem + LDS_X + (q >> 2) * 4096 + (row >> 4) * 1024 + (q & 3) * 256 + (((row & 15) ^ (q & 7)) << 4), xr[f]);
      }
    }
    WG_BARRIER();
    const int fastclk = __builtin_amdgcn_readfirstlane((int)(((unsigned)__builtin_amdgcn_s_memtime() - c0_) * 16u - ((unsigned)__builtin_amdgcn_s_memrealtime() - r0_) * (unsigned)AP_TH));
    __builtin_amdgcn_s_waitcnt(0xC07F); SBAR();
    {
      f32x4 acc[8][4]; PFrag<8> F[2];
#pragma unroll
      for (int i = 0; i < 8; ++i)
#pragma unroll
        for (int rt = 0; rt < 4; ++rt) acc[i][rt] = f32x4{0.f, 0.f, 0.f, 0.f};
      const int ab = pabase<QRS>(wid * 8), bbs = pbbase(LDS_X);
      { const int aa = ab + qslot(0), ba = pbaddr(bbs, kq0); static_for<20>([&](auto kc) { pread<8, QRS, decltype(kc)::value>(F[0], smem, aa, ba); }); }
      int sl = 1;
#pragma unroll 1
      for (int s = 0; s < 14; s += 2) {
        WG_BARRIER();
        pstep<8, QRS>(acc, F[0], F[1], smem, ab + qslot(sl), pbaddr(bbs, KQ(s + 1)));
        sl = sl == 2 ? 0 : sl + 1;
        WG_BARRIER();
        pstep<8, QRS>(acc, F[1], F[0], smem, ab + qslot(sl), pbaddr(bbs, KQ(s + 2)));
        sl = sl == 2 ? 0 : sl + 1;
      }
      q_handover(acc, smem, wid);
    }
    {
      const int w = wid, hh = w >> 1, rh = w & 1;
      const int lane = lane_id(), g = lane >> 4, c = lane & 15;
      f32x4 o[16][2];
#pragma unroll
      for (int dt = 0; dt < 16; ++dt) { o[dt][0] = f32x4{0.f, 0.f, 0.f, 0.f}; o[dt][1] = f32x4{0.f, 0.f, 0.f, 0.f}; }
      float mc[2] = {0.f, 0.f}, lsum[2] = {0.f, 0.f};
      f32x4 s0[2][2], s1[2][2];
      u32x4 p0[2], p1[2];
      u32x4 qown[4][2];
      int kb = LDS_K + c * 512 + ((g ^ c) << 4);
      int vb = LDS_V + (4 * g + (c >> 2)) * 512 + ((((4 * g + (c >> 2)) & 7)) << 5) + (c & 3) * 8;
      LAS char* qp = smem + LDS_QP + w * 8192 + lane * 16;
      constexpr int PD = 4, RN = 6;
      bf16x4 vl[RN], vh[RN]; bf16x8 kf[RN], qf[2][2];
      float t0, t1, t2, t3, mx;
      int fa = 0;
#define XORI(IMM, V) do { asm volatile("" : "+v"(V)); fa = (V) ^ (IMM); } while (0)
#define FETCH(N) do { constexpr int n_ = (N), blk_ = n_ >> 4, j_ = n_ & 15; \
        if (blk_ & 1) { if ((j_ & 3) == 0) XORI((j_ >> 2) << 6, kb); \
          kf[n_ % RN] = asbf(lds_r128(smem + fa + ((j_ >> 1) & 1) * 256 + (j_ & 1) * 8192 + (blk_ >> 1) * 16384)); } \
        else { if ((j_ & 1) == 0) XORI((j_ >> 1) << 5, vb); \
          LAS char* a_ = smem + fa + (j_ & 1) * 256 + (blk_ >> 1) * 16384; vl[n_ % RN] = lds_tr(a_); vh[n_ % RN] = lds_tr(a_ + 8192); } } while (0)
#define FETCHQ(V, RT) qf[(V) & 1][RT] = asbf(lds_r128(qp + ((V) * 2 + (RT)) * 1024))
#define XE(X, e) X[(e) >> 3][((e) >> 2) & 1][(e) & 3]
#define JOB_AB(X, j) do { \
        if ((j) < 4) { _Pragma("unroll") for (int i_ = 0; i_ < 4; ++i_) XE(X, 4 * (j) + i_) = fmaf(XE(X, 4 * (j) + i_), SCL, -mc[(j) & 1]); } \
        else if ((j) == 4) { t0 = max3(X[0][0][0], X[0][0][1], X[0][0][2]); t1 = max3(X[0][1][0], X[0][1][1], X[0][1][2]); t2 = max3(X[0][0][3], X[1][0][0], X[1][0][1]); } \
        else if ((j) == 5) { t3 = max3(X[0][1][3], X[1][1][0], X[1][1][1]); t0 = max3(t0, X[1][0][2], X[1][0][3]); t1 = max3(t1, X[1][1][2], X[1][1][3]); } \
        else if ((j) == 6) { t0 = fmaxf(t0, t2); t1 = fmaxf(t1, t3); } \
        else if ((j) == 7) { auto a_ = __builtin_amdgcn_permlane16_swap(__float_as_uint(t0), __float_as_uint(t1), false, false); \
                             t0 = fmaxf(__uint_as_float(a_[0]), __uint_as_float(a_[1])); } \
        else if ((j) == 8) { mx = hmax(t0); } } while (0)
#define JOB_EXP(X, P, j) do { XE(X, j) = __builtin_amdgcn_exp2f(XE(X, j)); \
        if ((j) > 0) lsum[(((j) - 1) >> 2) & 1] += XE(X, (j) - 1); \
        if ((j) >= 2 && !((j) & 1)) P[(((j) - 2) >> 2) & 1][(((j) - 2) >> 3) * 2 + ((((j) - 2) >> 1) & 1)] = pk2(XE(X, (j) - 2), XE(X, (j) - 1)); \
        if ((j) == 15) { lsum[1] += XE(X, 15); P[1][3] = pk2(XE(X, 14), XE(X, 15)); } } while (0)
#define MX2(M) do { auto m_ = __builtin_amdgcn_permlane16_swap(__float_as_uint(mx), __float_as_uint(mx), false, false); \
        M[0] = __uint_as_float(m_[0]); M[1] = __uint_as_float(m_[1]); } while (0)
#define RESCALE(X, PEND) do { if (__any(mx > THR)) { float d_[2], al_[2]; MX2(d_); \
          _Pragma("unroll") for (int r_ = 0; r_ < 2; ++r_) { d_[r_] = fmaxf(d_[r_], 0.f); al_[r_] = __builtin_amdgcn_exp2f(-d_[r_]); mc[r_] += d_[r_]; lsum[r_] *= al_[r_]; \
            X[0][r_] -= d_[r_]; X[1][r_] -= d_[r_]; \
            _Pragma("unroll") for (int dt = 0; dt < 16; ++dt) o[dt][r_] *= al_[r_]; \
            if (PEND) { _Pragma("unroll") for (int i_ = 0; i_ < 4; ++i_) \
              p0[r_][i_] = pk2(__uint_as_float(p0[r_][i_] << 16) * al_[r_], __uint_as_float(p0[r_][i_] & 0xffff0000u) * al_[r_]); } } } SBAR(); } while (0)
#define FETCH_AHEAD(B, j) switch (j) { \
            case 0: FETCH((B) * 16 + 0 + PD); break; case 1: FETCH((B) * 16 + 1 + PD); break; case 2: FETCH((B) * 16 + 2 + PD); break; case 3: FETCH((B) * 16 + 3 + PD); break; \
            case 4: FETCH((B) * 16 + 4 + PD); break; case 5: FETCH((B) * 16 + 5 + PD); break; case 6: FETCH((B) * 16 + 6 + PD); break; case 7: FETCH((B) * 16 + 7 + PD); break; \
            case 8: FETCH((B) * 16 + 8 + PD); break; case 9: FETCH((B) * 16 + 9 + PD); break; case 10: FETCH((B) * 16 + 10 + PD); break; case 11: FETCH((B) * 16 + 11 + PD); break; \
            case 12: FETCH(((B) * 16 + 12 + PD) & 63); break; case 13: FETCH(((B) * 16 + 13 + PD) & 63); break; case 14: FETCH(((B) * 16 + 14 + PD) & 63); break; default: FETCH(((B) * 16 + 15 + PD) & 63); break; }
#define PVBLOCK(B, P, X, LAST, PN) do { _Pragma("unroll") for (int j = 0; j < 16; ++j) { \
          const bf16x8 a_ = __builtin_shufflevector(vl[((B) * 16 + j) % RN], vh[((B) * 16 + j) % RN], 0, 1, 2, 3, 4, 5, 6, 7); \
          o[(j >> 1) + 8 * (j & 1)][0] = MFMA(a_, asbf(P[0]), o[(j >> 1) + 8 * (j & 1)][0]); \
          if (j + PD < 16 || !(LAST)) { FETCH_AHEAD(B, j) } \
          SBAR(); \
          o[(j >> 1) + 8 * (j & 1)][1] = MFMA(a_, asbf(P[1]), o[(j >> 1) + 8 * (j & 1)][1]); \
          if (!(LAST) && j >= 14) FETCHQ(0, j - 14); \
          if (!(LAST)) JOB_AB(X, j); \
          PACE(PN); \
          SBAR(); } } while (0)
#define QKBLOCK(B, S, NEXT, JOB, X, P, PN) do { _Pragma("unroll") for (int j = 0; j < 16; ++j) { \
          const bf16x8 q0_ = (j & 2) ? qf[(j >> 2) & 1][0] : asbf(qown[j >> 2][0]), q1_ = (j & 2) ? qf[(j >> 2) & 1][1] : asbf(qown[j >> 2][1]); \
          const f32x4 z_ = {0.f, 0.f, 0.f, 0.f}; \
          S[j & 1][0] = MFMA(kf[((B) * 16 + j) % RN], q0_, j < 2 ? z_ : S[j & 1][0]); \
          if (j + PD < 16 || (NEXT)) { FETCH_AHEAD(B, j) } \
          SBAR(); \
          S[j & 1][1] = MFMA(kf[((B) * 16 + j) % RN], q1_, j < 2 ? z_ : S[j & 1][1]); \
          if (!(j & 2) && j >= 4) FETCHQ(j >> 2, j & 1); \
          if (JOB) JOB_EXP(X, P, j); \
          PACE(PN); \
          SBAR(); } } while (0)
#define FETCH4(N) do { FETCH(N); FETCH((N) + 1); FETCH((N) + 2); FETCH((N) + 3); } while (0)
      static_assert(PD == 4, "FETCH4");
#define RAWBAR() do { SBAR(); asm volatile("s_barrier" ::: "memory"); SBAR(); } while (0)
      WG_BARRIER();
#pragma unroll
      for (int f = 0; f < 8; ++f) qown[f >> 1][f & 1] = lds_r128(smem + LDS_QT + w * 8192 + f * 1024 + lane * 16);
      FETCH4(16); FETCHQ(0, 0); FETCHQ(0, 1); SBAR();
      QKBLOCK(1, s0, 0, 0, s0, p0, -1);
      FETCH4(48); FETCHQ(0, 0); FETCHQ(0, 1); SBAR();
#pragma unroll
      for (int j = 0; j < 9; ++j) JOB_AB(s0, j);
      MX2(mc);
#pragma unroll
      for (int r = 0; r < 2; ++r) { s0[0][r] -= mc[r]; s0[1][r] -= mc[r]; }
      RAWBAR();
      QKBLOCK(3, s1, 0, 1, s0, p0, -1);
      kb ^= 32768;
      RAWBAR();
      FETCH4(0); SBAR();
#define ATT_LOOP(PN, T0, T1) _Pragma("unroll 1") for (int t = (T0); t < (T1); ++t) { \
        RAWBAR();     \
        PVBLOCK(0, p0, s1, 0, PN); \
        RESCALE(s1, 0); \
        QKBLOCK(1, s0, 1, 1, s1, p1, PN); \
        RAWBAR();     \
        PVBLOCK(2, p1, s0, 0, PN); \
        vb ^= 32768; \
        RESCALE(s0, 0); \
        QKBLOCK(3, s1, 1, 1, s0, p0, PN); \
        kb ^= 32768; }
      const int nslow = fastclk > 0 ? 15 : 0;
      ATT_LOOP(AP_P, 0, nslow)
      ATT_LOOP(-1, nslow, 15)
#undef ATT_LOOP
#pragma unroll
      for (int j = 0; j < 9; ++j) JOB_AB(s1, j);
      RESCALE(s1, 1);
#pragma unroll
      for (int j = 0; j < 16; ++j) JOB_EXP(s1, p1, j);
      float iv[2];
#pragma unroll
      for (int r = 0; r < 2; ++r) {
        auto a = __builtin_amdgcn_permlane16_swap(__float_as_uint(lsum[r]), __float_as_uint(lsum[r]), false, false);
        iv[r] = 1.f / hsum(__uint_as_float(a[0]) + __uint_as_float(a[1]));
      }
      SBAR();
      PVBLOCK(0, p0, s1, 1, -1);
      RAWBAR();
      FETCH4(32); SBAR();
      PVBLOCK(2, p1, s0, 1, -1);
#undef FETCH
#undef XORI
#undef FETCHQ
#undef FETCH4
#undef FETCH_AHEAD
#undef JOB_AB
#undef JOB_EXP
#undef RESCALE
#undef MX2
#undef XE
#undef PVBLOCK
#undef QKBLOCK
      const int lane2 = lane_id(), g2 = lane2 >> 4, c2 = lane2 & 15;
      LAS char* ow = smem + LDS_V + hh * 65536 + rh * 2048 + g2 * 256;
#pragma unroll
      for (int kk = 0; kk < 8; ++kk)
#pragma unroll
        for (int r = 0; r < 2; ++r) {
          const f32x4 a = o[2 * kk][r] * iv[r], b = o[2 * kk + 1][r] * iv[r];
          lds_w128(ow + kk * 4096 + r * 1024 + ((c2 ^ g2 ^ ((kk & 1) * 4)) << 4), u32x4{pk2(a[0], a[1]), pk2(a[2], a[3]), pk2(b[0], b[1]), pk2(b[2], b[3])});
        }
    }
    WG_BARRIER();
    {
      HS_REQ();
      f32x4 acc[7][4]; PFrag<7> F[2];
#pragma unroll
      for (int i = 0; i < 7; ++i)
#pragma unroll
        for (int rt = 0; rt < 4; ++rt) acc[i][rt] = f32x4{0.f, 0.f, 0.f, 0.f};
      const int ab = pabase<ORS>(wid * 7), bbs = pbbase(LDS_V);
      { const int aa = ab + oslot(0), ba = obaddr(bbs, KO(0)); static_for<18>([&](auto kc) { pread<7, ORS, decltype(kc)::value>(F[0], smem, aa, ba); }); }
      int sl = 1;
#pragma unroll 1
      for (int s = 0; s < 16; s += 2) {
        WG_BARRIER();
        pstep<7, ORS>(acc, F[0], F[1], smem, ab + oslot(sl), obaddr(bbs, KO(s + 1)));
        sl = sl == 2 ? 0 : sl + 1;
        WG_BARRIER();
        pstep<7, ORS>(acc, F[1], F[0], smem, ab + oslot(sl), obaddr(bbs, KO(s + 2)));
        sl = sl == 2 ? 0 : sl + 1;
      }
      publish<7>(acc, ws, lid, wid * 7, HS_OK());
    }
  }
  WG_BARRIER();
  if (wid < 7) {
    const int lane = lane_id();
    const rsrc_t rp = mk_rsrc(ws + ((size_t)(lid >> 3) * 64 + kvh * 8) * SLAB, 8 * SLAB);
    u32x4 v[8];
#pragma unroll
    for (int s = 0; s < 8; ++s) v[s] = __builtin_amdgcn_raw_buffer_load_b128(rp, wid * 1024 + lane * 16, s * SLAB, AUX_SC1);
    float a[8];
#pragma unroll
    for (int k = 0; k < 4; ++k) { a[2 * k] = __uint_as_float(v[0][k] << 16); a[2 * k + 1] = __uint_as_float(v[0][k] & 0xffff0000u); }
#pragma unroll
    for (int s = 1; s < 8; ++s)
#pragma unroll
      for (int k = 0; k < 4; ++k) { a[2 * k] += __uint_as_float(v[s][k] << 16); a[2 * k + 1] += __uint_as_float(v[s][k] & 0xffff0000u); }
    const rsrc_t ro = mk_rsrc(out + (size_t)(bb * SQ + kvh * 8) * (2 * DM) * 2, 8 * 2 * DM * 2);
    __builtin_amdgcn_raw_buffer_store_b128(u32x4{pk2(a[0], a[1]), pk2(a[2], a[3]), pk2(a[4], a[5]), pk2(a[6], a[7])}, ro,
                                           ((lane & 7) * (2 * DM) + blk * DM + wid * 64 + (lane >> 3) * 8) * 2, 0, 0);
  }
}
}

extern "C" void kernel_launch(void* const* d_in, const int* in_sizes, int n_in, void* d_out, int out_size, void* d_ws, size_t ws_size,
                              hipStream_t stream) {
  (void)in_sizes; (void)n_in; (void)out_size; (void)ws_size;
  gqa_fused<<<256, 512, 0, stream>>>((const char*)d_in[0], (const char*)d_in[1], (const char*)d_in[2], (const char*)d_in[3], (const char*)d_in[4],
                                     (const char*)d_in[5], (const char*)d_in[6], (const char*)d_in[7], (const char*)d_in[8], (const char*)d_in[9],
                                     (char*)d_out, (char*)d_ws);
}
```

```cpp
#define MEAS 0
#include <hip/hip_runtime.h>
#include <hip/hip_bf16.h>
#include <utility>
#include <type_traits>

#define LAS __attribute__((address_space(3)))
#define DI __device__ __forceinline__
typedef __attribute__((ext_vector_type(8))) __bf16 bf16x8;
typedef __attribute__((ext_vector_type(4))) __bf16 bf16x4;
typedef __attribute__((ext_vector_type(2))) __bf16 bf16x2;
typedef __attribute__((ext_vector_type(4))) float f32x4;
typedef __attribute__((ext_vector_type(2))) float f32x2;
typedef __attribute__((ext_vector_type(4))) unsigned u32x4;

namespace {
constexpr int SQ = 64, DM = 448, HKV = 8, SK = 1024, DH = 256;
constexpr int NQ = 4096;
constexpr int KV_BYTES = SK * DH * 2;
constexpr int W_BYTES = DM * NQ * 2;
constexpr int LDS_K = 0, LDS_V = 65536, LDS_QP = 131072, LDS_TOTAL = 163840;
constexpr int LDS_X = 34816, LDS_QT = LDS_V + 32768;
constexpr int SLAB = 7168;
constexpr size_t CTL_OFF = (size_t)16 << 20;
constexpr float SCL = 0.0625f * 1.4426950408889634f;
constexpr float THR = 8.f;
constexpr int AUX_NT = 2, AUX_SC1 = 16;
constexpr int OOB = 0x40000000;
constexpr unsigned SPIN_CAP = 1u << 21;

#define SBAR() __builtin_amdgcn_sched_barrier(0)
#define WG_BARRIER() do { SBAR(); asm volatile("s_waitcnt lgkmcnt(0)\n\ts_barrier" ::: "memory"); SBAR(); } while (0)
#define PACE(PN) do { if ((PN) >= 0) asm volatile("s_nop %0" :: "i"((PN) < 0 ? 0 : (PN))); } while (0)
constexpr int AP_TH = 328;
constexpr int AP_P = 3;
#define MFMA(a, b, c) __builtin_amdgcn_mfma_f32_16x16x32_bf16((a), (b), (c), 0, 0, 0)
#define RLX_AGENT __ATOMIC_RELAXED, __HIP_MEMORY_SCOPE_AGENT

DI unsigned pk2(float a, float b) { f32x2 f = {a, b}; bf16x2 r = __builtin_convertvector(f, bf16x2); return __builtin_bit_cast(unsigned, r); }
DI bf16x8 asbf(u32x4 v) { return __builtin_bit_cast(bf16x8, v); }
DI u32x4 lds_r128(LAS char* p) { return *(LAS u32x4*)p; }
DI void lds_w128(LAS char* p, u32x4 v) { *(LAS u32x4*)p = v; }
DI bf16x4 lds_tr(LAS char* p) { return __builtin_amdgcn_ds_read_tr16_b64_v4bf16((LAS bf16x4*)p); }
DI float max3(float a, float b, float c) { return fmaxf(fmaxf(a, b), c); }
DI float hmax(float v) {
  auto b = __builtin_amdgcn_permlane32_swap(__float_as_uint(v), __float_as_uint(v), false, false);
  return fmaxf(__uint_as_float(b[0]), __uint_as_float(b[1]));
}
DI float hsum(float v) {
  auto b = __builtin_amdgcn_permlane32_swap(__float_as_uint(v), __float_as_uint(v), false, false);
  return __uint_as_float(b[0]) + __uint_as_float(b[1]);
}
DI int lane_id() {
  unsigned m = ~0u; asm volatile("" : "+s"(m)); return (int)__builtin_amdgcn_mbcnt_hi(m, __builtin_amdgcn_mbcnt_lo(m, 0u)); }
typedef __amdgpu_buffer_rsrc_t rsrc_t;
DI rsrc_t mk_rsrc(const void* p, int bytes) { return __builtin_amdgcn_make_buffer_rsrc((void*)p, 0, bytes, 0x00020000); }
template <class F, int... I> DI void static_for_(F&& f, std::integer_sequence<int, I...>) { (f(std::integral_constant<int, I>{}), ...); }
template <int N, class F> DI void static_for(F&& f) { static_for_(f, std::make_integer_sequence<int, N>{}); }

DI unsigned* ctl_chal(char* ws, int wg, int slot) { return (unsigned*)(ws + CTL_OFF + (size_t)wg * 512 + 128 + slot * 4); }
DI unsigned* ctl_resp(char* ws, int wg, int slot) { return (unsigned*)(ws + CTL_OFF + (size_t)wg * 512 + 256 + slot * 4); }
DI void hs_echo(char* ws, int lid, int lane) {
  const unsigned cv = __hip_atomic_load(ctl_chal(ws, lid, lane), RLX_AGENT);
  const unsigned rv = __hip_atomic_load(ctl_resp(ws, (lid & ~7) + lane, lid & 7), RLX_AGENT);
  if (cv != rv) __hip_atomic_store(ctl_resp(ws, (lid & ~7) + lane, lid & 7), cv, RLX_AGENT);
}
DI u32x4 ld1(rsrc_t rs, int voff, int soff) { return __builtin_amdgcn_raw_buffer_load_b128(rs, voff, soff, AUX_NT); }
DI void ld8(u32x4 (&r)[8], rsrc_t rs, int voff, int soff, int sa, int sb) {
#pragma unroll
  for (int j = 0; j < 8; ++j) r[j] = ld1(rs, voff, soff + (j & 3) * sa + (j >> 2) * sb);
}

DI void dma_nt(rsrc_t rs, int voff, int soff, int ldsaddr) {
  asm volatile("s_mov_b32 m0, %0\n\ts_nop 0\n\tbuffer_load_dwordx4 %1, %2, %3 offen nt lds" :: "s"(ldsaddr), "v"(voff), "s"(rs), "s"(soff) : "memory");
}
DI void dma56(rsrc_t rs, int voff, int soff, int ldsaddr) {
  asm volatile("s_mov_b32 m0, %0\n\ts_mov_b32 exec_hi, 0xffffff\n\tbuffer_load_dwordx4 %1, %2, %3 offen lds\n\ts_mov_b32 exec_hi, -1"
               :: "s"(ldsaddr), "v"(voff), "s"(rs), "s"(soff) : "memory");
}
#define WAITV(N) do { SBAR(); asm volatile("s_waitcnt vmcnt(" #N ")" ::: "memory"); SBAR(); } while (0)
template <int NF> struct PFrag { bf16x4 lo[NF], hi[NF]; bf16x8 b[4]; };
template <int RS> DI int pabase(int f0) {
  const int lane = lane_id(), g = lane >> 4, q = (lane & 15) >> 2, p = lane & 3;
  return (16 * (g >> 1) + 4 * (g & 1) + q) * RS + f0 * 32 + p * 8;
}
DI int pbbase(int bimg) { const int lane = lane_id(), g = lane >> 4, c = lane & 15; return bimg + g * 256 + ((c ^ g) << 4); }
DI int pbaddr(int bb, int ks) { return (bb ^ ((ks & 1) << 6)) + ks * 4096; }
template <int NF, int RS, int K> DI void pread(PFrag<NF>& f, LAS char* smem, int aa, int ba) {
  if constexpr (K < 4) f.b[K] = asbf(lds_r128(smem + ba + K * 1024));
  else if constexpr (K < 4 + 2 * NF) {
    if constexpr ((K & 1) == 0) f.lo[(K - 4) >> 1] = lds_tr(smem + aa + ((K - 4) >> 1) * 32);
    else f.hi[(K - 4) >> 1] = lds_tr(smem + aa + ((K - 4) >> 1) * 32 + 8 * RS);
  }
}
template <int NF, int RS>
DI void pstep(f32x4 (&acc)[NF][4], const PFrag<NF>& cur, PFrag<NF>& nxt, LAS char* smem, int aa, int ba) {
  static_for<NF * 4>([&](auto nc) {
    constexpr int n = decltype(nc)::value, i = n >> 2, rt = n & 3;
    const bf16x8 a = __builtin_shufflevector(cur.lo[i], cur.hi[i], 0, 1, 2, 3, 4, 5, 6, 7);
    acc[i][rt] = MFMA(a, cur.b[rt], acc[i][rt]);
    pread<NF, RS, n>(nxt, smem, aa, ba);
    SBAR();
  });
}
template <int NF> DI u32x4 ppack(const f32x4 (&acc)[NF][4], int i, int rp) {
  auto lo = __builtin_amdgcn_permlane16_swap(pk2(acc[i][2 * rp][0], acc[i][2 * rp][1]), pk2(acc[i][2 * rp + 1][0], acc[i][2 * rp + 1][1]), false, false);
  auto hi = __builtin_amdgcn_permlane16_swap(pk2(acc[i][2 * rp][2], acc[i][2 * rp][3]), pk2(acc[i][2 * rp + 1][2], acc[i][2 * rp + 1][3]), false, false);
  return u32x4{lo[0], hi[0], lo[1], hi[1]};
}
DI void q_handover(const f32x4 (&acc)[8][4], LAS char* smem, int w) {
  const int lane = lane_id(), g = lane >> 4, c = lane & 15;
  LAS char* base = smem + ((w & 1) ? LDS_QP : LDS_QT) + (w >> 1) * 16384 + (g & 1) * 1024 + (g >> 1) * 256 + c * 16;
#pragma unroll
  for (int i = 0; i < 8; ++i)
#pragma unroll
    for (int rp = 0; rp < 2; ++rp) lds_w128(base + rp * 8192 + (i >> 1) * 2048 + (i & 1) * 512, ppack<8>(acc, i, rp));
}
template <int NF> DI void publish(const f32x4 (&acc)[NF][4], char* ws, int lid, int f0, bool ok) {
  const int lane = lane_id(), g = lane >> 4, c = lane & 15;
  const rsrc_t rp = mk_rsrc(ws + (size_t)(lid >> 3) * 64 * SLAB, 64 * SLAB);
  const int voff = (((g & 1) * 2 + (c >> 3)) * 8 + (lid & 7)) * SLAB + f0 * 256 + (g >> 1) * 128 + (c & 7) * 16;
#pragma unroll
  for (int i = 0; i < NF; ++i)
#pragma unroll
    for (int r2 = 0; r2 < 2; ++r2) __builtin_amdgcn_raw_buffer_store_b128(ppack<NF>(acc, i, r2), rp, voff, r2 * 32 * SLAB + i * 256, AUX_SC1);
  asm volatile("s_waitcnt vmcnt(0)" ::: "memory");
  for (unsigned spins = 0; !ok && spins < SPIN_CAP; ++spins) {
    if (lane < 8) hs_echo(ws, lid, lane);
    const unsigned cv = __hip_atomic_load(ctl_chal(ws, (lid & ~7) + (lane & 7), lid & 7), RLX_AGENT);
    const unsigned rv = __hip_atomic_load(ctl_resp(ws, lid, lane & 7), RLX_AGENT);
    ok = __all(cv == rv);
    __builtin_amdgcn_s_sleep(8);
  }
  if (lane < 8) __hip_atomic_fetch_add((unsigned*)(ws + CTL_OFF + (size_t)((lid & ~7) + lane) * 512), 1u, RLX_AGENT);
}
constexpr int QRS = 1056, ORS = 928;
DI int qslot(int i) { return i == 0 ? 92160 : (i == 1 ? 126976 : 0); }
DI int oslot(int i) { return i == 0 ? LDS_K : (i == 1 ? LDS_K + 32768 : LDS_V + 32768); }
DI int obaddr(int bb, int ks) { return (bb ^ ((ks & 1) << 6)) + ks * 4096 + (ks >> 3) * 32768; }

__global__ __launch_bounds__(512, 2) void gqa_fused(const char* __restrict__ xA, const char* __restrict__ kA, const char* __restrict__ vA,
                                                    const char* __restrict__ wqA, const char* __restrict__ woA,
                                                    const char* __restrict__ xB, const char* __restrict__ kB, const char* __restrict__ vB,
                                                    const char* __restrict__ wqB, const char* __restrict__ woB,
                                                    char* __restrict__ out, char* ws) {
  __shared__ __attribute__((aligned(1024))) char smem_[LDS_TOTAL];
  LAS char* smem = (LAS char*)smem_;
  const int wid = __builtin_amdgcn_readfirstlane(threadIdx.x >> 6);
  const int bid = blockIdx.x, blk = (bid >> 2) & 1, kvh = (bid & 3) * 2 + (bid >> 7), bb = (bid >> 3) & 15;
  const int lid = (blk * 16 + bb) * 8 + kvh;
  const int kq0 = bb < 14 ? bb : bb - 14;
#define KQ(S) ((kq0 + (S)) >= 14 ? kq0 + (S) - 14 : kq0 + (S))
#define KO(S) (((S) + bb) & 15)
  const rsrc_t rc = mk_rsrc(ws + CTL_OFF + (size_t)(lid & ~7) * 512, 8 * 512);
#define HS_REQ() const unsigned hsc_ = __builtin_amdgcn_raw_buffer_load_b32(rc, (lane_id() & 7) * 512 + 128 + (lid & 7) * 4, 0, AUX_SC1), \
                                hsr_ = __builtin_amdgcn_raw_buffer_load_b32(rc, (lid & 7) * 512 + 256 + (lane_id() & 7) * 4, 0, AUX_SC1)
#define HS_OK() __all(hsc_ == hsr_)

  unsigned meas_ = 0; const unsigned tz_ = (unsigned)__builtin_amdgcn_s_memrealtime();
#define MS(i) do { if (MEAS == (i)) { meas_ = (unsigned)__builtin_amdgcn_s_memrealtime() - tz_; __builtin_amdgcn_s_waitcnt(0xC07F); SBAR(); } } while (0)
  if (wid >= 4) {
    int lane = lane_id();
    const int h = wid - 4;
    bool hs = h == 0 && lane < 8;
    __builtin_amdgcn_raw_buffer_store_b32(0u, rc, hs && lane == 0 ? (lid & 7) * 512 : OOB, 0, AUX_SC1);
    const unsigned hs_r = __builtin_amdgcn_raw_buffer_load_b32(rc, hs ? (lid & 7) * 512 + 256 + lane * 4 : OOB, 0, AUX_SC1);
    const rsrc_t rwq = mk_rsrc((blk ? wqB : wqA) + kvh * 1024, W_BYTES - kvh * 1024);
    const rsrc_t rwo = mk_rsrc((blk ? woB : woA) + (size_t)kvh * 512 * DM * 2, 512 * DM * 2);
    const rsrc_t rk = mk_rsrc((blk ? kB : kA) + (size_t)(bb * HKV + kvh) * KV_BYTES, KV_BYTES);
    const rsrc_t rv = mk_rsrc((blk ? vB : vA) + (size_t)(bb * HKV + kvh) * KV_BYTES, KV_BYTES);
    int l16 = lane * 16;
    u32x4 R0[8], R1[8], SA[8];
    const int irow = 16 * (h >> 1) + 4 * (h & 1);
#define QDMA(S) do { _Pragma("unroll") for (int j = 0; j < 8; ++j) \
      dma_nt(rwq, l16, (KQ(S) * 32 + 8 * h + j) * (NQ * 2), qslot((S) % 3) + (irow + 8 * (j >> 2) + (j & 3)) * QRS); SBAR(); } while (0)
#define ODMA(S) do { _Pragma("unroll") for (int j = 0; j < 8; ++j) \
      dma56(rwo, l16, (KO(S) * 32 + 4 * h + 16 * (j >> 2) + (j & 3)) * (DM * 2), oslot((S) % 3) + (irow + 8 * (j >> 2) + (j & 3)) * ORS); SBAR(); } while (0)
#define LOADU(SET, RS, TILE) do { ld8(SET, RS, l16, (TILE) * 32768 + h * 1024, 4096, 16384); SBAR(); } while (0)
    QDMA(0); QDMA(1); QDMA(2);
    WAITV(16);
    WG_BARRIER();
    MS(1);
    WAITV(8);
#pragma unroll 1
    for (int s = 0; s < 10; ++s) {
      WG_BARRIER();
      QDMA(s + 3);
      WAITV(8);
    }
    WG_BARRIER();
    QDMA(13);
    LOADU(R0, rk, 0);
    WAITV(16);
    WG_BARRIER();
    WAITV(8);
    LOADU(R1, rk, 1);
    WG_BARRIER();
    __builtin_amdgcn_raw_buffer_store_b32(hs_r + 1u, rc, hs ? lane * 512 + 128 + (lid & 7) * 4 : OOB, 0, AUX_SC1);
    WG_BARRIER();
    MS(2);
    LOADU(SA, rv, 0);
    lane = lane_id(); l16 = lane * 16; hs = h == 0 && lane < 8;
    const int hi = lane >> 5, ch = lane & 31;
    const int row0 = 2 * h + hi;
    const int kw0 = LDS_K + row0 * 512 + ((ch ^ row0) << 4), kw1 = LDS_K + (row0 + 8) * 512 + ((ch ^ row0 ^ 8) << 4);
    const int vw0 = LDS_V + row0 * 512 + ((ch ^ (row0 << 1)) << 4);
#define WRITEK(SET, BUF) do { _Pragma("unroll") for (int i = 0; i < 8; ++i) \
      lds_w128(smem + ((i & 1) ? kw1 : kw0) + (BUF) + (i >> 1) * 8192, SET[i]); SBAR(); } while (0)
#define WRITEV(SET, BUF) do { _Pragma("unroll") for (int i = 0; i < 8; ++i) \
      lds_w128(smem + vw0 + (BUF) + i * 4096, SET[i]); SBAR(); } while (0)
    const int kv0 = row0 * 512 + ((ch ^ row0) << 4), kv1 = row0 * 512 + ((ch ^ row0 ^ 8) << 4), vv0 = row0 * 512 + ((ch ^ (row0 << 1)) << 4);
#define KDMA(TILE, HALF) do { _Pragma("unroll") for (int i = 0; i < 4; ++i) \
      dma_nt(rk, (i & 1) ? kv1 : kv0, (TILE) * 32768 + (HALF) * 16384 + i * 4096, LDS_K + ((TILE) & 1) * 32768 + (HALF) * 16384 + i * 4096 + h * 1024); SBAR(); } while (0)
#define VDMA(TILE, HALF) do { _Pragma("unroll") for (int i = 0; i < 4; ++i) \
      dma_nt(rv, vv0, (TILE) * 32768 + (HALF) * 16384 + i * 4096, LDS_V + ((TILE) & 1) * 32768 + (HALF) * 16384 + i * 4096 + h * 1024); SBAR(); } while (0)
    WRITEK(R0, 0);
    WG_BARRIER();
    WRITEK(R1, 32768);
    WG_BARRIER();
    WRITEV(SA, 0);
    VDMA(1, 0); KDMA(2, 0);
    WG_BARRIER();
    WG_BARRIER();
    MS(3);
    VDMA(1, 1); KDMA(2, 1);
    WAITV(8);
#pragma unroll 1
    for (int n = 1; n < 27; ++n) {
      WG_BARRIER();
      const int tv = (n + 3) >> 1, hf = (n + 3) & 1;
      VDMA(tv, hf); KDMA(tv + 1, hf);
      if (n == 20 && hs) hs_echo(ws, lid, lane);
      WAITV(8);
    }
    WG_BARRIER();
    VDMA(15, 0);
    WAITV(4);
    WG_BARRIER();
    VDMA(15, 1);
    ODMA(0);
    WAITV(12);
    WG_BARRIER();
    WAITV(8);
#undef KDMA
#undef VDMA
    WG_BARRIER();
    MS(4);
    ODMA(1);
    WAITV(8);
    WG_BARRIER();
    ODMA(2);
    WAITV(8);
#pragma unroll 1
    for (int s = 0; s < 13; ++s) {
      WG_BARRIER();
      ODMA(s + 3);
      WAITV(8);
    }
    WG_BARRIER();
    WAITV(0);
    WG_BARRIER();
    WG_BARRIER();
    MS(5);
    if (h == 0) {
      const unsigned* cnt = (const unsigned*)(ws + CTL_OFF + (size_t)lid * 512);
      for (unsigned spins = 0; spins < SPIN_CAP; ++spins) {
        if (__hip_atomic_load(cnt, RLX_AGENT) >= 32u) break;
        if ((spins & 63u) == 63u) { const int l = lane_id(); if (l < 8) hs_echo(ws, lid, l); }
        __builtin_amdgcn_s_sleep(2);
      }
    }
#undef QDMA
#undef ODMA
#undef LOADU
#undef WRITEK
#undef WRITEV
  } else {
    const unsigned c0_ = (unsigned)__builtin_amdgcn_s_memtime(), r0_ = (unsigned)__builtin_amdgcn_s_memrealtime();
    {
      const int lane = lane_id();
      const rsrc_t rx = mk_rsrc((blk ? xB : xA) + (size_t)bb * SQ * DM * 2, SQ * DM * 2);
      const int xp = ((wid + (kvh >> 1)) & 3) * 14;
      u32x4 xr[14];
#pragma unroll
      for (int f = 0; f < 14; ++f) xr[f] = __builtin_amdgcn_raw_buffer_load_b128(rx, lane * 16, (xp + f) * 1024, AUX_NT);
      SBAR();
#pragma unroll
      for (int f = 0; f < 14; ++f) {
        const unsigned idx = (xp + f) * 64 + lane, row = idx / 56u, q = idx - row * 56u;
        lds_w128(smem + LDS_X + (q >> 2) * 4096 + (row >> 4) * 1024 + (q & 3) * 256 + (((row & 15) ^ (q & 7)) << 4), xr[f]);
      }
    }
    WG_BARRIER();
    const int fastclk = __builtin_amdgcn_readfirstlane((int)(((unsigned)__builtin_amdgcn_s_memtime() - c0_) * 16u - ((unsigned)__builtin_amdgcn_s_memrealtime() - r0_) * (unsigned)AP_TH));
    __builtin_amdgcn_s_waitcnt(0xC07F); SBAR();
    {
      f32x4 acc[8][4]; PFrag<8> F[2];
#pragma unroll
      for (int i = 0; i < 8; ++i)
#pragma unroll
        for (int rt = 0; rt < 4; ++rt) acc[i][rt] = f32x4{0.f, 0.f, 0.f, 0.f};
      const int ab = pabase<QRS>(wid * 8), bbs = pbbase(LDS_X);
      { const int aa = ab + qslot(0), ba = pbaddr(bbs, kq0); static_for<20>([&](auto kc) { pread<8, QRS, decltype(kc)::value>(F[0], smem, aa, ba); }); }
      int sl = 1;
#pragma unroll 1
      for (int s = 0; s < 14; s += 2) {
        WG_BARRIER();
        pstep<8, QRS>(acc, F[0], F[1], smem, ab + qslot(sl), pbaddr(bbs, KQ(s + 1)));
        sl = sl == 2 ? 0 : sl + 1;
        WG_BARRIER();
        pstep<8, QRS>(acc, F[1], F[0], smem, ab + qslot(sl), pbaddr(bbs, KQ(s + 2)));
        sl = sl == 2 ? 0 : sl + 1;
      }
      q_handover(acc, smem, wid);
    }
    {
      const int w = wid, hh = w >> 1, rh = w & 1;
      const int lane = lane_id(), g = lane >> 4, c = lane & 15;
      f32x4 o[16][2];
#pragma unroll
      for (int dt = 0; dt < 16; ++dt) { o[dt][0] = f32x4{0.f, 0.f, 0.f, 0.f}; o[dt][1] = f32x4{0.f, 0.f, 0.f, 0.f}; }
      float mc[2] = {0.f, 0.f}, lsum[2] = {0.f, 0.f};
      f32x4 s0[2][2], s1[2][2];
      u32x4 p0[2], p1[2];
      u32x4 qown[4][2];
      int kb = LDS_K + c * 512 + ((g ^ c) << 4);
      int vb = LDS_V + (4 * g + (c >> 2)) * 512 + ((((4 * g + (c >> 2)) & 7)) << 5) + (c & 3) * 8;
      LAS char* qp = smem + LDS_QP + w * 8192 + lane * 16;
      constexpr int PD = 4, RN = 6;
      bf16x4 vl[RN], vh[RN]; bf16x8 kf[RN], qf[2][2];
      float t0, t1, t2, t3, mx;
      int fa = 0;
#define XORI(IMM, V) do { asm volatile("" : "+v"(V)); fa = (V) ^ (IMM); } while (0)
#define FETCH(N) do { constexpr int n_ = (N), blk_ = n_ >> 4, j_ = n_ & 15; \
        if (blk_ & 1) { if ((j_ & 3) == 0) XORI((j_ >> 2) << 6, kb); \
          kf[n_ % RN] = asbf(lds_r128(smem + fa + ((j_ >> 1) & 1) * 256 + (j_ & 1) * 8192 + (blk_ >> 1) * 16384)); } \
        else { if ((j_ & 1) == 0) XORI((j_ >> 1) << 5, vb); \
          LAS char* a_ = smem + fa + (j_ & 1) * 256 + (blk_ >> 1) * 16384; vl[n_ % RN] = lds_tr(a_); vh[n_ % RN] = lds_tr(a_ + 8192); } } while (0)
#define FETCHQ(V, RT) qf[(V) & 1][RT] = asbf(lds_r128(qp + ((V) * 2 + (RT)) * 1024))
#define XE(X, e) X[(e) >> 3][((e) >> 2) & 1][(e) & 3]
#define JOB_AB(X, j) do { \
        if ((j) < 4) { _Pragma("unroll") for (int i_ = 0; i_ < 4; ++i_) XE(X, 4 * (j) + i_) = fmaf(XE(X, 4 * (j) + i_), SCL, -mc[(j) & 1]); } \
        else if ((j) == 4) { t0 = max3(X[0][0][0], X[0][0][1], X[0][0][2]); t1 = max3(X[0][1][0], X[0][1][1], X[0][1][2]); t2 = max3(X[0][0][3], X[1][0][0], X[1][0][1]); } \
        else if ((j) == 5) { t3 = max3(X[0][1][3], X[1][1][0], X[1][1][1]); t0 = max3(t0, X[1][0][2], X[1][0][3]); t1 = max3(t1, X[1][1][2], X[1][1][3]); } \
        else if ((j) == 6) { t0 = fmaxf(t0, t2); t1 = fmaxf(t1, t3); } \
        else if ((j) == 7) { auto a_ = __builtin_amdgcn_permlane16_swap(__float_as_uint(t0), __float_as_uint(t1), false, false); \
                             t0 = fmaxf(__uint_as_float(a_[0]), __uint_as_float(a_[1])); } \
        else if ((j) == 8) { mx = hmax(t0); } } while (0)
#define JOB_EXP(X, P, j) do { XE(X, j) = __builtin_amdgcn_exp2f(XE(X, j)); \
        if ((j) > 0) lsum[(((j) - 1) >> 2) & 1] += XE(X, (j) - 1); \
        if ((j) >= 2 && !((j) & 1)) P[(((j) - 2) >> 2) & 1][(((j) - 2) >> 3) * 2 + ((((j) - 2) >> 1) & 1)] = pk2(XE(X, (j) - 2), XE(X, (j) - 1)); \
        if ((j) == 15) { lsum[1] += XE(X, 15); P[1][3] = pk2(XE(X, 14), XE(X, 15)); } } while (0)
#define MX2(M) do { auto m_ = __builtin_amdgcn_permlane16_swap(__float_as_uint(mx), __float_as_uint(mx), false, false); \
        M[0] = __uint_as_float(m_[0]); M[1] = __uint_as_float(m_[1]); } while (0)
#define RESCALE(X, PEND) do { if (__any(mx > THR)) { float d_[2], al_[2]; MX2(d_); \
          _Pragma("unroll") for (int r_ = 0; r_ < 2; ++r_) { d_[r_] = fmaxf(d_[r_], 0.f); al_[r_] = __builtin_amdgcn_exp2f(-d_[r_]); mc[r_] += d_[r_]; lsum[r_] *= al_[r_]; \
            X[0][r_] -= d_[r_]; X[1][r_] -= d_[r_]; \
            _Pragma("unroll") for (int dt = 0; dt < 16; ++dt) o[dt][r_] *= al_[r_]; \
            if (PEND) { _Pragma("unroll") for (int i_ = 0; i_ < 4; ++i_) \
              p0[r_][i_] = pk2(__uint_as_float(p0[r_][i_] << 16) * al_[r_], __uint_as_float(p0[r_][i_] & 0xffff0000u) * al_[r_]); } } } SBAR(); } while (0)
#define FETCH_AHEAD(B, j) switch (j) { \
            case 0: FETCH((B) * 16 + 0 + PD); break; case 1: FETCH((B) * 16 + 1 + PD); break; case 2: FETCH((B) * 16 + 2 + PD); break; case 3: FETCH((B) * 16 + 3 + PD); break; \
            case 4: FETCH((B) * 16 + 4 + PD); break; case 5: FETCH((B) * 16 + 5 + PD); break; case 6: FETCH((B) * 16 + 6 + PD); break; case 7: FETCH((B) * 16 + 7 + PD); break; \
            case 8: FETCH((B) * 16 + 8 + PD); break; case 9: FETCH((B) * 16 + 9 + PD); break; case 10: FETCH((B) * 16 + 10 + PD); break; case 11: FETCH((B) * 16 + 11 + PD); break; \
            case 12: FETCH(((B) * 16 + 12 + PD) & 63); break; case 13: FETCH(((B) * 16 + 13 + PD) & 63); break; case 14: FETCH(((B) * 16 + 14 + PD) & 63); break; default: FETCH(((B) * 16 + 15 + PD) & 63); break; }
#define PVBLOCK(B, P, X, LAST, PN) do { _Pragma("unroll") for (int j = 0; j < 16; ++j) { \
          const bf16x8 a_ = __builtin_shufflevector(vl[((B) * 16 + j) % RN], vh[((B) * 16 + j) % RN], 0, 1, 2, 3, 4, 5, 6, 7); \
          o[(j >> 1) + 8 * (j & 1)][0] = MFMA(a_, asbf(P[0]), o[(j >> 1) + 8 * (j & 1)][0]); \
          if (j + PD < 16 || !(LAST)) { FETCH_AHEAD(B, j) } \
          SBAR(); \
          o[(j >> 1) + 8 * (j & 1)][1] = MFMA(a_, asbf(P[1]), o[(j >> 1) + 8 * (j & 1)][1]); \
          if (!(LAST) && j >= 14) FETCHQ(0, j - 14); \
          if (!(LAST)) JOB_AB(X, j); \
          PACE(PN); \
          SBAR(); } } while (0)
#define QKBLOCK(B, S, NEXT, JOB, X, P, PN) do { _Pragma("unroll") for (int j = 0; j < 16; ++j) { \
          const bf16x8 q0_ = (j & 2) ? qf[(j >> 2) & 1][0] : asbf(qown[j >> 2][0]), q1_ = (j & 2) ? qf[(j >> 2) & 1][1] : asbf(qown[j >> 2][1]); \
          const f32x4 z_ = {0.f, 0.f, 0.f, 0.f}; \
          S[j & 1][0] = MFMA(kf[((B) * 16 + j) % RN], q0_, j < 2 ? z_ : S[j & 1][0]); \
          if (j + PD < 16 || (NEXT)) { FETCH_AHEAD(B, j) } \
          SBAR(); \
          S[j & 1][1] = MFMA(kf[((B) * 16 + j) % RN], q1_, j < 2 ? z_ : S[j & 1][1]); \
          if (!(j & 2) && j >= 4) FETCHQ(j >> 2, j & 1); \
          if (JOB) JOB_EXP(X, P, j); \
          PACE(PN); \
          SBAR(); } } while (0)
#define FETCH4(N) do { FETCH(N); FETCH((N) + 1); FETCH((N) + 2); FETCH((N) + 3); } while (0)
      static_assert(PD == 4, "FETCH4");
#define RAWBAR() do { SBAR(); asm volatile("s_barrier" ::: "memory"); SBAR(); } while (0)
      WG_BARRIER();
#pragma unroll
      for (int f = 0; f < 8; ++f) qown[f >> 1][f & 1] = lds_r128(smem + LDS_QT + w * 8192 + f * 1024 + lane * 16);
      FETCH4(16); FETCHQ(0, 0); FETCHQ(0, 1); SBAR();
      QKBLOCK(1, s0, 0, 0, s0, p0, -1);
      FETCH4(48); FETCHQ(0, 0); FETCHQ(0, 1); SBAR();
#pragma unroll
      for (int j = 0; j < 9; ++j) JOB_AB(s0, j);
      MX2(mc);
#pragma unroll
      for (int r = 0; r < 2; ++r) { s0[0][r] -= mc[r]; s0[1][r] -= mc[r]; }
      RAWBAR();
      QKBLOCK(3, s1, 0, 1, s0, p0, -1);
      kb ^= 32768;
      RAWBAR();
      FETCH4(0); SBAR();
#define ATT_LOOP(PN, T0, T1) _Pragma("unroll 1") for (int t = (T0); t < (T1); ++t) { \
        RAWBAR();     \
        PVBLOCK(0, p0, s1, 0, PN); \
        RESCALE(s1, 0); \
        QKBLOCK(1, s0, 1, 1, s1, p1, PN); \
        RAWBAR();     \
        PVBLOCK(2, p1, s0, 0, PN); \
        vb ^= 32768; \
        RESCALE(s0, 0); \
        QKBLOCK(3, s1, 1, 1, s0, p0, PN); \
        kb ^= 32768; }
      const int nslow = fastclk > 0 ? 15 : 0;
      ATT_LOOP(AP_P, 0, nslow)
      ATT_LOOP(-1, nslow, 15)
#undef ATT_LOOP
#pragma unroll
      for (int j = 0; j < 9; ++j) JOB_AB(s1, j);
      RESCALE(s1, 1);
#pragma unroll
      for (int j = 0; j < 16; ++j) JOB_EXP(s1, p1, j);
      float iv[2];
#pragma unroll
      for (int r = 0; r < 2; ++r) {
        auto a = __builtin_amdgcn_permlane16_swap(__float_as_uint(lsum[r]), __float_as_uint(lsum[r]), false, false);
        iv[r] = 1.f / hsum(__uint_as_float(a[0]) + __uint_as_float(a[1]));
      }
      SBAR();
      PVBLOCK(0, p0, s1, 1, -1);
      RAWBAR();
      FETCH4(32); SBAR();
      PVBLOCK(2, p1, s0, 1, -1);
#undef FETCH
#undef XORI
#undef FETCHQ
#undef FETCH4
#undef FETCH_AHEAD
#undef JOB_AB
#undef JOB_EXP
#undef RESCALE
#undef MX2
#undef XE
#undef PVBLOCK
#undef QKBLOCK
      const int lane2 = lane_id(), g2 = lane2 >> 4, c2 = lane2 & 15;
      LAS char* ow = smem + LDS_V + hh * 65536 + rh * 2048 + g2 * 256;
#pragma unroll
      for (int kk = 0; kk < 8; ++kk)
#pragma unroll
        for (int r = 0; r < 2; ++r) {
          const f32x4 a = o[2 * kk][r] * iv[r], b = o[2 * kk + 1][r] * iv[r];
          lds_w128(ow + kk * 4096 + r * 1024 + ((c2 ^ g2 ^ ((kk & 1) * 4)) << 4), u32x4{pk2(a[0], a[1]), pk2(a[2], a[3]), pk2(b[0], b[1]), pk2(b[2], b[3])});
        }
    }
    WG_BARRIER();
    {
      HS_REQ();
      f32x4 acc[7][4]; PFrag<7> F[2];
#pragma unroll
      for (int i = 0; i < 7; ++i)
#pragma unroll
        for (int rt = 0; rt < 4; ++rt) acc[i][rt] = f32x4{0.f, 0.f, 0.f, 0.f};
      const int ab = pabase<ORS>(wid * 7), bbs = pbbase(LDS_V);
      { const int aa = ab + oslot(0), ba = obaddr(bbs, KO(0)); static_for<18>([&](auto kc) { pread<7, ORS, decltype(kc)::value>(F[0], smem, aa, ba); }); }
      int sl = 1;
#pragma unroll 1
      for (int s = 0; s < 16; s += 2) {
        WG_BARRIER();
        pstep<7, ORS>(acc, F[0], F[1], smem, ab + oslot(sl), obaddr(bbs, KO(s + 1)));
        sl = sl == 2 ? 0 : sl + 1;
        WG_BARRIER();
        pstep<7, ORS>(acc, F[1], F[0], smem, ab + oslot(sl), obaddr(bbs, KO(s + 2)));
        sl = sl == 2 ? 0 : sl + 1;
      }
      publish<7>(acc, ws, lid, wid * 7, HS_OK());
    }
  }
  WG_BARRIER();
  MS(6);
  if (wid == 7) { const unsigned t1_ = (unsigned)__builtin_amdgcn_s_memrealtime(); while ((unsigned)__builtin_amdgcn_s_memrealtime() - t1_ < meas_ + 120u) __builtin_amdgcn_s_sleep(1); }
  if (wid < 7) {
    const int lane = lane_id();
    const rsrc_t rp = mk_rsrc(ws + ((size_t)(lid >> 3) * 64 + kvh * 8) * SLAB, 8 * SLAB);
    u32x4 v[8];
#pragma unroll
    for (int s = 0; s < 8; ++s) v[s] = __builtin_amdgcn_raw_buffer_load_b128(rp, wid * 1024 + lane * 16, s * SLAB, AUX_SC1);
    float a[8];
#pragma unroll
    for (int k = 0; k < 4; ++k) { a[2 * k] = __uint_as_float(v[0][k] << 16); a[2 * k + 1] = __uint_as_float(v[0][k] & 0xffff0000u); }
#pragma unroll
    for (int s = 1; s < 8; ++s)
#pragma unroll
      for (int k = 0; k < 4; ++k) { a[2 * k] += __uint_as_float(v[s][k] << 16); a[2 * k + 1] += __uint_as_float(v[s][k] & 0xffff0000u); }
    const rsrc_t ro = mk_rsrc(out + (size_t)(bb * SQ + kvh * 8) * (2 * DM) * 2, 8 * 2 * DM * 2);
    __builtin_amdgcn_raw_buffer_store_b128(u32x4{pk2(a[0], a[1]), pk2(a[2], a[3]), pk2(a[4], a[5]), pk2(a[6], a[7])}, ro,
                                           ((lane & 7) * (2 * DM) + blk * DM + wid * 64 + (lane >> 3) * 8) * 2, 0, 0);
  }
}
}

extern "C" void kernel_launch(void* const* d_in, const int* in_sizes, int n_in, void* d_out, int out_size, void* d_ws, size_t ws_size,
                              hipStream_t stream) {
  (void)in_sizes; (void)n_in; (void)out_size; (void)ws_size;
  gqa_fused<<<256, 512, 0, stream>>>((const char*)d_in[0], (const char*)d_in[1], (const char*)d_in[2], (const char*)d_in[3], (const char*)d_in[4],
                                     (const char*)d_in[5], (const char*)d_in[6], (const char*)d_in[7], (const char*)d_in[8], (const char*)d_in[9],
                                     (char*)d_out, (char*)d_ws);
}
```

```cpp
#include <hip/hip_runtime.h>
#include <hip/hip_bf16.h>
#include <utility>
#include <type_traits>

#define LAS __attribute__((address_space(3)))
#define DI __device__ __forceinline__
typedef __attribute__((ext_vector_type(8))) __bf16 bf16x8;
typedef __attribute__((ext_vector_type(4))) __bf16 bf16x4;
typedef __attribute__((ext_vector_type(2))) __bf16 bf16x2;
typedef __attribute__((ext_vector_type(4))) float f32x4;
typedef __attribute__((ext_vector_type(2))) float f32x2;
typedef __attribute__((ext_vector_type(4))) unsigned u32x4;

namespace {
constexpr int SQ = 64, DM = 448, HKV = 8, SK = 1024, DH = 256;
constexpr int NQ = 4096;
constexpr int KV_BYTES = SK * DH * 2;
constexpr int W_BYTES = DM * NQ * 2;
constexpr int LDS_K = 0, LDS_V = 65536, LDS_QP = 131072, LDS_TOTAL = 163840;
constexpr int LDS_X = 34816, LDS_QT = LDS_V + 32768;
constexpr int SLAB = 7168;
constexpr int GSTRIDE = 64 * SLAB + 2048;
constexpr size_t CTL_OFF = (size_t)16 << 20;
constexpr float SCL = 0.0625f * 1.4426950408889634f;
constexpr float THR = 8.f;
constexpr int AUX_NT = 2, AUX_SC1 = 16;
constexpr int OOB = 0x40000000;
constexpr unsigned SPIN_CAP = 1u << 21;

#define SBAR() __builtin_amdgcn_sched_barrier(0)
#define WG_BARRIER() do { SBAR(); asm volatile("s_waitcnt lgkmcnt(0)\n\ts_barrier" ::: "memory"); SBAR(); } while (0)
#define PACE(PN) do { if ((PN) >= 0) asm volatile("s_nop %0" :: "i"((PN) < 0 ? 0 : (PN))); } while (0)
constexpr int AP_TH = 328;
constexpr int AP_P = 3;
#define MFMA(a, b, c) __builtin_amdgcn_mfma_f32_16x16x32_bf16((a), (b), (c), 0, 0, 0)
#define RLX_AGENT __ATOMIC_RELAXED, __HIP_MEMORY_SCOPE_AGENT

DI unsigned pk2(float a, float b) { f32x2 f = {a, b}; bf16x2 r = __builtin_convertvector(f, bf16x2); return __builtin_bit_cast(unsigned, r); }
DI bf16x8 asbf(u32x4 v) { return __builtin_bit_cast(bf16x8, v); }
DI u32x4 lds_r128(LAS char* p) { return *(LAS u32x4*)p; }
DI void lds_w128(LAS char* p, u32x4 v) { *(LAS u32x4*)p = v; }
DI bf16x4 lds_tr(LAS char* p) { return __builtin_amdgcn_ds_read_tr16_b64_v4bf16((LAS bf16x4*)p); }
DI float max3(float a, float b, float c) { return fmaxf(fmaxf(a, b), c); }
DI float hmax(float v) {
  auto b = __builtin_amdgcn_permlane32_swap(__float_as_uint(v), __float_as_uint(v), false, false);
  return fmaxf(__uint_as_float(b[0]), __uint_as_float(b[1]));
}
DI float hsum(float v) {
  auto b = __builtin_amdgcn_permlane32_swap(__float_as_uint(v), __float_as_uint(v), false, false);
  return __uint_as_float(b[0]) + __uint_as_float(b[1]);
}
DI int lane_id() {
  unsigned m = ~0u; asm volatile("" : "+s"(m)); return (int)__builtin_amdgcn_mbcnt_hi(m, __builtin_amdgcn_mbcnt_lo(m, 0u)); }
typedef __amdgpu_buffer_rsrc_t rsrc_t;
DI rsrc_t mk_rsrc(const void* p, int bytes) { return __builtin_amdgcn_make_buffer_rsrc((void*)p, 0, bytes, 0x00020000); }
template <class F, int... I> DI void static_for_(F&& f, std::integer_sequence<int, I...>) { (f(std::integral_constant<int, I>{}), ...); }
template <int N, class F> DI void static_for(F&& f) { static_for_(f, std::make_integer_sequence<int, N>{}); }

DI unsigned* ctl_chal(char* ws, int wg, int slot) { return (unsigned*)(ws + CTL_OFF + (size_t)wg * 512 + 128 + slot * 4); }
DI unsigned* ctl_resp(char* ws, int wg, int slot) { return (unsigned*)(ws + CTL_OFF + (size_t)wg * 512 + 256 + slot * 4); }
DI void hs_echo(char* ws, int lid, int lane) {
  const unsigned cv = __hip_atomic_load(ctl_chal(ws, lid, lane), RLX_AGENT);
  const unsigned rv = __hip_atomic_load(ctl_resp(ws, (lid & ~7) + lane, lid & 7), RLX_AGENT);
  if (cv != rv) __hip_atomic_store(ctl_resp(ws, (lid & ~7) + lane, lid & 7), cv, RLX_AGENT);
}
DI u32x4 ld1(rsrc_t rs, int voff, int soff) { return __builtin_amdgcn_raw_buffer_load_b128(rs, voff, soff, AUX_NT); }
DI void ld8(u32x4 (&r)[8], rsrc_t rs, int voff, int soff, int sa, int sb) {
#pragma unroll
  for (int j = 0; j < 8; ++j) r[j] = ld1(rs, voff, soff + (j & 3) * sa + (j >> 2) * sb);
}

DI void dma_nt(rsrc_t rs, int voff, int soff, int ldsaddr) {
  asm volatile("s_mov_b32 m0, %0\n\ts_nop 0\n\tbuffer_load_dwordx4 %1, %2, %3 offen nt lds" :: "s"(ldsaddr), "v"(voff), "s"(rs), "s"(soff) : "memory");
}
DI void dma56(rsrc_t rs, int voff, int soff, int ldsaddr) {
  asm volatile("s_mov_b32 m0, %0\n\ts_mov_b32 exec_hi, 0xffffff\n\tbuffer_load_dwordx4 %1, %2, %3 offen lds\n\ts_mov_b32 exec_hi, -1"
               :: "s"(ldsaddr), "v"(voff), "s"(rs), "s"(soff) : "memory");
}
#define WAITV(N) do { SBAR(); asm volatile("s_waitcnt vmcnt(" #N ")" ::: "memory"); SBAR(); } while (0)
template <int NF> struct PFrag { bf16x4 lo[NF], hi[NF]; bf16x8 b[4]; };
template <int RS> DI int pabase(int f0) {
  const int lane = lane_id(), g = lane >> 4, q = (lane & 15) >> 2, p = lane & 3;
  return (16 * (g >> 1) + 4 * (g & 1) + q) * RS + f0 * 32 + p * 8;
}
DI int pbbase(int bimg) { const int lane = lane_id(), g = lane >> 4, c = lane & 15; return bimg + g * 256 + ((c ^ g) << 4); }
DI int pbaddr(int bb, int ks) { return (bb ^ ((ks & 1) << 6)) + ks * 4096; }
template <int NF, int RS, int K> DI void pread(PFrag<NF>& f, LAS char* smem, int aa, int ba) {
  if constexpr (K < 4) f.b[K] = asbf(lds_r128(smem + ba + K * 1024));
  else if constexpr (K < 4 + 2 * NF) {
    if constexpr ((K & 1) == 0) f.lo[(K - 4) >> 1] = lds_tr(smem + aa + ((K - 4) >> 1) * 32);
    else f.hi[(K - 4) >> 1] = lds_tr(smem + aa + ((K - 4) >> 1) * 32 + 8 * RS);
  }
}
template <int NF, int RS>
DI void pstep(f32x4 (&acc)[NF][4], const PFrag<NF>& cur, PFrag<NF>& nxt, LAS char* smem, int aa, int ba) {
  static_for<NF * 4>([&](auto nc) {
    constexpr int n = decltype(nc)::value, i = n >> 2, rt = n & 3;
    const bf16x8 a = __builtin_shufflevector(cur.lo[i], cur.hi[i], 0, 1, 2, 3, 4, 5, 6, 7);
    acc[i][rt] = MFMA(a, cur.b[rt], acc[i][rt]);
    pread<NF, RS, n>(nxt, smem, aa, ba);
    SBAR();
  });
}
template <int NF> DI u32x4 ppack(const f32x4 (&acc)[NF][4], int i, int rp) {
  auto lo = __builtin_amdgcn_permlane16_swap(pk2(acc[i][2 * rp][0], acc[i][2 * rp][1]), pk2(acc[i][2 * rp + 1][0], acc[i][2 * rp + 1][1]), false, false);
  auto hi = __builtin_amdgcn_permlane16_swap(pk2(acc[i][2 * rp][2], acc[i][2 * rp][3]), pk2(acc[i][2 * rp + 1][2], acc[i][2 * rp + 1][3]), false, false);
  return u32x4{lo[0], hi[0], lo[1], hi[1]};
}
DI void q_handover(const f32x4 (&acc)[8][4], LAS char* smem, int w) {
  const int lane = lane_id(), g = lane >> 4, c = lane & 15;
  LAS char* base = smem + ((w & 1) ? LDS_QP : LDS_QT) + (w >> 1) * 16384 + (g & 1) * 1024 + (g >> 1) * 256 + c * 16;
#pragma unroll
  for (int i = 0; i < 8; ++i)
#pragma unroll
    for (int rp = 0; rp < 2; ++rp) lds_w128(base + rp * 8192 + (i >> 1) * 2048 + (i & 1) * 512, ppack<8>(acc, i, rp));
}
template <int NF> DI void publish(const f32x4 (&acc)[NF][4], char* ws, int lid, int f0, bool ok) {
  const int lane = lane_id(), g = lane >> 4, c = lane & 15;
  const rsrc_t rp = mk_rsrc(ws + (size_t)(lid >> 3) * GSTRIDE + 1024, 64 * SLAB);
  const int voff = (((g & 1) * 2 + (c >> 3)) * 8 + (lid & 7)) * SLAB + f0 * 256 + (g >> 1) * 128 + (c & 7) * 16;
#pragma unroll
  for (int i = 0; i < NF; ++i)
#pragma unroll
    for (int r2 = 0; r2 < 2; ++r2) __builtin_amdgcn_raw_buffer_store_b128(ppack<NF>(acc, i, r2), rp, voff, r2 * 32 * SLAB + i * 256, AUX_SC1);
  asm volatile("s_waitcnt vmcnt(0)" ::: "memory");
  for (unsigned spins = 0; !ok && spins < SPIN_CAP; ++spins) {
    if (lane < 8) hs_echo(ws, lid, lane);
    const unsigned cv = __hip_atomic_load(ctl_chal(ws, (lid & ~7) + (lane & 7), lid & 7), RLX_AGENT);
    const unsigned rv = __hip_atomic_load(ctl_resp(ws, lid, lane & 7), RLX_AGENT);
    ok = __all(cv == rv);
    __builtin_amdgcn_s_sleep(8);
  }
  if (lane < 8) __hip_atomic_fetch_add((unsigned*)(ws + CTL_OFF + (size_t)((lid & ~7) + lane) * 512), 1u, RLX_AGENT);
}
constexpr int QRS = 1056, ORS = 928;
DI int qslot(int i) { return i == 0 ? 92160 : (i == 1 ? 126976 : 0); }
DI int oslot(int i) { return i == 0 ? LDS_K : (i == 1 ? LDS_K + 32768 : LDS_V + 32768); }
DI int obaddr(int bb, int ks) { return (bb ^ ((ks & 1) << 6)) + ks * 4096 + (ks >> 3) * 32768; }

__global__ __launch_bounds__(512, 2) void gqa_fused(const char* __restrict__ xA, const char* __restrict__ kA, const char* __restrict__ vA,
                                                    const char* __restrict__ wqA, const char* __restrict__ woA,
                                                    const char* __restrict__ xB, const char* __restrict__ kB, const char* __restrict__ vB,
                                                    const char* __restrict__ wqB, const char* __restrict__ woB,
                                                    char* __restrict__ out, char* ws) {
  __shared__ __attribute__((aligned(1024))) char smem_[LDS_TOTAL];
  LAS char* smem = (LAS char*)smem_;
  const int wid = __builtin_amdgcn_readfirstlane(threadIdx.x >> 6);
  const int bid = blockIdx.x, blk = (bid >> 2) & 1, kvh = (bid & 3) * 2 + (bid >> 7), bb = (bid >> 3) & 15;
  const int lid = (blk * 16 + bb) * 8 + kvh;
  const int kq0 = bb < 14 ? bb : bb - 14;
#define KQ(S) ((kq0 + (S)) >= 14 ? kq0 + (S) - 14 : kq0 + (S))
#define KO(S) (((S) + bb) & 15)
  const rsrc_t rc = mk_rsrc(ws + CTL_OFF + (size_t)(lid & ~7) * 512, 8 * 512);
#define HS_REQ() const unsigned hsc_ = __builtin_amdgcn_raw_buffer_load_b32(rc, (lane_id() & 7) * 512 + 128 + (lid & 7) * 4, 0, AUX_SC1), \
                                hsr_ = __builtin_amdgcn_raw_buffer_load_b32(rc, (lid & 7) * 512 + 256 + (lane_id() & 7) * 4, 0, AUX_SC1)
#define HS_OK() __all(hsc_ == hsr_)

  if (wid >= 4) {
    int lane = lane_id();
    const int h = wid - 4;
    bool hs = h == 0 && lane < 8;
    __builtin_amdgcn_raw_buffer_store_b32(0u, rc, hs && lane == 0 ? (lid & 7) * 512 : OOB, 0, AUX_SC1);
    const unsigned hs_r = __builtin_amdgcn_raw_buffer_load_b32(rc, hs ? (lid & 7) * 512 + 256 + lane * 4 : OOB, 0, AUX_SC1);
    const rsrc_t rwq = mk_rsrc((blk ? wqB : wqA) + kvh * 1024, W_BYTES - kvh * 1024);
    const rsrc_t rwo = mk_rsrc((blk ? woB : woA) + (size_t)kvh * 512 * DM * 2, 512 * DM * 2);
    const rsrc_t rk = mk_rsrc((blk ? kB : kA) + (size_t)(bb * HKV + kvh) * KV_BYTES, KV_BYTES);
    const rsrc_t rv = mk_rsrc((blk ? vB : vA) + (size_t)(bb * HKV + kvh) * KV_BYTES, KV_BYTES);
    int l16 = lane * 16;
    u32x4 R0[8], R1[8], SA[8];
    const int irow = 16 * (h >> 1) + 4 * (h & 1);
#define QDMA(S) do { _Pragma("unroll") for (int j = 0; j < 8; ++j) \
      dma_nt(rwq, l16, (KQ(S) * 32 + 8 * h + j) * (NQ * 2), qslot((S) % 3) + (irow + 8 * (j >> 2) + (j & 3)) * QRS); SBAR(); } while (0)
#define ODMA(S) do { _Pragma("unroll") for (int j = 0; j < 8; ++j) \
      dma56(rwo, l16, (KO(S) * 32 + 4 * h + 16 * (j >> 2) + (j & 3)) * (DM * 2), oslot((S) % 3) + (irow + 8 * (j >> 2) + (j & 3)) * ORS); SBAR(); } while (0)
#define LOADU(SET, RS, TILE) do { ld8(SET, RS, l16, (TILE) * 32768 + h * 1024, 4096, 16384); SBAR(); } while (0)
    QDMA(0); QDMA(1); QDMA(2);
    WAITV(16);
    WG_BARRIER();
    WAITV(8);
#pragma unroll 1
    for (int s = 0; s < 10; ++s) {
      WG_BARRIER();
      QDMA(s + 3);
      WAITV(8);
    }
    WG_BARRIER();
    QDMA(13);
    LOADU(R0, rk, 0);
    WAITV(16);
    WG_BARRIER();
    WAITV(8);
    LOADU(R1, rk, 1);
    WG_BARRIER();
    __builtin_amdgcn_raw_buffer_store_b32(hs_r + 1u, rc, hs ? lane * 512 + 128 + (lid & 7) * 4 : OOB, 0, AUX_SC1);
    WG_BARRIER();
    LOADU(SA, rv, 0);
    lane = lane_id(); l16 = lane * 16; hs = h == 0 && lane < 8;
    const int hi = lane >> 5, ch = lane & 31;
    const int row0 = 2 * h + hi;
    const int kw0 = LDS_K + row0 * 512 + ((ch ^ row0) << 4), kw1 = LDS_K + (row0 + 8) * 512 + ((ch ^ row0 ^ 8) << 4);
    const int vw0 = LDS_V + row0 * 512 + ((ch ^ (row0 << 1)) << 4);
#define WRITEK(SET, BUF) do { _Pragma("unroll") for (int i = 0; i < 8; ++i) \
      lds_w128(smem + ((i & 1) ? kw1 : kw0) + (BUF) + (i >> 1) * 8192, SET[i]); SBAR(); } while (0)
#define WRITEV(SET, BUF) do { _Pragma("unroll") for (int i = 0; i < 8; ++i) \
      lds_w128(smem + vw0 + (BUF) + i * 4096, SET[i]); SBAR(); } while (0)
    const int kv0 = row0 * 512 + ((ch ^ row0) << 4), kv1 = row0 * 512 + ((ch ^ row0 ^ 8) << 4), vv0 = row0 * 512 + ((ch ^ (row0 << 1)) << 4);
#define KDMA(TILE, HALF) do { _Pragma("unroll") for (int i = 0; i < 4; ++i) \
      dma_nt(rk, (i & 1) ? kv1 : kv0, (TILE) * 32768 + (HALF) * 16384 + i * 4096, LDS_K + ((TILE) & 1) * 32768 + (HALF) * 16384 + i * 4096 + h * 1024); SBAR(); } while (0)
#define VDMA(TILE, HALF) do { _Pragma("unroll") for (int i = 0; i < 4; ++i) \
      dma_nt(rv, vv0, (TILE) * 32768 + (HALF) * 16384 + i * 4096, LDS_V + ((TILE) & 1) * 32768 + (HALF) * 16384 + i * 4096 + h * 1024); SBAR(); } while (0)
    WRITEK(R0, 0);
    WG_BARRIER();
    WRITEK(R1, 32768);
    WG_BARRIER();
    WRITEV(SA, 0);
    VDMA(1, 0); KDMA(2, 0);
    WG_BARRIER();
    WG_BARRIER();
    VDMA(1, 1); KDMA(2, 1);
    WAITV(8);
#pragma unroll 1
    for (int n = 1; n < 27; ++n) {
      WG_BARRIER();
      const int tv = (n + 3) >> 1, hf = (n + 3) & 1;
      VDMA(tv, hf); KDMA(tv + 1, hf);
      if (n == 20 && hs) hs_echo(ws, lid, lane);
      WAITV(8);
    }
    WG_BARRIER();
    VDMA(15, 0);
    WAITV(4);
    WG_BARRIER();
    VDMA(15, 1);
    ODMA(0);
    WAITV(12);
    WG_BARRIER();
    WAITV(8);
#undef KDMA
#undef VDMA
    WG_BARRIER();
    ODMA(1);
    WAITV(8);
    WG_BARRIER();
    ODMA(2);
    WAITV(8);
#pragma unroll 1
    for (int s = 0; s < 13; ++s) {
      WG_BARRIER();
      ODMA(s + 3);
      WAITV(8);
    }
    WG_BARRIER();
    WAITV(0);
    WG_BARRIER();
    WG_BARRIER();
    if (h == 0) {
      const unsigned* cnt = (const unsigned*)(ws + CTL_OFF + (size_t)lid * 512);
      for (unsigned spins = 0; spins < SPIN_CAP; ++spins) {
        if (__hip_atomic_load(cnt, RLX_AGENT) >= 32u) break;
        if ((spins & 63u) == 63u) { const int l = lane_id(); if (l < 8) hs_echo(ws, lid, l); }
        __builtin_amdgcn_s_sleep(2);
      }
    }
#undef QDMA
#undef ODMA
#undef LOADU
#undef WRITEK
#undef WRITEV
  } else {
    const unsigned c0_ = (unsigned)__builtin_amdgcn_s_memtime(), r0_ = (unsigned)__builtin_amdgcn_s_memrealtime();
    {
      const int lane = lane_id();
      const rsrc_t rx = mk_rsrc((blk ? xB : xA) + (size_t)bb * SQ * DM * 2, SQ * DM * 2);
      const int xp = ((wid + (kvh >> 1)) & 3) * 14;
      u32x4 xr[14];
#pragma unroll
      for (int f = 0; f < 14; ++f) xr[f] = __builtin_amdgcn_raw_buffer_load_b128(rx, lane * 16, (xp + f) * 1024, AUX_NT);
      SBAR();
      {
        const char* p0 = wid == 0 ? (blk ? kB : kA) + (size_t)(bb * HKV + kvh) * KV_BYTES
                       : wid == 1 ? (blk ? woB : woA) + (size_t)kvh * 512 * DM * 2
                       : wid == 2 ? (const char*)ws + (size_t)(lid >> 3) * GSTRIDE : (const char*)out + ((size_t)(bb * SQ + kvh * 8) * (2 * DM) + blk * DM) * 2;
        const char* p1 = wid == 0 ? (blk ? vB : vA) + (size_t)(bb * HKV + kvh) * KV_BYTES
                       : wid == 1 ? p0 + 512 * DM * 2 - 64 : wid == 2 ? p0 + GSTRIDE - 64 : p0 + 7 * (2 * DM) * 2;
        const unsigned long long a0 = (unsigned long long)p0, a1 = (unsigned long long)p1;
        const unsigned long long u0 = ((unsigned long long)__builtin_amdgcn_readfirstlane((unsigned)(a0 >> 32)) << 32) | (unsigned)__builtin_amdgcn_readfirstlane((unsigned)a0);
        const unsigned long long u1 = ((unsigned long long)__builtin_amdgcn_readfirstlane((unsigned)(a1 >> 32)) << 32) | (unsigned)__builtin_amdgcn_readfirstlane((unsigned)a1);
        unsigned d0, d1;
        asm volatile("s_nop 4\n\ts_load_dword %0, %2, 0x0\n\ts_load_dword %1, %3, 0x0\n\ts_waitcnt lgkmcnt(0)" : "=&s"(d0), "=&s"(d1) : "s"(u0), "s"(u1) : "memory");
      }
#pragma unroll
      for (int f = 0; f < 14; ++f) {
        const unsigned idx = (xp + f) * 64 + lane, row = idx / 56u, q = idx - row * 56u;
        lds_w128(smem + LDS_X + (q >> 2) * 4096 + (row >> 4) * 1024 + (q & 3) * 256 + (((row & 15) ^ (q & 7)) << 4), xr[f]);
      }
    }
    WG_BARRIER();
    const int fastclk = __builtin_amdgcn_readfirstlane((int)(((unsigned)__builtin_amdgcn_s_memtime() - c0_) * 16u - ((unsigned)__builtin_amdgcn_s_memrealtime() - r0_) * (unsigned)AP_TH));
    __builtin_amdgcn_s_waitcnt(0xC07F); SBAR();
    {
      f32x4 acc[8][4]; PFrag<8> F[2];
#pragma unroll
      for (int i = 0; i < 8; ++i)
#pragma unroll
        for (int rt = 0; rt < 4; ++rt) acc[i][rt] = f32x4{0.f, 0.f, 0.f, 0.f};
      const int ab = pabase<QRS>(wid * 8), bbs = pbbase(LDS_X);
      { const int aa = ab + qslot(0), ba = pbaddr(bbs, kq0); static_for<20>([&](auto kc) { pread<8, QRS, decltype(kc)::value>(F[0], smem, aa, ba); }); }
      int sl = 1;
#pragma unroll 1
      for (int s = 0; s < 14; s += 2) {
        WG_BARRIER();
        pstep<8, QRS>(acc, F[0], F[1], smem, ab + qslot(sl), pbaddr(bbs, KQ(s + 1)));
        sl = sl == 2 ? 0 : sl + 1;
        WG_BARRIER();
        pstep<8, QRS>(acc, F[1], F[0], smem, ab + qslot(sl), pbaddr(bbs, KQ(s + 2)));
        sl = sl == 2 ? 0 : sl + 1;
      }
      q_handover(acc, smem, wid);
    }
    {
      const int w = wid, hh = w >> 1, rh = w & 1;
      const int lane = lane_id(), g = lane >> 4, c = lane & 15;
      f32x4 o[16][2];
#pragma unroll
      for (int dt = 0; dt < 16; ++dt) { o[dt][0] = f32x4{0.f, 0.f, 0.f, 0.f}; o[dt][1] = f32x4{0.f, 0.f, 0.f, 0.f}; }
      float mc[2] = {0.f, 0.f}, lsum[2] = {0.f, 0.f};
      f32x4 s0[2][2], s1[2][2];
      u32x4 p0[2], p1[2];
      u32x4 qown[4][2];
      int kb = LDS_K + c * 512 + ((g ^ c) << 4);
      int vb = LDS_V + (4 * g + (c >> 2)) * 512 + ((((4 * g + (c >> 2)) & 7)) << 5) + (c & 3) * 8;
      LAS char* qp = smem + LDS_QP + w * 8192 + lane * 16;
      constexpr int PD = 4, RN = 6;
      bf16x4 vl[RN], vh[RN]; bf16x8 kf[RN], qf[2][2];
      float t0, t1, t2, t3, mx;
      int fa = 0;
#define XORI(IMM, V) do { asm volatile("" : "+v"(V)); fa = (V) ^ (IMM); } while (0)
#define FETCH(N) do { constexpr int n_ = (N), blk_ = n_ >> 4, j_ = n_ & 15; \
        if (blk_ & 1) { if ((j_ & 3) == 0) XORI((j_ >> 2) << 6, kb); \
          kf[n_ % RN] = asbf(lds_r128(smem + fa + ((j_ >> 1) & 1) * 256 + (j_ & 1) * 8192 + (blk_ >> 1) * 16384)); } \
        else { if ((j_ & 1) == 0) XORI((j_ >> 1) << 5, vb); \
          LAS char* a_ = smem + fa + (j_ & 1) * 256 + (blk_ >> 1) * 16384; vl[n_ % RN] = lds_tr(a_); vh[n_ % RN] = lds_tr(a_ + 8192); } } while (0)
#define FETCHQ(V, RT) qf[(V) & 1][RT] = asbf(lds_r128(qp + ((V) * 2 + (RT)) * 1024))
#define XE(X, e) X[(e) >> 3][((e) >> 2) & 1][(e) & 3]
#define JOB_AB(X, j) do { \
        if ((j) < 4) { _Pragma("unroll") for (int i_ = 0; i_ < 4; ++i_) XE(X, 4 * (j) + i_) = fmaf(XE(X, 4 * (j) + i_), SCL, -mc[(j) & 1]); } \
        else if ((j) == 4) { t0 = max3(X[0][0][0], X[0][0][1], X[0][0][2]); t1 = max3(X[0][1][0], X[0][1][1], X[0][1][2]); t2 = max3(X[0][0][3], X[1][0][0], X[1][0][1]); } \
        else if ((j) == 5) { t3 = max3(X[0][1][3], X[1][1][0], X[1][1][1]); t0 = max3(t0, X[1][0][2], X[1][0][3]); t1 = max3(t1, X[1][1][2], X[1][1][3]); } \
        else if ((j) == 6) { t0 = fmaxf(t0, t2); t1 = fmaxf(t1, t3); } \
        else if ((j) == 7) { auto a_ = __builtin_amdgcn_permlane16_swap(__float_as_uint(t0), __float_as_uint(t1), false, false); \
                             t0 = fmaxf(__uint_as_float(a_[0]), __uint_as_float(a_[1])); } \
        else if ((j) == 8) { mx = hmax(t0); } } while (0)
#define JOB_EXP(X, P, j) do { XE(X, j) = __builtin_amdgcn_exp2f(XE(X, j)); \
        if ((j) > 0) lsum[(((j) - 1) >> 2) & 1] += XE(X, (j) - 1); \
        if ((j) >= 2 && !((j) & 1)) P[(((j) - 2) >> 2) & 1][(((j) - 2) >> 3) * 2 + ((((j) - 2) >> 1) & 1)] = pk2(XE(X, (j) - 2), XE(X, (j) - 1)); \
        if ((j) == 15) { lsum[1] += XE(X, 15); P[1][3] = pk2(XE(X, 14), XE(X, 15)); } } while (0)
#define MX2(M) do { auto m_ = __builtin_amdgcn_permlane16_swap(__float_as_uint(mx), __float_as_uint(mx), false, false); \
        M[0] = __uint_as_float(m_[0]); M[1] = __uint_as_float(m_[1]); } while (0)
#define RESCALE(X, PEND) do { if (__any(mx > THR)) { float d_[2], al_[2]; MX2(d_); \
          _Pragma("unroll") for (int r_ = 0; r_ < 2; ++r_) { d_[r_] = fmaxf(d_[r_], 0.f); al_[r_] = __builtin_amdgcn_exp2f(-d_[r_]); mc[r_] += d_[r_]; lsum[r_] *= al_[r_]; \
            X[0][r_] -= d_[r_]; X[1][r_] -= d_[r_]; \
            _Pragma("unroll") for (int dt = 0; dt < 16; ++dt) o[dt][r_] *= al_[r_]; \
            if (PEND) { _Pragma("unroll") for (int i_ = 0; i_ < 4; ++i_) \
              p0[r_][i_] = pk2(__uint_as_float(p0[r_][i_] << 16) * al_[r_], __uint_as_float(p0[r_][i_] & 0xffff0000u) * al_[r_]); } } } SBAR(); } while (0)
#define FETCH_AHEAD(B, j) switch (j) { \
            case 0: FETCH((B) * 16 + 0 + PD); break; case 1: FETCH((B) * 16 + 1 + PD); break; case 2: FETCH((B) * 16 + 2 + PD); break; case 3: FETCH((B) * 16 + 3 + PD); break; \
            case 4: FETCH((B) * 16 + 4 + PD); break; case 5: FETCH((B) * 16 + 5 + PD); break; case 6: FETCH((B) * 16 + 6 + PD); break; case 7: FETCH((B) * 16 + 7 + PD); break; \
            case 8: FETCH((B) * 16 + 8 + PD); break; case 9: FETCH((B) * 16 + 9 + PD); break; case 10: FETCH((B) * 16 + 10 + PD); break; case 11: FETCH((B) * 16 + 11 + PD); break; \
            case 12: FETCH(((B) * 16 + 12 + PD) & 63); break; case 13: FETCH(((B) * 16 + 13 + PD) & 63); break; case 14: FETCH(((B) * 16 + 14 + PD) & 63); break; default: FETCH(((B) * 16 + 15 + PD) & 63); break; }
#define PVBLOCK(B, P, X, LAST, PN) do { _Pragma("unroll") for (int j = 0; j < 16; ++j) { \
          const bf16x8 a_ = __builtin_shufflevector(vl[((B) * 16 + j) % RN], vh[((B) * 16 + j) % RN], 0, 1, 2, 3, 4, 5, 6, 7); \
          o[(j >> 1) + 8 * (j & 1)][0] = MFMA(a_, asbf(P[0]), o[(j >> 1) + 8 * (j & 1)][0]); \
          if (j + PD < 16 || !(LAST)) { FETCH_AHEAD(B, j) } \
          SBAR(); \
          o[(j >> 1) + 8 * (j & 1)][1] = MFMA(a_, asbf(P[1]), o[(j >> 1) + 8 * (j & 1)][1]); \
          if (!(LAST) && j >= 14) FETCHQ(0, j - 14); \
          if (!(LAST)) JOB_AB(X, j); \
          PACE(PN); \
          SBAR(); } } while (0)
#define QKBLOCK(B, S, NEXT, JOB, X, P, PN) do { _Pragma("unroll") for (int j = 0; j < 16; ++j) { \
          const bf16x8 q0_ = (j & 2) ? qf[(j >> 2) & 1][0] : asbf(qown[j >> 2][0]), q1_ = (j & 2) ? qf[(j >> 2) & 1][1] : asbf(qown[j >> 2][1]); \
          const f32x4 z_ = {0.f, 0.f, 0.f, 0.f}; \
          S[j & 1][0] = MFMA(kf[((B) * 16 + j) % RN], q0_, j < 2 ? z_ : S[j & 1][0]); \
          if (j + PD < 16 || (NEXT)) { FETCH_AHEAD(B, j) } \
          SBAR(); \
          S[j & 1][1] = MFMA(kf[((B) * 16 + j) % RN], q1_, j < 2 ? z_ : S[j & 1][1]); \
          if (!(j & 2) && j >= 4) FETCHQ(j >> 2, j & 1); \
          if (JOB) JOB_EXP(X, P, j); \
          PACE(PN); \
          SBAR(); } } while (0)
#define FETCH4(N) do { FETCH(N); FETCH((N) + 1); FETCH((N) + 2); FETCH((N) + 3); } while (0)
      static_assert(PD == 4, "FETCH4");
#define RAWBAR() do { SBAR(); asm volatile("s_barrier" ::: "memory"); SBAR(); } while (0)
      WG_BARRIER();
#pragma unroll
      for (int f = 0; f < 8; ++f) qown[f >> 1][f & 1] = lds_r128(smem + LDS_QT + w * 8192 + f * 1024 + lane * 16);
      FETCH4(16); FETCHQ(0, 0); FETCHQ(0, 1); SBAR();
      QKBLOCK(1, s0, 0, 0, s0, p0, -1);
      FETCH4(48); FETCHQ(0, 0); FETCHQ(0, 1); SBAR();
#pragma unroll
      for (int j = 0; j < 9; ++j) JOB_AB(s0, j);
      MX2(mc);
#pragma unroll
      for (int r = 0; r < 2; ++r) { s0[0][r] -= mc[r]; s0[1][r] -= mc[r]; }
      RAWBAR();
      QKBLOCK(3, s1, 0, 1, s0, p0, -1);
      kb ^= 32768;
      RAWBAR();
      FETCH4(0); SBAR();
#define ATT_LOOP(PN, T0, T1) _Pragma("unroll 1") for (int t = (T0); t < (T1); ++t) { \
        RAWBAR();     \
        PVBLOCK(0, p0, s1, 0, PN); \
        RESCALE(s1, 0); \
        QKBLOCK(1, s0, 1, 1, s1, p1, PN); \
        RAWBAR();     \
        PVBLOCK(2, p1, s0, 0, PN); \
        vb ^= 32768; \
        RESCALE(s0, 0); \
        QKBLOCK(3, s1, 1, 1, s0, p0, PN); \
        kb ^= 32768; }
      const int nslow = fastclk > 0 ? 15 : 0;
      ATT_LOOP(AP_P, 0, nslow)
      ATT_LOOP(-1, nslow, 15)
#undef ATT_LOOP
#pragma unroll
      for (int j = 0; j < 9; ++j) JOB_AB(s1, j);
      RESCALE(s1, 1);
#pragma unroll
      for (int j = 0; j < 16; ++j) JOB_EXP(s1, p1, j);
      float iv[2];
#pragma unroll
      for (int r = 0; r < 2; ++r) {
        auto a = __builtin_amdgcn_permlane16_swap(__float_as_uint(lsum[r]), __float_as_uint(lsum[r]), false, false);
        iv[r] = 1.f / hsum(__uint_as_float(a[0]) + __uint_as_float(a[1]));
      }
      SBAR();
      PVBLOCK(0, p0, s1, 1, -1);
      RAWBAR();
      FETCH4(32); SBAR();
      PVBLOCK(2, p1, s0, 1, -1);
#undef FETCH
#undef XORI
#undef FETCHQ
#undef FETCH4
#undef FETCH_AHEAD
#undef JOB_AB
#undef JOB_EXP
#undef RESCALE
#undef MX2
#undef XE
#undef PVBLOCK
#undef QKBLOCK
      const int lane2 = lane_id(), g2 = lane2 >> 4, c2 = lane2 & 15;
      LAS char* ow = smem + LDS_V + hh * 65536 + rh * 2048 + g2 * 256;
#pragma unroll
      for (int kk = 0; kk < 8; ++kk)
#pragma unroll
        for (int r = 0; r < 2; ++r) {
          const f32x4 a = o[2 * kk][r] * iv[r], b = o[2 * kk + 1][r] * iv[r];
          lds_w128(ow + kk * 4096 + r * 1024 + ((c2 ^ g2 ^ ((kk & 1) * 4)) << 4), u32x4{pk2(a[0], a[1]), pk2(a[2], a[3]), pk2(b[0], b[1]), pk2(b[2], b[3])});
        }
    }
    WG_BARRIER();
    {
      HS_REQ();
      f32x4 acc[7][4]; PFrag<7> F[2];
#pragma unroll
      for (int i = 0; i < 7; ++i)
#pragma unroll
        for (int rt = 0; rt < 4; ++rt) acc[i][rt] = f32x4{0.f, 0.f, 0.f, 0.f};
      const int ab = pabase<ORS>(wid * 7), bbs = pbbase(LDS_V);
      { const int aa = ab + oslot(0), ba = obaddr(bbs, KO(0)); static_for<18>([&](auto kc) { pread<7, ORS, decltype(kc)::value>(F[0], smem, aa, ba); }); }
      int sl = 1;
#pragma unroll 1
      for (int s = 0; s < 16; s += 2) {
        WG_BARRIER();
        pstep<7, ORS>(acc, F[0], F[1], smem, ab + oslot(sl), obaddr(bbs, KO(s + 1)));
        sl = sl == 2 ? 0 : sl + 1;
        WG_BARRIER();
        pstep<7, ORS>(acc, F[1], F[0], smem, ab + oslot(sl), obaddr(bbs, KO(s + 2)));
        sl = sl == 2 ? 0 : sl + 1;
      }
      publish<7>(acc, ws, lid, wid * 7, HS_OK());
    }
  }
  WG_BARRIER();
  if (wid < 7) {
    const int lane = lane_id();
    const rsrc_t rp = mk_rsrc(ws + (size_t)(lid >> 3) * GSTRIDE + 1024 + kvh * 8 * SLAB, 8 * SLAB);
    u32x4 v[8];
#pragma unroll
    for (int s = 0; s < 8; ++s) v[s] = __builtin_amdgcn_raw_buffer_load_b128(rp, wid * 1024 + lane * 16, s * SLAB, AUX_SC1);
    float a[8];
#pragma unroll
    for (int k = 0; k < 4; ++k) { a[2 * k] = __uint_as_float(v[0][k] << 16); a[2 * k + 1] = __uint_as_float(v[0][k] & 0xffff0000u); }
#pragma unroll
    for (int s = 1; s < 8; ++s)
#pragma unroll
      for (int k = 0; k < 4; ++k) { a[2 * k] += __uint_as_float(v[s][k] << 16); a[2 * k + 1] += __uint_as_float(v[s][k] & 0xffff0000u); }
    const rsrc_t ro = mk_rsrc(out + (size_t)(bb * SQ + kvh * 8) * (2 * DM) * 2, 8 * 2 * DM * 2);
    __builtin_amdgcn_raw_buffer_store_b128(u32x4{pk2(a[0], a[1]), pk2(a[2], a[3]), pk2(a[4], a[5]), pk2(a[6], a[7])}, ro,
                                           ((lane & 7) * (2 * DM) + blk * DM + wid * 64 + (lane >> 3) * 8) * 2, 0, 0);
  }
}
}

extern "C" void kernel_launch(void* const* d_in, const int* in_sizes, int n_in, void* d_out, int out_size, void* d_ws, size_t ws_size,
                              hipStream_t stream) {
  (void)in_sizes; (void)n_in; (void)out_size; (void)ws_size;
  gqa_fused<<<256, 512, 0, stream>>>((const char*)d_in[0], (const char*)d_in[1], (const char*)d_in[2], (const char*)d_in[3], (const char*)d_in[4],
                                     (const char*)d_in[5], (const char*)d_in[6], (const char*)d_in[7], (const char*)d_in[8], (const char*)d_in[9],
                                     (char*)d_out, (char*)d_ws);
}
```

```cpp
#include <hip/hip_runtime.h>
#include <hip/hip_bf16.h>
#include <utility>
#include <type_traits>

#define LAS __attribute__((address_space(3)))
#define DI __device__ __forceinline__
typedef __attribute__((ext_vector_type(8))) __bf16 bf16x8;
typedef __attribute__((ext_vector_type(4))) __bf16 bf16x4;
typedef __attribute__((ext_vector_type(2))) __bf16 bf16x2;
typedef __attribute__((ext_vector_type(4))) float f32x4;
typedef __attribute__((ext_vector_type(2))) float f32x2;
typedef __attribute__((ext_vector_type(4))) unsigned u32x4;

namespace {
constexpr int SQ = 64, DM = 448, HKV = 8, SK = 1024, DH = 256;
constexpr int NQ = 4096;
constexpr int KV_BYTES = SK * DH * 2;
constexpr int W_BYTES = DM * NQ * 2;
constexpr int LDS_K = 0, LDS_V = 65536, LDS_QP = 131072, LDS_TOTAL = 163840;
constexpr int LDS_X = 34816, LDS_QT = LDS_V + 32768;
constexpr int SLAB = 7168;
constexpr int GSTRIDE = 64 * SLAB + 2048;
constexpr size_t CTL_OFF = (size_t)16 << 20;
constexpr float SCL = 0.0625f * 1.4426950408889634f;
constexpr float THR = 8.f;
constexpr int AUX_NT = 2, AUX_SC1 = 16;
constexpr int OOB = 0x40000000;
constexpr unsigned SPIN_CAP = 1u << 21;

#define SBAR() __builtin_amdgcn_sched_barrier(0)
#define WG_BARRIER() do { SBAR(); asm volatile("s_waitcnt lgkmcnt(0)\n\ts_barrier" ::: "memory"); SBAR(); } while (0)
#define PACE(PN) do { if ((PN) >= 0) asm volatile("s_nop %0" :: "i"((PN) < 0 ? 0 : (PN))); } while (0)
constexpr int AP_TH = 328;
constexpr int AP_P = 3;
#define MFMA(a, b, c) __builtin_amdgcn_mfma_f32_16x16x32_bf16((a), (b), (c), 0, 0, 0)
#define RLX_AGENT __ATOMIC_RELAXED, __HIP_MEMORY_SCOPE_AGENT

DI unsigned pk2(float a, float b) { f32x2 f = {a, b}; bf16x2 r = __builtin_convertvector(f, bf16x2); return __builtin_bit_cast(unsigned, r); }
DI bf16x8 asbf(u32x4 v) { return __builtin_bit_cast(bf16x8, v); }
DI u32x4 lds_r128(LAS char* p) { return *(LAS u32x4*)p; }
DI void lds_w128(LAS char* p, u32x4 v) { *(LAS u32x4*)p = v; }
DI bf16x4 lds_tr(LAS char* p) { return __builtin_amdgcn_ds_read_tr16_b64_v4bf16((LAS bf16x4*)p); }
DI float max3(float a, float b, float c) { return fmaxf(fmaxf(a, b), c); }
DI float hmax(float v) {
  auto b = __builtin_amdgcn_permlane32_swap(__float_as_uint(v), __float_as_uint(v), false, false);
  return fmaxf(__uint_as_float(b[0]), __uint_as_float(b[1]));
}
DI float hsum(float v) {
  auto b = __builtin_amdgcn_permlane32_swap(__float_as_uint(v), __float_as_uint(v), false, false);
  return __uint_as_float(b[0]) + __uint_as_float(b[1]);
}
DI int lane_id() {
  unsigned m = ~0u; asm volatile("" : "+s"(m)); return (int)__builtin_amdgcn_mbcnt_hi(m, __builtin_amdgcn_mbcnt_lo(m, 0u)); }
typedef __amdgpu_buffer_rsrc_t rsrc_t;
DI rsrc_t mk_rsrc(const void* p, int bytes) { return __builtin_amdgcn_make_buffer_rsrc((void*)p, 0, bytes, 0x00020000); }
template <class F, int... I> DI void static_for_(F&& f, std::integer_sequence<int, I...>) { (f(std::integral_constant<int, I>{}), ...); }
template <int N, class F> DI void static_for(F&& f) { static_for_(f, std::make_integer_sequence<int, N>{}); }

DI unsigned* ctl_chal(char* ws, int wg, int slot) { return (unsigned*)(ws + CTL_OFF + (size_t)wg * 512 + 128 + slot * 4); }
DI unsigned* ctl_resp(char* ws, int wg, int slot) { return (unsigned*)(ws + CTL_OFF + (size_t)wg * 512 + 256 + slot * 4); }
DI void hs_echo(char* ws, int lid, int lane) {
  const unsigned cv = __hip_atomic_load(ctl_chal(ws, lid, lane), RLX_AGENT);
  const unsigned rv = __hip_atomic_load(ctl_resp(ws, (lid & ~7) + lane, lid & 7), RLX_AGENT);
  if (cv != rv) __hip_atomic_store(ctl_resp(ws, (lid & ~7) + lane, lid & 7), cv, RLX_AGENT);
}
DI u32x4 ld1(rsrc_t rs, int voff, int soff) { return __builtin_amdgcn_raw_buffer_load_b128(rs, voff, soff, AUX_NT); }
DI void ld8(u32x4 (&r)[8], rsrc_t rs, int voff, int soff, int sa, int sb) {
#pragma unroll
  for (int j = 0; j < 8; ++j) r[j] = ld1(rs, voff, soff + (j & 3) * sa + (j >> 2) * sb);
}

DI void dma_nt(rsrc_t rs, int voff, int soff, int ldsaddr) {
  asm volatile("s_mov_b32 m0, %0\n\ts_nop 0\n\tbuffer_load_dwordx4 %1, %2, %3 offen nt lds" :: "s"(ldsaddr), "v"(voff), "s"(rs), "s"(soff) : "memory");
}
DI void dma56(rsrc_t rs, int voff, int soff, int ldsaddr) {
  asm volatile("s_mov_b32 m0, %0\n\ts_mov_b32 exec_hi, 0xffffff\n\tbuffer_load_dwordx4 %1, %2, %3 offen lds\n\ts_mov_b32 exec_hi, -1"
               :: "s"(ldsaddr), "v"(voff), "s"(rs), "s"(soff) : "memory");
}
#define WAITV(N) do { SBAR(); asm volatile("s_waitcnt vmcnt(" #N ")" ::: "memory"); SBAR(); } while (0)
template <int NF> struct PFrag { bf16x4 lo[NF], hi[NF]; bf16x8 b[4]; };
template <int RS> DI int pabase(int f0) {
  const int lane = lane_id(), g = lane >> 4, q = (lane & 15) >> 2, p = lane & 3;
  return (16 * (g >> 1) + 4 * (g & 1) + q) * RS + f0 * 32 + p * 8;
}
DI int pbbase(int bimg) { const int lane = lane_id(), g = lane >> 4, c = lane & 15; return bimg + g * 256 + ((c ^ g) << 4); }
DI int pbaddr(int bb, int ks) { return (bb ^ ((ks & 1) << 6)) + ks * 4096; }
template <int NF, int RS, int K> DI void pread(PFrag<NF>& f, LAS char* smem, int aa, int ba) {
  if constexpr (K < 4) f.b[K] = asbf(lds_r128(smem + ba + K * 1024));
  else if constexpr (K < 4 + 2 * NF) {
    if constexpr ((K & 1) == 0) f.lo[(K - 4) >> 1] = lds_tr(smem + aa + ((K - 4) >> 1) * 32);
    else f.hi[(K - 4) >> 1] = lds_tr(smem + aa + ((K - 4) >> 1) * 32 + 8 * RS);
  }
}
template <int NF, int RS>
DI void pstep(f32x4 (&acc)[NF][4], const PFrag<NF>& cur, PFrag<NF>& nxt, LAS char* smem, int aa, int ba) {
  static_for<NF * 4>([&](auto nc) {
    constexpr int n = decltype(nc)::value, i = n >> 2, rt = n & 3;
    const bf16x8 a = __builtin_shufflevector(cur.lo[i], cur.hi[i], 0, 1, 2, 3, 4, 5, 6, 7);
    acc[i][rt] = MFMA(a, cur.b[rt], acc[i][rt]);
    pread<NF, RS, n>(nxt, smem, aa, ba);
    SBAR();
  });
}
template <int NF> DI u32x4 ppack(const f32x4 (&acc)[NF][4], int i, int rp) {
  auto lo = __builtin_amdgcn_permlane16_swap(pk2(acc[i][2 * rp][0], acc[i][2 * rp][1]), pk2(acc[i][2 * rp + 1][0], acc[i][2 * rp + 1][1]), false, false);
  auto hi = __builtin_amdgcn_permlane16_swap(pk2(acc[i][2 * rp][2], acc[i][2 * rp][3]), pk2(acc[i][2 * rp + 1][2], acc[i][2 * rp + 1][3]), false, false);
  return u32x4{lo[0], hi[0], lo[1], hi[1]};
}
DI void q_handover(const f32x4 (&acc)[8][4], LAS char* smem, int w) {
  const int lane = lane_id(), g = lane >> 4, c = lane & 15;
  LAS char* base = smem + ((w & 1) ? LDS_QP : LDS_QT) + (w >> 1) * 16384 + (g & 1) * 1024 + (g >> 1) * 256 + c * 16;
#pragma unroll
  for (int i = 0; i < 8; ++i)
#pragma unroll
    for (int rp = 0; rp < 2; ++rp) lds_w128(base + rp * 8192 + (i >> 1) * 2048 + (i & 1) * 512, ppack<8>(acc, i, rp));
}
template <int NF> DI void publish(const f32x4 (&acc)[NF][4], char* ws, int lid, int f0, bool ok) {
  const int lane = lane_id(), g = lane >> 4, c = lane & 15;
  const rsrc_t rp = mk_rsrc(ws + (size_t)(lid >> 3) * GSTRIDE + 1024, 64 * SLAB);
  const int voff = (((g & 1) * 2 + (c >> 3)) * 8 + (lid & 7)) * SLAB + f0 * 256 + (g >> 1) * 128 + (c & 7) * 16;
#pragma unroll
  for (int i = 0; i < NF; ++i)
#pragma unroll
    for (int r2 = 0; r2 < 2; ++r2) __builtin_amdgcn_raw_buffer_store_b128(ppack<NF>(acc, i, r2), rp, voff, r2 * 32 * SLAB + i * 256, AUX_SC1);
  asm volatile("s_waitcnt vmcnt(0)" ::: "memory");
  for (unsigned spins = 0; !ok && spins < SPIN_CAP; ++spins) {
    if (lane < 8) hs_echo(ws, lid, lane);
    const unsigned cv = __hip_atomic_load(ctl_chal(ws, (lid & ~7) + (lane & 7), lid & 7), RLX_AGENT);
    const unsigned rv = __hip_atomic_load(ctl_resp(ws, lid, lane & 7), RLX_AGENT);
    ok = __all(cv == rv);
    __builtin_amdgcn_s_sleep(8);
  }
  if (lane < 8) __hip_atomic_fetch_add((unsigned*)(ws + CTL_OFF + (size_t)((lid & ~7) + lane) * 512), 1u, RLX_AGENT);
}
constexpr int QRS = 1056, ORS = 928;
DI int qslot(int i) { return i == 0 ? 92160 : (i == 1 ? 126976 : 0); }
DI int oslot(int i) { return i == 0 ? LDS_K : (i == 1 ? LDS_K + 32768 : LDS_V + 32768); }
DI int obaddr(int bb, int ks) { return (bb ^ ((ks & 1) << 6)) + ks * 4096 + (ks >> 3) * 32768; }

__global__ __launch_bounds__(512, 2) void gqa_fused(const char* __restrict__ xA, const char* __restrict__ kA, const char* __restrict__ vA,
                                                    const char* __restrict__ wqA, const char* __restrict__ woA,
                                                    const char* __restrict__ xB, const char* __restrict__ kB, const char* __restrict__ vB,
                                                    const char* __restrict__ wqB, const char* __restrict__ woB,
                                                    char* __restrict__ out, char* ws) {
  __shared__ __attribute__((aligned(1024))) char smem_[LDS_TOTAL];
  LAS char* smem = (LAS char*)smem_;
  const int wid = __builtin_amdgcn_readfirstlane(threadIdx.x >> 6);
  unsigned long long pc0; int clen;
  asm volatile("s_getpc_b64 %0\n.Lgqa_pc0:\n\ts_mov_b32 %1, .Lgqa_end-.Lgqa_pc0" : "=s"(pc0), "=s"(clen));
  const int bid = blockIdx.x, blk = (bid >> 2) & 1, kvh = (bid & 3) * 2 + (bid >> 7), bb = (bid >> 3) & 15;
  const int lid = (blk * 16 + bb) * 8 + kvh;
  const int kq0 = bb < 14 ? bb : bb - 14;
#define KQ(S) ((kq0 + (S)) >= 14 ? kq0 + (S) - 14 : kq0 + (S))
#define KO(S) (((S) + bb) & 15)
  const rsrc_t rc = mk_rsrc(ws + CTL_OFF + (size_t)(lid & ~7) * 512, 8 * 512);
#define HS_REQ() const unsigned hsc_ = __builtin_amdgcn_raw_buffer_load_b32(rc, (lane_id() & 7) * 512 + 128 + (lid & 7) * 4, 0, AUX_SC1), \
                                hsr_ = __builtin_amdgcn_raw_buffer_load_b32(rc, (lid & 7) * 512 + 256 + (lane_id() & 7) * 4, 0, AUX_SC1)
#define HS_OK() __all(hsc_ == hsr_)

  if (wid >= 4) {
    int lane = lane_id();
    const int h = wid - 4;
    bool hs = h == 0 && lane < 8;
    __builtin_amdgcn_raw_buffer_store_b32(0u, rc, hs && lane == 0 ? (lid & 7) * 512 : OOB, 0, AUX_SC1);
    const unsigned hs_r = __builtin_amdgcn_raw_buffer_load_b32(rc, hs ? (lid & 7) * 512 + 256 + lane * 4 : OOB, 0, AUX_SC1);
    const rsrc_t rwq = mk_rsrc((blk ? wqB : wqA) + kvh * 1024, W_BYTES - kvh * 1024);
    const rsrc_t rwo = mk_rsrc((blk ? woB : woA) + (size_t)kvh * 512 * DM * 2, 512 * DM * 2);
    const rsrc_t rk = mk_rsrc((blk ? kB : kA) + (size_t)(bb * HKV + kvh) * KV_BYTES, KV_BYTES);
    const rsrc_t rv = mk_rsrc((blk ? vB : vA) + (size_t)(bb * HKV + kvh) * KV_BYTES, KV_BYTES);
    int l16 = lane * 16;
    u32x4 R0[8], R1[8], SA[8];
    const int irow = 16 * (h >> 1) + 4 * (h & 1);
#define QDMA(S) do { _Pragma("unroll") for (int j = 0; j < 8; ++j) \
      dma_nt(rwq, l16, (KQ(S) * 32 + 8 * h + j) * (NQ * 2), qslot((S) % 3) + (irow + 8 * (j >> 2) + (j & 3)) * QRS); SBAR(); } while (0)
#define ODMA(S) do { _Pragma("unroll") for (int j = 0; j < 8; ++j) \
      dma56(rwo, l16, (KO(S) * 32 + 4 * h + 16 * (j >> 2) + (j & 3)) * (DM * 2), oslot((S) % 3) + (irow + 8 * (j >> 2) + (j & 3)) * ORS); SBAR(); } while (0)
#define LOADU(SET, RS, TILE) do { ld8(SET, RS, l16, (TILE) * 32768 + h * 1024, 4096, 16384); SBAR(); } while (0)
    QDMA(0); QDMA(1); QDMA(2);
    WAITV(16);
    WG_BARRIER();
    WAITV(8);
#pragma unroll 1
    for (int s = 0; s < 10; ++s) {
      WG_BARRIER();
      QDMA(s + 3);
      WAITV(8);
    }
    WG_BARRIER();
    QDMA(13);
    LOADU(R0, rk, 0);
    WAITV(16);
    WG_BARRIER();
    WAITV(8);
    LOADU(R1, rk, 1);
    WG_BARRIER();
    __builtin_amdgcn_raw_buffer_store_b32(hs_r + 1u, rc, hs ? lane * 512 + 128 + (lid & 7) * 4 : OOB, 0, AUX_SC1);
    WG_BARRIER();
    LOADU(SA, rv, 0);
    lane = lane_id(); l16 = lane * 16; hs = h == 0 && lane < 8;
    const int hi = lane >> 5, ch = lane & 31;
    const int row0 = 2 * h + hi;
    const int kw0 = LDS_K + row0 * 512 + ((ch ^ row0) << 4), kw1 = LDS_K + (row0 + 8) * 512 + ((ch ^ row0 ^ 8) << 4);
    const int vw0 = LDS_V + row0 * 512 + ((ch ^ (row0 << 1)) << 4);
#define WRITEK(SET, BUF) do { _Pragma("unroll") for (int i = 0; i < 8; ++i) \
      lds_w128(smem + ((i & 1) ? kw1 : kw0) + (BUF) + (i >> 1) * 8192, SET[i]); SBAR(); } while (0)
#define WRITEV(SET, BUF) do { _Pragma("unroll") for (int i = 0; i < 8; ++i) \
      lds_w128(smem + vw0 + (BUF) + i * 4096, SET[i]); SBAR(); } while (0)
    const int kv0 = row0 * 512 + ((ch ^ row0) << 4), kv1 = row0 * 512 + ((ch ^ row0 ^ 8) << 4), vv0 = row0 * 512 + ((ch ^ (row0 << 1)) << 4);
#define KDMA(TILE, HALF) do { _Pragma("unroll") for (int i = 0; i < 4; ++i) \
      dma_nt(rk, (i & 1) ? kv1 : kv0, (TILE) * 32768 + (HALF) * 16384 + i * 4096, LDS_K + ((TILE) & 1) * 32768 + (HALF) * 16384 + i * 4096 + h * 1024); SBAR(); } while (0)
#define VDMA(TILE, HALF) do { _Pragma("unroll") for (int i = 0; i < 4; ++i) \
      dma_nt(rv, vv0, (TILE) * 32768 + (HALF) * 16384 + i * 4096, LDS_V + ((TILE) & 1) * 32768 + (HALF) * 16384 + i * 4096 + h * 1024); SBAR(); } while (0)
    WRITEK(R0, 0);
    WG_BARRIER();
    WRITEK(R1, 32768);
    WG_BARRIER();
    WRITEV(SA, 0);
    VDMA(1, 0); KDMA(2, 0);
    WG_BARRIER();
    WG_BARRIER();
    VDMA(1, 1); KDMA(2, 1);
    WAITV(8);
#pragma unroll 1
    for (int n = 1; n < 27; ++n) {
      WG_BARRIER();
      const int tv = (n + 3) >> 1, hf = (n + 3) & 1;
      VDMA(tv, hf); KDMA(tv + 1, hf);
      if (n == 20 && hs) hs_echo(ws, lid, lane);
      WAITV(8);
    }
    WG_BARRIER();
    VDMA(15, 0);
    WAITV(4);
    WG_BARRIER();
    VDMA(15, 1);
    ODMA(0);
    WAITV(12);
    WG_BARRIER();
    WAITV(8);
#undef KDMA
#undef VDMA
    WG_BARRIER();
    ODMA(1);
    WAITV(8);
    WG_BARRIER();
    ODMA(2);
    WAITV(8);
#pragma unroll 1
    for (int s = 0; s < 13; ++s) {
      WG_BARRIER();
      ODMA(s + 3);
      WAITV(8);
    }
    WG_BARRIER();
    WAITV(0);
    WG_BARRIER();
    WG_BARRIER();
    if (h == 0) {
      const unsigned* cnt = (const unsigned*)(ws + CTL_OFF + (size_t)lid * 512);
      for (unsigned spins = 0; spins < SPIN_CAP; ++spins) {
        if (__hip_atomic_load(cnt, RLX_AGENT) >= 32u) break;
        if ((spins & 63u) == 63u) { const int l = lane_id(); if (l < 8) hs_echo(ws, lid, l); }
        __builtin_amdgcn_s_sleep(2);
      }
    }
#undef QDMA
#undef ODMA
#undef LOADU
#undef WRITEK
#undef WRITEV
  } else {
    const unsigned c0_ = (unsigned)__builtin_amdgcn_s_memtime(), r0_ = (unsigned)__builtin_amdgcn_s_memrealtime();
    {
      const int lane = lane_id();
      const rsrc_t rx = mk_rsrc((blk ? xB : xA) + (size_t)bb * SQ * DM * 2, SQ * DM * 2);
      const int xp = ((wid + (kvh >> 1)) & 3) * 14;
      const u32x4 cpf = __builtin_amdgcn_raw_buffer_load_b128(mk_rsrc((const void*)pc0, clen), wid < 2 ? lane * 16 + wid * 32768 + (bid >> 3) * 1024 : OOB, 0, 0);
      u32x4 xr[14];
#pragma unroll
      for (int f = 0; f < 14; ++f) xr[f] = __builtin_amdgcn_raw_buffer_load_b128(rx, lane * 16, (xp + f) * 1024, AUX_NT);
      SBAR();
      {
        const char* p0 = wid == 0 ? (blk ? kB : kA) + (size_t)(bb * HKV + kvh) * KV_BYTES
                       : wid == 1 ? (blk ? woB : woA) + (size_t)kvh * 512 * DM * 2
                       : wid == 2 ? (const char*)ws + (size_t)(lid >> 3) * GSTRIDE : (const char*)out + ((size_t)(bb * SQ + kvh * 8) * (2 * DM) + blk * DM) * 2;
        const char* p1 = wid == 0 ? (blk ? vB : vA) + (size_t)(bb * HKV + kvh) * KV_BYTES
                       : wid == 1 ? p0 + 512 * DM * 2 - 64 : wid == 2 ? p0 + GSTRIDE - 64 : p0 + 7 * (2 * DM) * 2;
        const unsigned long long a0 = (unsigned long long)p0, a1 = (unsigned long long)p1;
        const unsigned long long u0 = ((unsigned long long)__builtin_amdgcn_readfirstlane((unsigned)(a0 >> 32)) << 32) | (unsigned)__builtin_amdgcn_readfirstlane((unsigned)a0);
        const unsigned long long u1 = ((unsigned long long)__builtin_amdgcn_readfirstlane((unsigned)(a1 >> 32)) << 32) | (unsigned)__builtin_amdgcn_readfirstlane((unsigned)a1);
        unsigned d0, d1;
        asm volatile("s_nop 4\n\ts_load_dword %0, %2, 0x0\n\ts_load_dword %1, %3, 0x0\n\ts_waitcnt lgkmcnt(0)" : "=&s"(d0), "=&s"(d1) : "s"(u0), "s"(u1) : "memory");
      }
#pragma unroll
      for (int f = 0; f < 14; ++f) {
        const unsigned idx = (xp + f) * 64 + lane, row = idx / 56u, q = idx - row * 56u;
        lds_w128(smem + LDS_X + (q >> 2) * 4096 + (row >> 4) * 1024 + (q & 3) * 256 + (((row & 15) ^ (q & 7)) << 4), xr[f]);
      }
      asm volatile("" :: "v"(cpf[0]));
    }
    WG_BARRIER();
    const int fastclk = __builtin_amdgcn_readfirstlane((int)(((unsigned)__builtin_amdgcn_s_memtime() - c0_) * 16u - ((unsigned)__builtin_amdgcn_s_memrealtime() - r0_) * (unsigned)AP_TH));
    __builtin_amdgcn_s_waitcnt(0xC07F); SBAR();
    {
      f32x4 acc[8][4]; PFrag<8> F[2];
#pragma unroll
      for (int i = 0; i < 8; ++i)
#pragma unroll
        for (int rt = 0; rt < 4; ++rt) acc[i][rt] = f32x4{0.f, 0.f, 0.f, 0.f};
      const int ab = pabase<QRS>(wid * 8), bbs = pbbase(LDS_X);
      { const int aa = ab + qslot(0), ba = pbaddr(bbs, kq0); static_for<20>([&](auto kc) { pread<8, QRS, decltype(kc)::value>(F[0], smem, aa, ba); }); }
      int sl = 1;
#pragma unroll 1
      for (int s = 0; s < 14; s += 2) {
        WG_BARRIER();
        pstep<8, QRS>(acc, F[0], F[1], smem, ab + qslot(sl), pbaddr(bbs, KQ(s + 1)));
        sl = sl == 2 ? 0 : sl + 1;
        WG_BARRIER();
        pstep<8, QRS>(acc, F[1], F[0], smem, ab + qslot(sl), pbaddr(bbs, KQ(s + 2)));
        sl = sl == 2 ? 0 : sl + 1;
      }
      q_handover(acc, smem, wid);
    }
    {
      const int w = wid, hh = w >> 1, rh = w & 1;
      const int lane = lane_id(), g = lane >> 4, c = lane & 15;
      f32x4 o[16][2];
#pragma unroll
      for (int dt = 0; dt < 16; ++dt) { o[dt][0] = f32x4{0.f, 0.f, 0.f, 0.f}; o[dt][1] = f32x4{0.f, 0.f, 0.f, 0.f}; }
      float mc[2] = {0.f, 0.f}, lsum[2] = {0.f, 0.f};
      f32x4 s0[2][2], s1[2][2];
      u32x4 p0[2], p1[2];
      u32x4 qown[4][2];
      int kb = LDS_K + c * 512 + ((g ^ c) << 4);
      int vb = LDS_V + (4 * g + (c >> 2)) * 512 + ((((4 * g + (c >> 2)) & 7)) << 5) + (c & 3) * 8;
      LAS char* qp = smem + LDS_QP + w * 8192 + lane * 16;
      constexpr int PD = 4, RN = 6;
      bf16x4 vl[RN], vh[RN]; bf16x8 kf[RN], qf[2][2];
      float t0, t1, t2, t3, mx;
      int fa = 0;
#define XORI(IMM, V) do { asm volatile("" : "+v"(V)); fa = (V) ^ (IMM); } while (0)
#define FETCH(N) do { constexpr int n_ = (N), blk_ = n_ >> 4, j_ = n_ & 15; \
        if (blk_ & 1) { if ((j_ & 3) == 0) XORI((j_ >> 2) << 6, kb); \
          kf[n_ % RN] = asbf(lds_r128(smem + fa + ((j_ >> 1) & 1) * 256 + (j_ & 1) * 8192 + (blk_ >> 1) * 16384)); } \
        else { if ((j_ & 1) == 0) XORI((j_ >> 1) << 5, vb); \
          LAS char* a_ = smem + fa + (j_ & 1) * 256 + (blk_ >> 1) * 16384; vl[n_ % RN] = lds_tr(a_); vh[n_ % RN] = lds_tr(a_ + 8192); } } while (0)
#define FETCHQ(V, RT) qf[(V) & 1][RT] = asbf(lds_r128(qp + ((V) * 2 + (RT)) * 1024))
#define XE(X, e) X[(e) >> 3][((e) >> 2) & 1][(e) & 3]
#define JOB_AB(X, j) do { \
        if ((j) < 4) { _Pragma("unroll") for (int i_ = 0; i_ < 4; ++i_) XE(X, 4 * (j) + i_) = fmaf(XE(X, 4 * (j) + i_), SCL, -mc[(j) & 1]); } \
        else if ((j) == 4) { t0 = max3(X[0][0][0], X[0][0][1], X[0][0][2]); t1 = max3(X[0][1][0], X[0][1][1], X[0][1][2]); t2 = max3(X[0][0][3], X[1][0][0], X[1][0][1]); } \
        else if ((j) == 5) { t3 = max3(X[0][1][3], X[1][1][0], X[1][1][1]); t0 = max3(t0, X[1][0][2], X[1][0][3]); t1 = max3(t1, X[1][1][2], X[1][1][3]); } \
        else if ((j) == 6) { t0 = fmaxf(t0, t2); t1 = fmaxf(t1, t3); } \
        else if ((j) == 7) { auto a_ = __builtin_amdgcn_permlane16_swap(__float_as_uint(t0), __float_as_uint(t1), false, false); \
                             t0 = fmaxf(__uint_as_float(a_[0]), __uint_as_float(a_[1])); } \
        else if ((j) == 8) { mx = hmax(t0); } } while (0)
#define JOB_EXP(X, P, j) do { XE(X, j) = __builtin_amdgcn_exp2f(XE(X, j)); \
        if ((j) > 0) lsum[(((j) - 1) >> 2) & 1] += XE(X, (j) - 1); \
        if ((j) >= 2 && !((j) & 1)) P[(((j) - 2) >> 2) & 1][(((j) - 2) >> 3) * 2 + ((((j) - 2) >> 1) & 1)] = pk2(XE(X, (j) - 2), XE(X, (j) - 1)); \
        if ((j) == 15) { lsum[1] += XE(X, 15); P[1][3] = pk2(XE(X, 14), XE(X, 15)); } } while (0)
#define MX2(M) do { auto m_ = __builtin_amdgcn_permlane16_swap(__float_as_uint(mx), __float_as_uint(mx), false, false); \
        M[0] = __uint_as_float(m_[0]); M[1] = __uint_as_float(m_[1]); } while (0)
#define RESCALE(X, PEND) do { if (__any(mx > THR)) { float d_[2], al_[2]; MX2(d_); \
          _Pragma("unroll") for (int r_ = 0; r_ < 2; ++r_) { d_[r_] = fmaxf(d_[r_], 0.f); al_[r_] = __builtin_amdgcn_exp2f(-d_[r_]); mc[r_] += d_[r_]; lsum[r_] *= al_[r_]; \
            X[0][r_] -= d_[r_]; X[1][r_] -= d_[r_]; \
            _Pragma("unroll") for (int dt = 0; dt < 16; ++dt) o[dt][r_] *= al_[r_]; \
            if (PEND) { _Pragma("unroll") for (int i_ = 0; i_ < 4; ++i_) \
              p0[r_][i_] = pk2(__uint_as_float(p0[r_][i_] << 16) * al_[r_], __uint_as_float(p0[r_][i_] & 0xffff0000u) * al_[r_]); } } } SBAR(); } while (0)
#define FETCH_AHEAD(B, j) switch (j) { \
            case 0: FETCH((B) * 16 + 0 + PD); break; case 1: FETCH((B) * 16 + 1 + PD); break; case 2: FETCH((B) * 16 + 2 + PD); break; case 3: FETCH((B) * 16 + 3 + PD); break; \
            case 4: FETCH((B) * 16 + 4 + PD); break; case 5: FETCH((B) * 16 + 5 + PD); break; case 6: FETCH((B) * 16 + 6 + PD); break; case 7: FETCH((B) * 16 + 7 + PD); break; \
            case 8: FETCH((B) * 16 + 8 + PD); break; case 9: FETCH((B) * 16 + 9 + PD); break; case 10: FETCH((B) * 16 + 10 + PD); break; case 11: FETCH((B) * 16 + 11 + PD); break; \
            case 12: FETCH(((B) * 16 + 12 + PD) & 63); break; case 13: FETCH(((B) * 16 + 13 + PD) & 63); break; case 14: FETCH(((B) * 16 + 14 + PD) & 63); break; default: FETCH(((B) * 16 + 15 + PD) & 63); break; }
#define PVBLOCK(B, P, X, LAST, PN) do { _Pragma("unroll") for (int j = 0; j < 16; ++j) { \
          const bf16x8 a_ = __builtin_shufflevector(vl[((B) * 16 + j) % RN], vh[((B) * 16 + j) % RN], 0, 1, 2, 3, 4, 5, 6, 7); \
          o[(j >> 1) + 8 * (j & 1)][0] = MFMA(a_, asbf(P[0]), o[(j >> 1) + 8 * (j & 1)][0]); \
          if (j + PD < 16 || !(LAST)) { FETCH_AHEAD(B, j) } \
          SBAR(); \
          o[(j >> 1) + 8 * (j & 1)][1] = MFMA(a_, asbf(P[1]), o[(j >> 1) + 8 * (j & 1)][1]); \
          if (!(LAST) && j >= 14) FETCHQ(0, j - 14); \
          if (!(LAST)) JOB_AB(X, j); \
          PACE(PN); \
          SBAR(); } } while (0)
#define QKBLOCK(B, S, NEXT, JOB, X, P, PN) do { _Pragma("unroll") for (int j = 0; j < 16; ++j) { \
          const bf16x8 q0_ = (j & 2) ? qf[(j >> 2) & 1][0] : asbf(qown[j >> 2][0]), q1_ = (j & 2) ? qf[(j >> 2) & 1][1] : asbf(qown[j >> 2][1]); \
          const f32x4 z_ = {0.f, 0.f, 0.f, 0.f}; \
          S[j & 1][0] = MFMA(kf[((B) * 16 + j) % RN], q0_, j < 2 ? z_ : S[j & 1][0]); \
          if (j + PD < 16 || (NEXT)) { FETCH_AHEAD(B, j) } \
          SBAR(); \
          S[j & 1][1] = MFMA(kf[((B) * 16 + j) % RN], q1_, j < 2 ? z_ : S[j & 1][1]); \
          if (!(j & 2) && j >= 4) FETCHQ(j >> 2, j & 1); \
          if (JOB) JOB_EXP(X, P, j); \
          PACE(PN); \
          SBAR(); } } while (0)
#define FETCH4(N) do { FETCH(N); FETCH((N) + 1); FETCH((N) + 2); FETCH((N) + 3); } while (0)
#define RAWBAR() do { SBAR(); asm volatile("s_barrier" ::: "memory"); SBAR(); } while (0)
      WG_BARRIER();
#pragma unroll
      for (int f = 0; f < 8; ++f) qown[f >> 1][f & 1] = lds_r128(smem + LDS_QT + w * 8192 + f * 1024 + lane * 16);
      FETCH4(16); FETCHQ(0, 0); FETCHQ(0, 1); SBAR();
      QKBLOCK(1, s0, 0, 0, s0, p0, -1);
      FETCH4(48); FETCHQ(0, 0); FETCHQ(0, 1); SBAR();
#pragma unroll
      for (int j = 0; j < 9; ++j) JOB_AB(s0, j);
      MX2(mc);
#pragma unroll
      for (int r = 0; r < 2; ++r) { s0[0][r] -= mc[r]; s0[1][r] -= mc[r]; }
      RAWBAR();
      QKBLOCK(3, s1, 0, 1, s0, p0, -1);
      kb ^= 32768;
      RAWBAR();
      FETCH4(0); SBAR();
#define ATT_LOOP(PN, T0, T1) _Pragma("unroll 1") for (int t = (T0); t < (T1); ++t) { \
        RAWBAR();     \
        PVBLOCK(0, p0, s1, 0, PN); \
        RESCALE(s1, 0); \
        QKBLOCK(1, s0, 1, 1, s1, p1, PN); \
        RAWBAR();     \
        PVBLOCK(2, p1, s0, 0, PN); \
        vb ^= 32768; \
        RESCALE(s0, 0); \
        QKBLOCK(3, s1, 1, 1, s0, p0, PN); \
        kb ^= 32768; }
      const int nslow = fastclk > 0 ? 15 : 0;
      ATT_LOOP(AP_P, 0, nslow)
      ATT_LOOP(-1, nslow, 15)
#undef ATT_LOOP
#pragma unroll
      for (int j = 0; j < 9; ++j) JOB_AB(s1, j);
      RESCALE(s1, 1);
#pragma unroll
      for (int j = 0; j < 16; ++j) JOB_EXP(s1, p1, j);
      float iv[2];
#pragma unroll
      for (int r = 0; r < 2; ++r) {
        auto a = __builtin_amdgcn_permlane16_swap(__float_as_uint(lsum[r]), __float_as_uint(lsum[r]), false, false);
        iv[r] = 1.f / hsum(__uint_as_float(a[0]) + __uint_as_float(a[1]));
      }
      SBAR();
      PVBLOCK(0, p0, s1, 1, -1);
      RAWBAR();
      FETCH4(32); SBAR();
      PVBLOCK(2, p1, s0, 1, -1);
#undef FETCH
#undef XORI
#undef FETCHQ
#undef FETCH4
#undef FETCH_AHEAD
#undef JOB_AB
#undef JOB_EXP
#undef RESCALE
#undef MX2
#undef XE
#undef PVBLOCK
#undef QKBLOCK
      const int lane2 = lane_id(), g2 = lane2 >> 4, c2 = lane2 & 15;
      LAS char* ow = smem + LDS_V + hh * 65536 + rh * 2048 + g2 * 256;
#pragma unroll
      for (int kk = 0; kk < 8; ++kk)
#pragma unroll
        for (int r = 0; r < 2; ++r) {
          const f32x4 a = o[2 * kk][r] * iv[r], b = o[2 * kk + 1][r] * iv[r];
          lds_w128(ow + kk * 4096 + r * 1024 + ((c2 ^ g2 ^ ((kk & 1) * 4)) << 4), u32x4{pk2(a[0], a[1]), pk2(a[2], a[3]), pk2(b[0], b[1]), pk2(b[2], b[3])});
        }
    }
    WG_BARRIER();
    {
      HS_REQ();
      f32x4 acc[7][4]; PFrag<7> F[2];
#pragma unroll
      for (int i = 0; i < 7; ++i)
#pragma unroll
        for (int rt = 0; rt < 4; ++rt) acc[i][rt] = f32x4{0.f, 0.f, 0.f, 0.f};
      const int ab = pabase<ORS>(wid * 7), bbs = pbbase(LDS_V);
      { const int aa = ab + oslot(0), ba = obaddr(bbs, KO(0)); static_for<18>([&](auto kc) { pread<7, ORS, decltype(kc)::value>(F[0], smem, aa, ba); }); }
      int sl = 1;
#pragma unroll 1
      for (int s = 0; s < 16; s += 2) {
        WG_BARRIER();
        pstep<7, ORS>(acc, F[0], F[1], smem, ab + oslot(sl), obaddr(bbs, KO(s + 1)));
        sl = sl == 2 ? 0 : sl + 1;
        WG_BARRIER();
        pstep<7, ORS>(acc, F[1], F[0], smem, ab + oslot(sl), obaddr(bbs, KO(s + 2)));
        sl = sl == 2 ? 0 : sl + 1;
      }
      publish<7>(acc, ws, lid, wid * 7, HS_OK());
    }
  }
  WG_BARRIER();
  if (wid < 7) {
    const int lane = lane_id();
    const rsrc_t rp = mk_rsrc(ws + (size_t)(lid >> 3) * GSTRIDE + 1024 + kvh * 8 * SLAB, 8 * SLAB);
    u32x4 v[8];
#pragma unroll
    for (int s = 0; s < 8; ++s) v[s] = __builtin_amdgcn_raw_buffer_load_b128(rp, wid * 1024 + lane * 16, s * SLAB, AUX_SC1);
    float a[8];
#pragma unroll
    for (int k = 0; k < 4; ++k) { a[2 * k] = __uint_as_float(v[0][k] << 16); a[2 * k + 1] = __uint_as_float(v[0][k] & 0xffff0000u); }
#pragma unroll
    for (int s = 1; s < 8; ++s)
#pragma unroll
      for (int k = 0; k < 4; ++k) { a[2 * k] += __uint_as_float(v[s][k] << 16); a[2 * k + 1] += __uint_as_float(v[s][k] & 0xffff0000u); }
    const rsrc_t ro = mk_rsrc(out + (size_t)(bb * SQ + kvh * 8) * (2 * DM) * 2, 8 * 2 * DM * 2);
    __builtin_amdgcn_raw_buffer_store_b128(u32x4{pk2(a[0], a[1]), pk2(a[2], a[3]), pk2(a[4], a[5]), pk2(a[6], a[7])}, ro,
                                           ((lane & 7) * (2 * DM) + blk * DM + wid * 64 + (lane >> 3) * 8) * 2, 0, 0);
  }
  asm volatile(".Lgqa_end:");
}
}

extern "C" void kernel_launch(void* const* d_in, const int* in_sizes, int n_in, void* d_out, int out_size, void* d_ws, size_t ws_size,
                              hipStream_t stream) {
  (void)in_sizes; (void)n_in; (void)out_size; (void)ws_size;
  gqa_fused<<<256, 512, 0, stream>>>((const char*)d_in[0], (const char*)d_in[1], (const char*)d_in[2], (const char*)d_in[3], (const char*)d_in[4],
                                     (const char*)d_in[5], (const char*)d_in[6], (const char*)d_in[7], (const char*)d_in[8], (const char*)d_in[9],
                                     (char*)d_out, (char*)d_ws);
}
```
